```python
import torch

def _softmax(x, axis=-1):
    return torch.softmax(x, dim=axis)
def _gelu(x):
    return torch.nn.functional.gelu(x, approximate="tanh")
def _silu(x):
    return torch.nn.functional.silu(x)
def _sigmoid(x):
    return torch.sigmoid(x)
def _layernorm(x, axis=-1, eps=1e-5):
    m = x.mean(dim=axis, keepdim=True)
    v = x.var(dim=axis, keepdim=True, unbiased=False)
    return (x - m) / torch.sqrt(v + eps)
def _scatter_add(tgt, idx, src, axis=0):
    return tgt.clone().index_add_(axis, idx, src)
def _segment_sum(src, seg, n):
    out = torch.zeros((n,) + src.shape[1:], dtype=src.dtype)
    return out.index_add_(0, seg, src)
def _pad(x, pw):
    flat = tuple(v for pair in reversed(pw) for v in pair)
    return torch.nn.functional.pad(x, flat)

kernel_name = 'parallel_sum__sliding_block__dense_activation_gelu__prompt'

def setup_inputs():

    _w = (lambda a: (lambda t: t.to(torch.bfloat16) if t.is_floating_point() else t)(torch.as_tensor(a)))
    def _A_setup():
        B, S, D = 1, 2048, 576
        return {
            "x": _w((lambda *s: torch.randn(*s, dtype=torch.bfloat16) * 0.05)(B, S, D)),
            "wqkv": _w((lambda *s: torch.randn(*s, dtype=torch.bfloat16) * 0.05)(D, 3 * D)),
            "wo": _w((lambda *s: torch.randn(*s, dtype=torch.bfloat16) * 0.05)(D, D)),
        }
    _A_in = _A_setup()

    def _B_setup():
        B, S, D, F = 1, 2048, 576, 4608
        return {
            "x": _w((lambda *s: torch.randn(*s, dtype=torch.bfloat16) * 0.05)(B, S, D)),
            "w1": _w((lambda *s: torch.randn(*s, dtype=torch.bfloat16) * 0.8)(D, F) * (D ** -0.5)),
            "b1": _w((lambda *s: torch.randn(*s, dtype=torch.bfloat16) * 0.8)(F) * 0.01),
            "w2": _w((lambda *s: torch.randn(*s, dtype=torch.bfloat16) * 0.8)(F, D) * (F ** -0.5)),
            "b2": _w((lambda *s: torch.randn(*s, dtype=torch.bfloat16) * 0.8)(D) * 0.01),
        }
    _B_in = _B_setup()

    return {
        "A_x": _A_in["x"],
        "A_wqkv": _A_in["wqkv"],
        "A_wo": _A_in["wo"],
        "B_x": _B_in["x"],
        "B_w1": _B_in["w1"],
        "B_b1": _B_in["b1"],
        "B_w2": _B_in["w2"],
        "B_b2": _B_in["b2"],
    }

def reference(A_x, A_wqkv, A_wo, B_x, B_w1, B_b1, B_w2, B_b2):
    def _A_ref(x, wqkv, wo):
        B, S, D = x.shape
        H = 32; Dh = D // H; W = 256
        qkv = (lambda x, *d: x.permute(*d))((x @ wqkv).reshape(B, S, 3, H, Dh), 2, 0, 3, 1, 4)
        q, k, v = qkv[0], qkv[1], qkv[2]
        scores = torch.einsum("bhqd,bhkd->bhqk", q, k) * (Dh ** -0.5)
        idx = torch.arange(S)
        mask = (idx[:, None] - idx[None, :] >= 0) & (idx[:, None] - idx[None, :] < W)
        scores = torch.where(mask[None, None], scores, -1e9)
        p = _softmax(scores, axis=-1)
        o = torch.einsum("bhqk,bhkd->bhqd", p, v)
        return (lambda x, *d: x.permute(*d))(o, 0, 2, 1, 3).reshape(B, S, D) @ wo

    def _B_ref(x, w1, b1, w2, b2):
        h = torch.einsum("bsd,df->bsf", x, w1) + b1
        h = _gelu(h)
        return torch.einsum("bsf,fd->bsd", h, w2) + b2

    x = A_x
    xn = _layernorm(x, axis=-1)
    return x + _A_ref(xn, A_wqkv, A_wo) + _B_ref(xn, B_w1, B_b1, B_w2, B_b2)
```

```cpp
#include <hip/hip_runtime.h>
#include <hip/hip_bf16.h>
#include <stdint.h>

typedef unsigned short u16;
__device__ __forceinline__ float bf2f(u16 v) { return __uint_as_float(((unsigned)v) << 16); }
__device__ __forceinline__ u16 f2bf(float f) { __hip_bfloat16 h = __float2bfloat16(f); return *(u16*)&h; }
__device__ __forceinline__ float rb(float f) { return bf2f(f2bf(f)); }

constexpr int S = 2048, D = 576, F = 4608, H = 32, DH = 18, W = 256;

#define MIMIC 0

__global__ void ln_k(const u16* x, u16* xn) {
  int row = blockIdx.x; int t = threadIdx.x;
  float v[9]; float s = 0;
  for (int i = 0; i < 9; ++i) { v[i] = bf2f(x[row * D + t + 64 * i]); s += v[i]; }
  for (int o = 32; o; o >>= 1) s += __shfl_xor(s, o);
  float m = s / D; float q = 0;
  for (int i = 0; i < 9; ++i) { float d = v[i] - m; q += d * d; }
  for (int o = 32; o; o >>= 1) q += __shfl_xor(q, o);
  float var = q / D;
  float r = rsqrtf(var + 1e-5f);
  for (int i = 0; i < 9; ++i) xn[row * D + t + 64 * i] = f2bf((v[i] - m) * r);
}
__global__ void mm_k(const u16* A, const u16* B, const u16* bias, void* C, int M, int N, int K, int mode) {
  __shared__ float As[16][17], Bs[16][17];
  int tx = threadIdx.x & 15, ty = threadIdx.x >> 4;
  int r = blockIdx.y * 16 + ty, c = blockIdx.x * 16 + tx;
  float acc = 0;
  for (int k0 = 0; k0 < K; k0 += 16) {
    As[ty][tx] = bf2f(A[r * K + k0 + tx]);
    Bs[ty][tx] = bf2f(B[(k0 + ty) * N + c]);
    __syncthreads();
    for (int k = 0; k < 16; ++k) acc += As[ty][k] * Bs[k][tx];
    __syncthreads();
  }
  if (mode == 0) ((u16*)C)[r * N + c] = f2bf(acc);
  else if (mode == 1) {
    float h = acc + bf2f(bias[c]);
    float g = 0.5f * h * (1.f + tanhf(0.7978845608028654f * (h + 0.044715f * h * h * h)));
    ((u16*)C)[r * N + c] = f2bf(g);
  } else ((float*)C)[r * N + c] = acc;
}
__global__ void attn_k(const u16* qkv, u16* o) {
  int i = blockIdx.x, h = blockIdx.y; int t = threadIdx.x;
  __shared__ float p[256]; __shared__ float red[256];
  int j = i - t;
  float s = -1e30f;
  if (j >= 0) {
    float a = 0;
    for (int d = 0; d < DH; ++d) a += bf2f(qkv[i * 3 * D + h * DH + d]) * bf2f(qkv[j * 3 * D + D + h * DH + d]);
    s = a * 0.23570226039551584f;
  }
  red[t] = s; __syncthreads();
  for (int o2 = 128; o2; o2 >>= 1) { if (t < o2) red[t] = fmaxf(red[t], red[t + o2]); __syncthreads(); }
  float m = red[0]; __syncthreads();
  float e = j >= 0 ? __expf(s - m) : 0.f;
  p[t] = e; red[t] = e; __syncthreads();
  for (int o2 = 128; o2; o2 >>= 1) { if (t < o2) red[t] += red[t + o2]; __syncthreads(); }
  float l = red[0];
  if (t < DH) {
    float a = 0;
    for (int k = 0; k < 256 && i - k >= 0; ++k) a += p[k] * bf2f(qkv[(i - k) * 3 * D + 2 * D + h * DH + t]);
    o[i * D + h * DH + t] = f2bf(a / l);
  }
}
__global__ void fin_k(const u16* x, const float* a, const float* b, const u16* b2, u16* out) {
  int i = blockIdx.x * 256 + threadIdx.x;
  out[i] = f2bf(bf2f(x[i]) + a[i] + b[i] + bf2f(b2[i % D]));
}

extern "C" void kernel_launch(void* const* d_in, const int* in_sizes, int n_in,
                              void* d_out, int out_size, void* d_ws, size_t ws_size,
                              hipStream_t stream) {
  const u16* x = (const u16*)d_in[0]; const u16* wqkv = (const u16*)d_in[1]; const u16* wo = (const u16*)d_in[2];
  const u16* w1 = (const u16*)d_in[4]; const u16* b1 = (const u16*)d_in[5]; const u16* w2 = (const u16*)d_in[6]; const u16* b2 = (const u16*)d_in[7];
  char* ws = (char*)d_ws;
  u16* xn = (u16*)ws; ws += (size_t)S * D * 2;
  u16* qkv = (u16*)ws; ws += (size_t)S * 3 * D * 2;
  u16* h = (u16*)ws; ws += (size_t)S * F * 2;
  u16* o = (u16*)ws; ws += (size_t)S * D * 2;
  float* a = (float*)ws; ws += (size_t)S * D * 4;
  float* b = (float*)ws; ws += (size_t)S * D * 4;
  ln_k<<<S, 64, 0, stream>>>(x, xn);
  mm_k<<<dim3(3 * D / 16, S / 16), 256, 0, stream>>>(xn, wqkv, nullptr, qkv, S, 3 * D, D, 0);
  mm_k<<<dim3(F / 16, S / 16), 256, 0, stream>>>(xn, w1, b1, h, S, F, D, 1);
  attn_k<<<dim3(S, H), 256, 0, stream>>>(qkv, o);
  mm_k<<<dim3(D / 16, S / 16), 256, 0, stream>>>(o, wo, nullptr, a, S, D, D, 2);
  mm_k<<<dim3(D / 16, S / 16), 256, 0, stream>>>(h, w2, nullptr, b, S, D, F, 2);
  fin_k<<<S * D / 256, 256, 0, stream>>>(x, a, b, b2, (u16*)d_out);
}
```

```cpp
#include <hip/hip_runtime.h>
#include <hip/hip_bf16.h>
#include <stdint.h>

typedef unsigned short u16;
typedef __attribute__((ext_vector_type(8))) short bf16x8;
typedef __attribute__((ext_vector_type(4))) short s16x4;
typedef __attribute__((ext_vector_type(16))) float f32x16;
typedef __attribute__((ext_vector_type(4))) float f32x4;
typedef __attribute__((ext_vector_type(2))) float f32x2;
typedef __attribute__((ext_vector_type(4))) unsigned u32x4;
typedef __attribute__((ext_vector_type(2))) unsigned u32x2;
typedef __attribute__((ext_vector_type(2))) __bf16 bf16x2_t;
typedef short v4i16_t __attribute__((ext_vector_type(4)));
#define LAS __attribute__((address_space(3)))
typedef LAS const char* lds_cptr;
typedef LAS char* lds_ptr;

#define DI __device__ __forceinline__
#define SBAR() __builtin_amdgcn_sched_barrier(0)
#define PIN(x) asm volatile("" : "+v"(x))
#define MFMA32(a, b, c) __builtin_amdgcn_mfma_f32_32x32x16_bf16(a, b, c, 0, 0, 0)
#define MFMA16(a, b, c) __builtin_amdgcn_mfma_f32_16x16x32_bf16(a, b, c, 0, 0, 0)
#define WAITV_BAR(N) asm volatile("s_waitcnt vmcnt(" #N ")\n\ts_barrier" ::: "memory")
#define WAITVL_BAR(N) asm volatile("s_waitcnt vmcnt(" #N ") lgkmcnt(0)\n\ts_barrier" ::: "memory")

constexpr int S_ = 2048, D_ = 576, F_ = 4608;
constexpr unsigned WS_FLAGS = 0;
constexpr unsigned WS_DUMMY = 65536;
constexpr unsigned WS_H = 131072;
constexpr unsigned WS_Q = WS_H + S_ * F_ * 2;
constexpr unsigned WS_K = WS_Q + S_ * D_ * 2;
constexpr unsigned WS_V = WS_K + S_ * D_ * 2;
constexpr unsigned WS_O = WS_V + S_ * D_ * 2;
constexpr unsigned WS_P = WS_O + 128 * 4 * 5 * 1024;
constexpr unsigned WS_END = WS_P + 256 * 73728;

constexpr unsigned WS_DBG = 200u << 20;
#define STAMP(i)
DI unsigned cvtpk(float lo, float hi) { f32x2 v = {lo, hi}; return __builtin_bit_cast(unsigned, __builtin_convertvector(v, bf16x2_t)); }
DI float bfr(float f) { return __uint_as_float(cvtpk(f, f) & 0xffff0000u); }
DI void glds16(const void* g, unsigned lds_base) {
  unsigned sv;
  asm volatile("s_mov_b32 %0, m0\n\ts_mov_b32 m0, %2\n\ts_nop 0\n\tglobal_load_lds_dwordx4 %1, off\n\ts_mov_b32 m0, %0" : "=&s"(sv) : "v"(g), "s"(lds_base) : "memory");
}
DI s16x4 vtr(lds_cptr p) { return __builtin_bit_cast(s16x4, __builtin_amdgcn_ds_read_tr16_b64_v4i16((LAS v4i16_t*)p)); }
DI bf16x8 cat8(s16x4 lo, s16x4 hi) { return __builtin_shufflevector(lo, hi, 0, 1, 2, 3, 4, 5, 6, 7); }

#define KIDX 0
constexpr int K1_SLOT = 24576, K1_BIAS16 = 6 * K1_SLOT, K1_BIAS32 = K1_BIAS16 + 1024, K1_LDS = K1_BIAS32 + 352 * 4;

struct K1Tab { unsigned long long t[16][2]; };
static inline int k1_tile_h(int q, int c, int r, int cg) {
  const int nf = (c & 1) ? 4 : 5, p0 = (9 * c + 1) >> 1;
  if (r < nf) return 36 * q + 2 * (p0 + r) + cg;
  const int j = 2 * (r - nf) + cg;
  const int cnt = (c & 1) ? 4 : (c == 0 ? 3 : 3 - (q & 1));
  const int start = c == 0 ? 0 : (c == 1 ? 3 : (c == 2 ? 7 : 10 - (q & 1)));
  if (j >= cnt) return -1;
  const int e = start + j, nq = (q & 1) ? 4 : 5, sA = (9 * q + 1) >> 1, sB = (9 * q) >> 1;
  if (e < nq) return 144 + sA + e;
  if (e < 9) return 162 + sB + (e - nq);
  return 180 + sA + (e - 9);
}

#define AB_NOMFMA 0
#define AB_NOEPI 0
#define K1_STORE_AUX 0

__global__ __launch_bounds__(512, 2) void k1_kernel(const u16* __restrict__ x, const u16* __restrict__ wqkv, const u16* __restrict__ w1,
                                                    const u16* __restrict__ b1, char* __restrict__ ws, const K1Tab tab) {
  extern __shared__ __attribute__((aligned(16))) char lds[];
  const int tid = threadIdx.x, lane = tid & 63;
  const int w = __builtin_amdgcn_readfirstlane(tid >> 6), rg = w & 3, cg = w >> 2;
  const int bid = blockIdx.x, g8 = bid & 7, q = g8 & 3, idx = bid >> 3, R = (g8 >> 2) * 8 + (idx >> 2), c = idx & 3;
  const unsigned lds0 = (unsigned)(uintptr_t)lds;
  const int hi = lane >> 5;
  const int nf = (c & 1) ? 4 : 5;
  const int nr = nf + ((c & 1) ? 2 : ((c == 2 && (q & 1)) ? 1 : 2));
  const unsigned long long tab0 = tab.t[q * 4 + c][0], tab1 = tab.t[q * 4 + c][1], tabw = cg ? tab1 : tab0;
  STAMP(0);
  if (bid < 16) { ((u32x4*)(ws + WS_FLAGS))[bid * 256 + (tid & 255)] = (u32x4){0, 0, 0, 0}; }
  const int f0 = 32 * (36 * q + 2 * ((9 * c + 1) >> 1));
  if (w == 0) { int o = f0 + lane * 8; o = o > F_ - 8 ? F_ - 8 : o; glds16(b1 + o, lds0 + K1_BIAS16); }
  {
    const u16* xs[6];
#pragma unroll
    for (int t = 0; t < 6; ++t) {
      const int i = (w * 6 + t) * 64 + lane, row = i / 24, cp = i - row * 24;
      const int cc = (cp & ~7) | ((cp ^ (row >> 1)) & 7);
      xs[t] = x + (size_t)(R * 128 + row) * D_ + cc * 8;
    }
#pragma unroll
    for (int kc = 0; kc < 3; ++kc)
#pragma unroll
      for (int t = 0; t < 6; ++t) glds16(xs[t] + kc * 192, lds0 + kc * 2 * K1_SLOT + (w * 6 + t) * 1024);
  }
  const int wc = (lane & 7) ^ (((lane >> 4) & 1) << 2);
  const int wunit = wc >> 2;
  const int wrow = w * 24 + (lane >> 3);
  const u16* pf; unsigned pf_ld8;
  auto set_pf = [&](int r) {
    const int t0 = (tab0 >> (8 * r)) & 255; int t1 = (tab1 >> (8 * r)) & 255;
    t1 = t1 == 255 ? t0 : t1;
    const int t = wunit ? t1 : t0;
    const bool ff = t < 144;
    const unsigned ld = ff ? F_ : 3 * D_;
    const u16* base = ff ? w1 + t * 32 : wqkv + (t - 144) * 32;
    pf = base + (size_t)wrow * ld + (wc & 3) * 8;
    pf_ld8 = ld * 8;
  };
  auto stage_w = [&](int slot) {
#pragma unroll
    for (int t = 0; t < 3; ++t) glds16(pf + (size_t)t * pf_ld8, lds0 + slot * K1_SLOT + (w * 3 + t) * 1024);
    pf += (size_t)pf_ld8 * 24;
  };
  bf16x8 xf[36];
  float sum = 0.f, sq = 0.f;
  const int xrow = rg * 32 + (lane & 31);
  auto read_x = [&](int kc) {
    const lds_cptr xb = (lds_cptr)lds + kc * 2 * K1_SLOT + xrow * 384;
#pragma unroll
    for (int s = 0; s < 12; ++s) {
      const int cch = 2 * s + hi;
      const int cp = (cch & ~7) | ((cch ^ (xrow >> 1)) & 7);
      xf[kc * 12 + s] = *(const LAS bf16x8*)(xb + cp * 16);
    }
#pragma unroll
    for (int s = 0; s < 12; ++s) {
      const u32x4 v = __builtin_bit_cast(u32x4, xf[kc * 12 + s]);
#pragma unroll
      for (int j = 0; j < 4; ++j) {
        const unsigned vj = v[j];
        const bf16x2_t bj = __builtin_bit_cast(bf16x2_t, vj);
        sum = __builtin_amdgcn_fdot2_f32_bf16(bj, __builtin_bit_cast(bf16x2_t, 0x3f803f80u), sum, false);
        sq = __builtin_amdgcn_fdot2_f32_bf16(bj, bj, sq, false);
      }
    }
  };
  set_pf(0);
  STAMP(1);
  WAITV_BAR(12);
  STAMP(2);
  if (tid < 352) {
    const unsigned b = tid < 320 ? (unsigned)*(const LAS u16*)((lds_cptr)lds + K1_BIAS16 + tid * 2) << 16 : 0u;
    *(LAS unsigned*)((lds_ptr)lds + K1_BIAS32 + tid * 4) = b;
  }
  read_x(0);
  WAITVL_BAR(6);
  stage_w(0); stage_w(1);
  read_x(1);
  WAITVL_BAR(6);
  stage_w(2); set_pf(1); stage_w(3);
  read_x(2);
  WAITVL_BAR(63);
  STAMP(3);
  stage_w(4);
  {
    auto r1 = __builtin_amdgcn_permlane32_swap(__float_as_uint(sum), __float_as_uint(sum), false, false);
    auto r2 = __builtin_amdgcn_permlane32_swap(__float_as_uint(sq), __float_as_uint(sq), false, false);
    const float ts = __uint_as_float(r1[0]) + __uint_as_float(r1[1]), tq = __uint_as_float(r2[0]) + __uint_as_float(r2[1]);
    const float m = ts * (1.f / D_);
    const float var = fmaxf(tq * (1.f / D_) - m * m, 0.f);
    const float sd = bfr(__builtin_sqrtf(bfr(bfr(var) + 1e-5f)));
    const float rs = 1.f / sd, nb = -bfr(m) * rs;
#pragma unroll
    for (int s = 0; s < 36; ++s) {
      u32x4 v = __builtin_bit_cast(u32x4, xf[s]);
#pragma unroll
      for (int j = 0; j < 4; ++j) {
        const float lo = __uint_as_float(v[j] << 16), hv = __uint_as_float(v[j] & 0xffff0000u);
        v[j] = cvtpk(fmaf(lo, rs, nb), fmaf(hv, rs, nb));
      }
      xf[s] = __builtin_bit_cast(bf16x8, v);
    }
  }
  const auto rsrc = __builtin_amdgcn_make_buffer_rsrc(ws, 0, WS_END, 0x00020000);
  const int blk = (lane >> 4) & 1, qq = (lane & 15) >> 2, pp = lane & 3;
  const lds_cptr wrd = (lds_cptr)lds + (8 * hi + qq) * 128 + (((cg * 4 + 2 * blk + (pp >> 1)) ^ (((qq >> 1) & 1) << 2)) * 16) + 8 * (pp & 1);
  const unsigned vo_h = ((lane >> 4) & 1) * 147456 + hi * 256 + (lane & 15) * 16, vo_q = lane * 16;
  const lds_cptr bias_rd = (lds_cptr)lds + K1_BIAS32 + hi * 16;
  f32x16 acc = {}, prev;
#pragma unroll
  for (int i = 0; i < 16; ++i) prev[i] = 0.f;
  float e_c1 = 0.f, e_c2 = 0.f, e_bu = -1000.f, e_dc = 1.f;
  unsigned e_vo = vo_q, e_so0 = WS_DUMMY, e_so1 = WS_DUMMY + 1024;
  s16x4 wl[3], wh[3];
  STAMP(4);
  WAITV_BAR(9);
  wl[0] = vtr(wrd); wh[0] = vtr(wrd + 512);
  wl[1] = vtr(wrd + 2048); wh[1] = vtr(wrd + 2048 + 512);
  const int rg32 = R * 4 + rg;

#define K1_EPI(g)                                                                                          \
  {                                                                                                        \
    constexpr int pi_ = (g) >> 2, ph_ = (g) & 3;                                                           \
    if constexpr (pi_ < 8) {                                                                               \
      if constexpr (ph_ == 0) { ta = fmaf(prev[2 * pi_] * prev[2 * pi_], e_c2, e_c1); tb = fmaf(prev[2 * pi_ + 1] * prev[2 * pi_ + 1], e_c2, e_c1); PIN(ta); PIN(tb); } \
      if constexpr (ph_ == 1) { ta = __builtin_amdgcn_exp2f(fmaf(prev[2 * pi_], ta, e_bu)); tb = __builtin_amdgcn_exp2f(fmaf(prev[2 * pi_ + 1], tb, e_bu)); PIN(ta); PIN(tb); } \
      if constexpr (ph_ == 2) { ta = __builtin_amdgcn_rcpf(ta + e_dc); tb = __builtin_amdgcn_rcpf(tb + e_dc); PIN(ta); PIN(tb); } \
      if constexpr (ph_ == 3) { pk[pi_ & 3] = cvtpk(prev[2 * pi_] * ta, prev[2 * pi_ + 1] * tb); PIN(pk[pi_ & 3]);                 \
        if constexpr (pi_ == 3) __builtin_amdgcn_raw_buffer_store_b128(pk, rsrc, e_vo, __builtin_amdgcn_readfirstlane(e_so0), K1_STORE_AUX);                       \
        if constexpr (pi_ == 7) __builtin_amdgcn_raw_buffer_store_b128(pk, rsrc, e_vo, __builtin_amdgcn_readfirstlane(e_so1), K1_STORE_AUX); }                     \
    } else {                                                                                               \
      const f32x4 bv = *(const LAS f32x4*)(bias_rd + bias_off + ph_ * 32);                                 \
      prev[4 * ph_] = bv[0]; prev[4 * ph_ + 1] = bv[1]; prev[4 * ph_ + 2] = bv[2]; prev[4 * ph_ + 3] = bv[3]; \
    }                                                                                                      \
  }
#define K1_GAP(MODE, g)                                                                                    \
  {                                                                                                        \
    if constexpr (MODE != 2 && !AB_NOMFMA) {                                                                             \
      constexpr int n_ = (g) + 2;                                                                          \
      const lds_cptr p_ = (n_ < 36 ? wcur : wnext) + ((n_ % 36) / 12) * K1_SLOT + (n_ % 12) * 2048;        \
      wl[n_ % 3] = vtr(p_); wh[n_ % 3] = vtr(p_ + 512);                                                    \
      const bf16x8 wf_ = cat8(wl[(g) % 3], wh[(g) % 3]);                                                   \
      if constexpr ((g) == 0) { acc = MODE == 1 ? MFMA32(xf[g], wf_, zero16) : MFMA32(wf_, xf[g], zero16); } \
      else { acc = MODE == 1 ? MFMA32(xf[g], wf_, acc) : MFMA32(wf_, xf[g], acc); }                        \
    }                                                                                                      \
    if constexpr (!AB_NOEPI) K1_EPI(g);                                                                                             \
    SBAR();                                                                                                \
  }
#define K1_STAGE12(MODE, b) K1_GAP(MODE, b + 0) K1_GAP(MODE, b + 1) K1_GAP(MODE, b + 2) K1_GAP(MODE, b + 3) K1_GAP(MODE, b + 4) K1_GAP(MODE, b + 5) \
                            K1_GAP(MODE, b + 6) K1_GAP(MODE, b + 7) K1_GAP(MODE, b + 8) K1_GAP(MODE, b + 9) K1_GAP(MODE, b + 10) K1_GAP(MODE, b + 11)
#define K1_ROUND(MODE)                                                                                     \
  {                                                                                                        \
    float ta, tb; u32x4 pk;                                                                                \
                        \
    if (!last) stage_w(sprev + 2);                                                                         \
    K1_STAGE12(MODE, 0)                                                                                    \
    if (last) { WAITV_BAR(0); } else { WAITV_BAR(9); }                                                     \
    if (r + 2 < nr) { set_pf(r + 2); stage_w(scur + 0); }                                                  \
    K1_STAGE12(MODE, 12)                                                                                   \
    if (r + 2 < nr) { WAITV_BAR(9); } else { WAITV_BAR(6); }                                               \
    if (r + 2 < nr) stage_w(scur + 1);                                                                     \
    K1_STAGE12(MODE, 24)                                                                                   \
    if (r + 2 < nr) { WAITV_BAR(9); } else { WAITV_BAR(3); }                                               \
  }
  const f32x16 zero16 = {0.f, 0.f, 0.f, 0.f, 0.f, 0.f, 0.f, 0.f, 0.f, 0.f, 0.f, 0.f, 0.f, 0.f, 0.f, 0.f};
  bool pending = true;
#pragma unroll 1
  for (int r = 0; r < nr; ++r) {
    const int t = (tabw >> (8 * r)) & 255;
    const bool last = r + 1 == nr;
    STAMP(5 + r);
    const int scur = (r & 1) * 3, sprev = 3 - scur;
    const lds_cptr wcur = wrd + scur * K1_SLOT, wnext = wrd + sprev * K1_SLOT;
    const int bias_off = (t < 144) ? (t * 32 - f0) * 4 : 320 * 4;
    if (t == 255) { K1_ROUND(2); pending = false; }
    else {
      K1_ROUND(0)
#pragma unroll
      for (int i = 0; i < 16; ++i) prev[i] += acc[i];
      const bool ff = t < 144, isq = t < 162, isv = t >= 180;
      e_c1 = ff ? -2.302208198f : 0.f; e_c2 = ff ? -0.1029432397f : 0.f; e_bu = ff ? 0.f : -1000.f;
      e_dc = (!ff && isq) ? (1.f / (0.23570226039551584f * 1.4426950408889634f)) : 1.f;
      e_vo = ff ? vo_h : vo_q;
      e_so0 = ff ? WS_H + (R * 8 + rg * 2) * 147456 + t * 1024
                 : (isv ? WS_V + (rg32 * 36 + 2 * (t - 180)) * 1024 : (isq ? WS_Q + (rg32 * 36 + 2 * (t - 144)) * 1024 : WS_K + (rg32 * 36 + 2 * (t - 162)) * 1024));
      e_so1 = e_so0 + (ff ? 512 : 1024);
    }
  }
  STAMP(13);
  if (pending) {
    u32x4 pk;
#pragma unroll
    for (int hh = 0; hh < 2; ++hh) {
#pragma unroll
      for (int i = 0; i < 4; ++i) {
        const float a = prev[8 * hh + 2 * i], b = prev[8 * hh + 2 * i + 1];
        const float ea = __builtin_amdgcn_exp2f(fmaf(a, fmaf(a * a, e_c2, e_c1), e_bu)), eb = __builtin_amdgcn_exp2f(fmaf(b, fmaf(b * b, e_c2, e_c1), e_bu));
        pk[i] = cvtpk(a * __builtin_amdgcn_rcpf(ea + e_dc), b * __builtin_amdgcn_rcpf(eb + e_dc));
      }
      __builtin_amdgcn_raw_buffer_store_b128(pk, rsrc, e_vo, __builtin_amdgcn_readfirstlane(hh ? e_so1 : e_so0), K1_STORE_AUX);
      asm volatile("s_nop 1" :: "v"(pk));
    }
  }
}

#undef KIDX
#define KIDX 1
#define GAS __attribute__((address_space(1)))
#define RLX_AGENT __ATOMIC_RELAXED, __HIP_MEMORY_SCOPE_AGENT
constexpr int K2_SLOT = 18432, K2_LDS = 81920 - 1024;
constexpr unsigned FL_O = 0, FL_P = 16384;
constexpr int AT_V = 36864, AT_ONE = 73728;

DI void glds16s(unsigned voff, const void* sbase, unsigned lds_base) {
  unsigned sv;
  asm volatile("s_mov_b32 %0, m0\n\ts_mov_b32 m0, %3\n\ts_nop 0\n\tglobal_load_lds_dwordx4 %1, %2\n\ts_mov_b32 m0, %0" : "=&s"(sv) : "v"(voff), "s"(sbase), "s"(lds_base) : "memory");
}
DI float max3f(float a, float b, float c) { return __builtin_fmaxf(__builtin_fmaxf(a, b), c); }

DI void k2_attn(char* __restrict__ ws, char* lds, int a) {
  const int tid = threadIdx.x, lane = tid & 63, hi = lane >> 5;
  const int w = __builtin_amdgcn_readfirstlane(tid >> 6), hb = w >> 2, qt = w & 3;
  const int g8 = a & 7, q = g8 & 3, idx = a >> 3, R = (g8 >> 2) * 8 + (idx >> 2), c = idx & 3;
  const int s0 = 9 * q + 2 * c;
  const unsigned lds0 = (unsigned)(uintptr_t)lds;
  STAMP(0);
  for (int n = w; n < 72; n += 8) {
    const int isv = n >= 36, m = n - 36 * isv, b = m / 3, f = m - 3 * b;
    int blk = 4 * R - 8 + b; blk = blk < 0 ? 0 : blk;
    const unsigned so = (isv ? WS_V : WS_K) + (blk * 36 + s0 + f) * 1024;
    const unsigned sw = isv ? (unsigned)(4 * (hi + 2 * (f & 1))) : 0u;
    glds16s((lane ^ sw) * 16, ws + so, lds0 + n * 1024);
  }
  if (tid < 48) *(LAS u32x4*)((lds_ptr)lds + AT_ONE + tid * 16) = (u32x4){0x3f803f80u, 0x3f803f80u, 0x3f803f80u, 0x3f803f80u};
  const int rg32 = 4 * R + qt;
  u32x4 qf[2];
#pragma unroll
  for (int f = 0; f < 2; ++f) {
    qf[f] = *(const u32x4*)(ws + WS_Q + (size_t)(rg32 * 36 + s0 + hb + f) * 1024 + lane * 16);
  }
  const int lo = 4 * c + 18 * hb - 16 * hb - 4 * hi;
#pragma unroll
  for (int f = 0; f < 2; ++f)
#pragma unroll
    for (int j = 0; j < 4; ++j) {
      const int col = 16 * f + 8 * (j >> 1) + 2 * (j & 1);
      qf[f][j] = ((unsigned)(col - lo) < 18u) ? qf[f][j] : 0u;
    }
  const bf16x8 q0 = __builtin_bit_cast(bf16x8, qf[0]), q1 = __builtin_bit_cast(bf16x8, qf[1]);
  WAITVL_BAR(0);
  STAMP(1);
  const lds_cptr kp = (lds_cptr)lds + (qt * 3 + hb) * 1024 + lane * 16;
  const int blk2 = (lane >> 4) & 1, qq = (lane & 15) >> 2, pp = lane & 3, slot = 4 * blk2 + pp;
  const int gr = hb == 0 ? c + slot : (slot < 4 ? c + 5 + slot : c + 4);
  const int vf = gr >> 2, vh = gr & 1, vj = (gr >> 1) & 1, vsw = 4 * (vh + 2 * (vf & 1));
  const bool one = slot >= 5;
  const int va0 = one ? AT_ONE : AT_V + (qt * 3 + vf) * 1024 + vh * 512 + ((4 * hi + qq) ^ vsw) * 16 + vj * 8;
  const int va1 = one ? AT_ONE : AT_V + (qt * 3 + vf) * 1024 + vh * 512 + ((8 + 4 * hi + qq) ^ vsw) * 16 + vj * 8;
  const int vstr = one ? 0 : 3072;
  const f32x16 zero16 = {0.f, 0.f, 0.f, 0.f, 0.f, 0.f, 0.f, 0.f, 0.f, 0.f, 0.f, 0.f, 0.f, 0.f, 0.f, 0.f};
  auto qk = [&](int bi, const f32x16& cin) {
    const bf16x8 k0 = *(const LAS bf16x8*)(kp + bi * 3072), k1 = *(const LAS bf16x8*)(kp + bi * 3072 + 1024);
    f32x16 s = MFMA32(k0, q0, cin); return MFMA32(k1, q1, s);
  };
  f32x16 o = zero16;
  auto pv = [&](int bi, u32x4 p0, u32x4 p1) {
    const lds_cptr a0 = (lds_cptr)lds + va0 + bi * vstr, a1 = (lds_cptr)lds + va1 + bi * vstr;
    o = MFMA32(cat8(vtr(a0), vtr(a1)), __builtin_bit_cast(bf16x8, p0), o);
    o = MFMA32(cat8(vtr(a0 + 256), vtr(a1 + 256)), __builtin_bit_cast(bf16x8, p1), o);
  };
  const int bi0 = rg32 >= 8 ? 1 : 8 - rg32;
  const float NEG = -__builtin_inff();
  const int aq = (lane & 31) - 4 * hi;
  f32x16 sm = qk(8, zero16);
  {
    f32x16 s0v = qk(0, zero16);
#pragma unroll
    for (int r = 0; r < 16; ++r) { const int cr = (r & 3) + 8 * (r >> 2); sm[r] = cr <= aq ? sm[r] : (rg32 >= 8 ? s0v[r] : NEG); }
  }
  float m0 = max3f(sm[0], sm[1], sm[2]), m1 = max3f(sm[3], sm[4], sm[5]);
#pragma unroll
  for (int r = 6; r < 14; r += 4) { m0 = max3f(m0, sm[r], sm[r + 1]); m1 = max3f(m1, sm[r + 2], sm[r + 3]); }
  m0 = max3f(m0, sm[14], sm[15]);
#pragma unroll 1
  for (int bi = bi0; bi < 8; ++bi) {
    const f32x16 s = qk(bi, zero16);
#pragma unroll
    for (int r = 0; r < 16; r += 4) { m0 = max3f(m0, s[r], s[r + 1]); m1 = max3f(m1, s[r + 2], s[r + 3]); }
  }
  float m = __builtin_fmaxf(m0, m1);
  { auto rr = __builtin_amdgcn_permlane32_swap(__float_as_uint(m), __float_as_uint(m), false, false); m = __builtin_fmaxf(__uint_as_float(rr[0]), __uint_as_float(rr[1])); }
  STAMP(2);
  f32x16 cT;
#pragma unroll
  for (int r = 0; r < 16; ++r) cT[r] = -m;
  {
    u32x4 pa[2], pb[2];
#pragma unroll
    for (int r = 0; r < 16; r += 2) {
      const int cr = (r & 3) + 8 * (r >> 2);
      const float e0 = __builtin_amdgcn_exp2f(sm[r] - m), e1 = __builtin_amdgcn_exp2f(sm[r + 1] - m);
      const unsigned pk = cvtpk(e0, e1);
      const unsigned msk = (cr <= aq ? 0xffffu : 0u) | (cr + 1 <= aq ? 0xffff0000u : 0u);
      pa[r >> 3][(r >> 1) & 3] = pk & msk; pb[r >> 3][(r >> 1) & 3] = pk & ~msk;
    }
    pv(8, pa[0], pa[1]);
    pv(0, pb[0], pb[1]);
  }
#pragma unroll 1
  for (int bi = bi0; bi < 8; ++bi) {
    const f32x16 s = qk(bi, cT);
    u32x4 p[2];
#pragma unroll
    for (int r = 0; r < 16; r += 2) p[r >> 3][(r >> 1) & 3] = cvtpk(__builtin_amdgcn_exp2f(s[r]), __builtin_amdgcn_exp2f(s[r + 1]));
    pv(bi, p[0], p[1]);
  }
  STAMP(3);
  const float inv = __builtin_amdgcn_rcpf(o[12]);
  const auto rsrc = __builtin_amdgcn_make_buffer_rsrc(ws, 0, WS_END, 0x00020000);
  u32x4 ov = {cvtpk(o[0] * inv, o[1] * inv), cvtpk(o[2] * inv, o[3] * inv), cvtpk(o[4] * inv, o[5] * inv), cvtpk(o[6] * inv, o[7] * inv)};
  const unsigned ex = hb == 0 ? cvtpk(o[8] * inv, o[9] * inv) : cvtpk(o[10] * inv, o[11] * inv);
  const unsigned ob = WS_O + ((rg32 * 2 + ((lane >> 4) & 1)) * 4 + q) * 5120 + (lane & 15) * 16;
  __builtin_amdgcn_raw_buffer_store_b128(ov, rsrc, ob + c * 1024 + (2 * hb + hi) * 256, 0, 16);
  const int hl = 2 * c + hb;
  if (hi == 0) __builtin_amdgcn_raw_buffer_store_b32(ex, rsrc, ob + 4096 + (hl >> 2) * 512 + (hl & 3) * 4, 0, 16);
  WAITVL_BAR(0);
  if (tid == 0) __hip_atomic_fetch_add((GAS unsigned*)(ws + WS_FLAGS + FL_O + (R * 4 + q) * 128), 1u, RLX_AGENT);
  STAMP(4);
}

#undef KIDX
#define KIDX 2
DI bool wait_ge(const char* p, unsigned want) {
#pragma unroll 1
  for (unsigned spins = 0; spins < (1u << 20); ++spins) {
    if ((unsigned)__builtin_amdgcn_readfirstlane(__hip_atomic_load((GAS unsigned*)p, RLX_AGENT)) >= want) return true;
    __builtin_amdgcn_s_sleep(1);
  }
  return false;
}

DI void k2_gemm(const u16* __restrict__ x, const u16* __restrict__ wo, const u16* __restrict__ w2, const u16* __restrict__ b2,
                u16* __restrict__ out, char* __restrict__ ws, char* lds, int g) {
  const int tid = threadIdx.x, lane = tid & 63;
  const int w = __builtin_amdgcn_readfirstlane(tid >> 6), rg = w & 3, kp = w >> 2;
  const int g8 = g & 7, q = g8 & 3, idx = g >> 3, R = (g8 >> 2) * 8 + (idx >> 2), c = idx & 3;
  const unsigned lds0 = (unsigned)(uintptr_t)lds;
  STAMP(0);
  unsigned wo_[3];
#pragma unroll
  for (int t = 0; t < 3; ++t) {
    int i = t * 64 + lane; i = i > 143 ? 143 : i;
    const int rr = i / 18, ch = i - rr * 18;
    wo_[t] = rr * 1152 + ch * 16;
  }
  const char* wsrc = (const char*)w2 + (size_t)(q * 1152 + 8 * w) * 1152 + c * 288;
  const char* wo_main = (const char*)wo + (size_t)(q * 144 + 18 * (w >> 1) + 2 * ((w >> 1) & 1) + 8 * (w & 1)) * 1152 + c * 288;
  const char* wo_ext = (const char*)wo + (size_t)(q * 144 + 36 * (w & 3) + 16) * 1152 + c * 288;
  auto stage_w = [&](int slot) {
    const unsigned d = lds0 + slot * K2_SLOT + w * 2304;
    glds16s(wo_[0], wsrc, d); glds16s(wo_[1], wsrc, d + 1024);
    unsigned sv;
    asm volatile("s_mov_b32 %0, m0\n\ts_mov_b32 m0, %3\n\ts_mov_b64 exec, 0xffff\n\tglobal_load_lds_dwordx4 %1, %2\n\ts_mov_b64 exec, -1\n\ts_mov_b32 m0, %0"
                 : "=&s"(sv) : "v"(wo_[2]), "s"(wsrc), "s"(d + 2048) : "memory");
  };
  u32x4 srd; { const uint64_t p = (uint64_t)ws; srd[0] = (unsigned)p; srd[1] = (unsigned)(p >> 32); srd[2] = WS_END; srd[3] = 0x00020000; }
  unsigned avo = lane * 16;
  unsigned as0 = WS_H + (8 * R + 2 * rg) * 147456 + (36 * q + kp) * 1024, ast = 147456;
  const unsigned as_o = WS_O + ((8 * R + 2 * rg) * 4 + q) * 5120 + kp * 1024;
  u32x4 hA[3][2];
  const auto rsrcA = __builtin_amdgcn_make_buffer_rsrc(ws, 0, WS_END, 0x00020000);
#define LOAD_A(set) do { hA[set][0] = __builtin_amdgcn_raw_buffer_load_b128(rsrcA, avo, as0, 16); hA[set][1] = __builtin_amdgcn_raw_buffer_load_b128(rsrcA, avo, as0 + ast, 16); } while (0)
  LOAD_A(0); stage_w(0); wsrc += 64 * 1152; as0 += 2048;
  LOAD_A(1); stage_w(1); wsrc += 64 * 1152; as0 += 2048;
  LOAD_A(2); stage_w(2); wsrc += 64 * 1152; as0 += 2048;
  f32x4 acc[2][9];
#pragma unroll
  for (int i = 0; i < 2; ++i)
#pragma unroll
    for (int j = 0; j < 9; ++j) acc[i][j] = (f32x4){0.f, 0.f, 0.f, 0.f};
  const int gq = lane >> 4, qq = (lane & 15) >> 2, pp = lane & 3;
  const lds_cptr wrd = (lds_cptr)lds + (32 * kp + 16 * (gq >> 1) + 4 * (gq & 1) + qq) * 288 + pp * 8;
  int slot = 0;
#define K2_STAGE(set, WN, ADV)                                                                              \
  {                                                                                                         \
    if (more) { asm volatile("s_waitcnt vmcnt(10)\n\ts_barrier" ::: "memory"); stage_w((slot + 3) & 3); } \
    else asm volatile("s_waitcnt vmcnt(" #WN ")\n\ts_barrier" ::: "memory"); \
    const lds_cptr p_ = wrd + slot * K2_SLOT;                                                               \
    const bf16x8 h0 = __builtin_bit_cast(bf16x8, hA[set][0]), h1 = __builtin_bit_cast(bf16x8, hA[set][1]);  \
    _Pragma("unroll") for (int j = 0; j < 9; ++j) {                                                         \
      const bf16x8 wf = cat8(vtr(p_ + j * 32), vtr(p_ + j * 32 + 8 * 288));                                 \
      acc[0][j] = MFMA16(wf, h0, acc[0][j]); acc[1][j] = MFMA16(wf, h1, acc[1][j]);                         \
    }                                                                                                       \
    SBAR();                                                                                                 \
    if (more) { LOAD_A(set); ADV; }                                                                         \
    slot = (slot + 1) & 3;                                                                                  \
  }
#pragma unroll 1
  for (int it = 0; it < 7; ++it) {
    const bool more = it < 6, tail = it == 5;
    if (tail) { wait_ge(ws + WS_FLAGS + FL_O + (R * 4 + q) * 128, 4u); wsrc = wo_main; as0 = as_o; ast = 20480; }
    K2_STAGE(0, 10, (wsrc += tail ? 72 * 1152 : 64 * 1152, as0 += 2048))
    K2_STAGE(1, 5, (wsrc = tail ? wo_ext : wsrc + 64 * 1152, as0 += 2048, avo = (tail && (kp || (lane & 16))) ? 0x7ffffff0u : avo))
    K2_STAGE(2, 0, (wsrc += 64 * 1152, as0 += 2048))
    if (it == 0) STAMP(1);
    if (it == 5) STAMP(2);
  }
  STAMP(3);
  asm volatile("s_waitcnt lgkmcnt(0)\n\ts_barrier" ::: "memory");
  {
    LAS f32x4* xb = (LAS f32x4*)((lds_ptr)lds + w * 9216 + lane * 16);
#pragma unroll
    for (int j = 0; j < 9; ++j) xb[j * 64] = kp ? acc[0][j] : acc[1][j];
    asm volatile("s_waitcnt lgkmcnt(0)\n\ts_barrier" ::: "memory");
    const LAS f32x4* yb = (const LAS f32x4*)((lds_cptr)lds + (w ^ 4) * 9216 + lane * 16);
#pragma unroll
    for (int j = 0; j < 9; ++j) acc[0][j] = (kp ? acc[1][j] : acc[0][j]) + yb[j * 64];
  }
  const auto rsrc = __builtin_amdgcn_make_buffer_rsrc(ws, 0, WS_END, 0x00020000);
  const unsigned tb = WS_P + (R * 4 + c) * 4 * 73728;
  if (rg != q) {
    const unsigned d = tb + rg * 73728 + q * 18432 + kp * 9216 + lane * 16;
#pragma unroll
    for (int j = 0; j < 9; ++j) __builtin_amdgcn_raw_buffer_store_b128(__builtin_bit_cast(u32x4, acc[0][j]), rsrc, d + j * 1024, 0, 16);
    asm volatile("s_waitcnt vmcnt(0)" ::: "memory");
    if (lane == 0) __hip_atomic_fetch_add((GAS unsigned*)(ws + WS_FLAGS + FL_P + (((R * 4 + c) * 4 + rg) * 2 + kp) * 64), 1u, RLX_AGENT);
    STAMP(4);
  } else {
    const int row = R * 128 + rg * 32 + kp * 16 + (lane & 15), col = c * 144 + 4 * gq;
#pragma unroll
    for (int j = 0; j < 9; ++j) {
      const u32x2 xv = *(const u32x2*)(x + (size_t)row * D_ + col + 16 * j), bv = *(const u32x2*)(b2 + col + 16 * j);
      const unsigned x0 = xv[0], x1 = xv[1], b0 = bv[0], b1_ = bv[1];
      acc[0][j][0] += __uint_as_float(x0 << 16) + __uint_as_float(b0 << 16);
      acc[0][j][1] += __uint_as_float(x0 & 0xffff0000u) + __uint_as_float(b0 & 0xffff0000u);
      acc[0][j][2] += __uint_as_float(x1 << 16) + __uint_as_float(b1_ << 16);
      acc[0][j][3] += __uint_as_float(x1 & 0xffff0000u) + __uint_as_float(b1_ & 0xffff0000u);
      PIN(acc[0][j]);
    }
    wait_ge(ws + WS_FLAGS + FL_P + (((R * 4 + c) * 4 + rg) * 2 + kp) * 64, 3u);
    STAMP(5);
    const unsigned d1 = tb + rg * 73728 + ((q + 1) & 3) * 18432 + kp * 9216, d2 = tb + rg * 73728 + ((q + 2) & 3) * 18432 + kp * 9216, d3 = tb + rg * 73728 + ((q + 3) & 3) * 18432 + kp * 9216;
    f32x4 pa[9], pb[9];
#pragma unroll
    for (int j = 0; j < 9; ++j) pa[j] = __builtin_bit_cast(f32x4, __builtin_amdgcn_raw_buffer_load_b128(rsrc, lane * 16 + j * 1024, d1, 16));
#pragma unroll
    for (int j = 0; j < 9; ++j) pb[j] = __builtin_bit_cast(f32x4, __builtin_amdgcn_raw_buffer_load_b128(rsrc, lane * 16 + j * 1024, d2, 16));
    SBAR();
#pragma unroll
    for (int j = 0; j < 9; ++j) { acc[0][j] += pa[j]; PIN(acc[0][j]); pa[j] = __builtin_bit_cast(f32x4, __builtin_amdgcn_raw_buffer_load_b128(rsrc, lane * 16 + j * 1024, d3, 16)); SBAR(); }
#pragma unroll
    for (int j = 0; j < 9; ++j) {
      const f32x4 r = acc[0][j] + pb[j] + pa[j];
      *(u32x2*)(out + (size_t)row * D_ + col + 16 * j) = (u32x2){cvtpk(r[0], r[1]), cvtpk(r[2], r[3])};
    }
    STAMP(6);
  }
}

__global__ __launch_bounds__(512, 2) void k2_kernel(const u16* __restrict__ x, const u16* __restrict__ wo, const u16* __restrict__ w2,
                                                    const u16* __restrict__ b2, u16* __restrict__ out, char* __restrict__ ws, int role) {
  extern __shared__ __attribute__((aligned(16))) char lds[];
  if (role == 0) k2_gemm(x, wo, w2, b2, out, ws, lds, blockIdx.x);
  else k2_attn(ws, lds, blockIdx.x);
}

extern "C" void kernel_launch(void* const* d_in, const int* in_sizes, int n_in, void* d_out, int out_size, void* d_ws, size_t ws_size,
                              hipStream_t stream) {
  const u16* x = (const u16*)d_in[0]; const u16* wqkv = (const u16*)d_in[1]; const u16* wo = (const u16*)d_in[2];
  const u16* w1 = (const u16*)d_in[4]; const u16* b1 = (const u16*)d_in[5]; const u16* w2 = (const u16*)d_in[6]; const u16* b2 = (const u16*)d_in[7];
  (void)hipFuncSetAttribute((const void*)k1_kernel, hipFuncAttributeMaxDynamicSharedMemorySize, K1_LDS);
  K1Tab tab;
  for (int i = 0; i < 16; ++i) for (int cg = 0; cg < 2; ++cg) { unsigned long long v = 0;
    for (int r = 0; r < 8; ++r) v |= (unsigned long long)(k1_tile_h(i >> 2, i & 3, r, cg) & 255) << (8 * r);
    tab.t[i][cg] = v; }
  k1_kernel<<<256, 512, K1_LDS, stream>>>(x, wqkv, w1, b1, (char*)d_ws, tab);
  (void)hipFuncSetAttribute((const void*)k2_kernel, hipFuncAttributeMaxDynamicSharedMemorySize, K2_LDS);
  k2_kernel<<<256, 512, K2_LDS, stream>>>(x, wo, w2, b2, (u16*)d_out, (char*)d_ws, 1);
  k2_kernel<<<256, 512, K2_LDS, stream>>>(x, wo, w2, b2, (u16*)d_out, (char*)d_ws, 0);
}
```

```cpp
#include <hip/hip_runtime.h>
#include <hip/hip_bf16.h>
#include <stdint.h>

typedef unsigned short u16;

constexpr unsigned S_ = 2048, D_ = 576, F_ = 4608;
constexpr unsigned FL_AB = 131072, FL_H = 262144, FL_O = FL_H + 262144, FL_P = FL_O + 262144;
constexpr unsigned WS_H = 1048576;
constexpr unsigned WS_Q = WS_H + S_ * F_ * 2;
constexpr unsigned WS_K = WS_Q + S_ * D_ * 2;
constexpr unsigned WS_V = WS_K + S_ * D_ * 2;
constexpr unsigned WS_O = WS_V + S_ * D_ * 2;
constexpr unsigned WS_P = WS_O + S_ * D_ * 2;
constexpr unsigned WS_END = WS_P + 256 * 40960;
static_assert(WS_END == 39845888, "generated constant");
constexpr unsigned OOB = 0x7ffffff0u;
constexpr unsigned K1_SLOT = 24576, K1_XB = 3 * K1_SLOT, K1_BIAS32 = K1_XB + 5 * 16384, K1_RS = K1_BIAS32 + 2048, AT_ONE = K1_RS + 512, K1_LDS = AT_ONE + 768;

constexpr bool has_lone(int q, int c) { return (c & 1) && !(c == 3 && (q & 1)); }
constexpr int tile_of(int q, int c, int r, int cg, int rt) {
  const int nf = (c & 1) ? 4 : 5, p0 = (9 * c + 1) >> 1, k = q * 2 + (c >> 1);
  int f = r - 1;
  if (c & 1) {
    if (r < 2) return 144 + 2 * (2 * k + r) + cg;
    if (has_lone(q, c) && r == 2) return cg ? -1 : 144 + 48 + (k - (k >> 2));
    f = r - (has_lone(q, c) ? 3 : 2);
  } else if (r == 0) return 144 + 2 * (16 + k) + cg;
  return f < nf ? 36 * q + 2 * (p0 + (f + rt) % nf) + cg : -1;
}
struct STab { unsigned s[256][8][48]; };
struct VTab { unsigned a[64][4]; unsigned b[8][4][64][4]; };
constexpr STab make_stab() {
  STab T{};
  for (unsigned bid = 0; bid < 256; ++bid) for (unsigned w = 0; w < 8; ++w) {
    const unsigned q = bid & 3, idx = bid >> 3, R = ((bid >> 2) & 1) * 8 + (idx >> 2), c = idx & 3, qc = q * 4 + c, rg = w & 3, cg = w >> 2, rg32 = R * 4 + rg;
    const unsigned nf = 5 - (c & 1), t0 = 36 * q + 2 * ((9 * c + 1) >> 1), rot = (idx >> 2) % nf;
    unsigned so[3] = {0, 0, 0}, sc[3] = {0, 0, 0};
    for (int j = 0; j < 3; ++j) { const int t = tile_of(q, c, j, cg, 0);
      so[j] = t < 0 ? OOB : t < 144 ? 0u : WS_Q + ((t - 144) / 18) * (S_ * D_ * 2) + (rg32 * 36 + 2 * ((t - 144) % 18)) * 1024;
      sc[j] = (t >= 144 && t < 162) ? 0x3eae1a93u : 0x3f800000u; }
    const unsigned kvh = has_lone(q, c) ? 73728u : 0u, s0 = 9 * q + 2 * c;
    auto kvs = [&](bool isv, int b) { int blk = 4 * (int)R - 8 + b; blk = blk < 0 ? 0 : blk; return (isv ? WS_V : WS_K) + (blk * 36 + s0) * 1024; };
    const int t1 = tile_of(q, c, 1, 0, 0);
    unsigned* s = T.s[bid][w];
    s[0] = so[2]; s[1] = (q * 144 + 18 * (w >> 1) + 2 * ((w >> 1) & 1) + 8 * (w & 1)) * 1152 + c * 288; s[2] = (q * 144 + 36 * rg + 16) * 1152 + c * 288;
    s[3] = (bid * 8 + w) * 3072; s[4] = rot; s[5] = nf; s[6] = WS_H + (rg32 * 288 + 2 * (t0 + cg)) * 1024;
    s[7] = so[0]; s[8] = so[1]; s[9] = sc[0]; s[10] = sc[1]; s[11] = t0 * 64; s[12] = 0;
    s[13] = w == 0 ? FL_AB + (R * 16 + qc) * 4 : OOB; s[14] = 0; s[15] = 0;
    s[16] = kvh; s[17] = kvs(false, w); s[18] = kvs(w >= 4, w < 4 ? 8 + w : w - 4); s[19] = kvs(true, 4 + w); s[20] = WS_Q + (rg32 * 36 + s0 + cg) * 1024;
    s[21] = w == 0 ? FL_AB + R * 64 - 128 : OOB; s[22] = w == 0 ? 0xffffu : 0u;
    s[23] = (q * 1152 + 8 * w) * 1152 + c * 288; s[24] = WS_H + (rg32 * 288 + 72 * q + 2 * cg) * 1024; s[25] = WS_O + (rg32 * 4 + q) * 9216;
    s[26] = FL_H + ((R * 4 + q) * 8 + w) * 64 + c * 4; s[27] = FL_O + ((R * 4 + q) * 4 + rg) * 64 + (c * 2 + cg) * 4;
    s[28] = R >= 2 ? 0xffffffffu : (R == 1 ? 0xffff0000u : 0u);
    s[29] = ((R * 128 + rg * 32 + cg * 16) * 1152 + c * 288) | (rg == q ? 1u << 27 : 0u) | ((rg32 >= 8 ? 0u : 8 - rg32) << 28);
    s[30] = WS_P + ((R * 4 + c) * 4 + rg) * 40960 + cg * 5120 + (rg == q ? 0 : q * 10240); s[31] = 1024 + (c * 2 + cg) * 4096;
    s[32] = ((idx >> 2) * 4 + rg) * 18 * 9216 + t0 * 64; s[33] = t1 >= 144 ? ((idx >> 2) * 4 + rg) * 18 * 3456 + (t1 - 144) * 64 : OOB;
    s[34] = w == 3 ? idx * 1280 : OOB; s[35] = FL_P + (((R * 4 + c) * 4 + rg) * 2 + cg) * 64 + q * 4;
    for (int g = 0; g < 2; ++g) for (int r = 0; r < 8; ++r) s[38 - 2 * g + (r >> 2)] |= (unsigned)(tile_of(q, c, r, g, idx >> 2) & 255) << (8 * (r & 3));
  }
  return T;
}
constexpr VTab make_vtab() {
  VTab T{};
  for (unsigned l = 0; l < 64; ++l) for (unsigned i = 0; i < 3; ++i) T.a[l][i] = ((i * 64 + l) / 18) * 1152 + ((i * 64 + l) % 18) * 16;
  for (unsigned c = 0; c < 4; ++c) for (unsigned hb = 0; hb < 2; ++hb) for (unsigned l = 0; l < 64; ++l) {
    const unsigned hi = l >> 5, qq = (l & 15) >> 2, pp = l & 3, slot = 4 * ((l >> 4) & 1) + pp, gq = l >> 4;
    const unsigned gr = hb == 0 ? c + slot : (slot < 4 ? c + 5 + slot : c + 4), vf = gr >> 2, vh = gr & 1, vj = (gr >> 1) & 1, vsw = 4 * (vh + 2 * (vf & 1));
    const bool one = slot >= 5;
    auto& e = T.b[c * 2 + hb];
    e[0][l][0] = one ? AT_ONE : 36864 + (24 + vf) * 1024 + vh * 512 + ((4 * hi + qq) ^ vsw) * 16 + vj * 8;
    e[0][l][1] = one ? AT_ONE : 36864 + (24 + vf) * 1024 + vh * 512 + ((8 + 4 * hi + qq) ^ vsw) * 16 + vj * 8;
    e[0][l][2] = one ? 0 : 3072;
    e[0][l][3] = hi == 0 ? 8192 + ((c >> 1) * 32 + l) * 16 + (c & 1) * 8 + hb * 4 : OOB;
    e[1][l][0] = (32 * hb + 16 * (gq >> 1) + 4 * (gq & 1) + qq) * 288 + pp * 8;
    e[1][l][1] = (gq >> 1) * 1024 + (gq & 1) * 512 + (l & 15) * 16;
    e[1][l][2] = (l & 15) * 1152 + gq * 8;
    e[1][l][3] = c * 288 + gq * 8;
    for (int j = 0; j < 8; ++j) { const int col = 8 * (j >> 1) + 2 * (j & 1) + 4 * (int)hi - (int)(4 * c + 2 * hb); e[2 + (j >> 2)][l][j & 3] = (col >= 0 && col < 18) ? 0xffffffffu : 0u; }
  }
  return T;
}
extern "C" {
__device__ __attribute__((used, visibility("hidden"))) extern const STab fk_stab = make_stab();
__device__ __attribute__((used, visibility("hidden"))) extern const VTab fk_vtab = make_vtab();
}
static_assert(sizeof(VTab) == 33792, "generated constant");

__global__ __launch_bounds__(512, 2) void fused_kernel(const u16* x, const u16* wqkv, const u16* w1, const u16* b1, const u16* wo, const u16* w2,
                                                       const u16* b2, u16* out, char* ws) {
  unsigned tid = threadIdx.x, bid = blockIdx.x;
  auto ka = __builtin_amdgcn_kernarg_segment_ptr();
  asm volatile(
      "s_load_dwordx16 s[68:83], s[0:1], 0x0\n\t"
      "s_load_dwordx2 s[84:85], s[0:1], 0x40\n\t"
      "v_readfirstlane_b32 s39, v0\n\t"
      "s_getpc_b64 s[54:55]\n\t"
      "s_add_u32 s54, s54, fk_stab@rel32@lo+4\n\t"
      "s_addc_u32 s55, s55, fk_stab@rel32@hi+12\n\t"
      "s_mul_i32 s51, s2, 1536\n\t"
      "s_mul_i32 s52, s39, 3\n\t"
      "s_add_u32 s51, s51, s52\n\t"
      "s_add_u32 s54, s54, s51\n\t"
      "s_addc_u32 s55, s55, 0\n\t"
      "s_load_dwordx8 s[56:63], s[54:55], 0x80\n\t"
      "s_load_dwordx16 s[4:19], s[54:55], 0x0\n\t"
      "s_load_dwordx16 s[20:35], s[54:55], 0x40\n\t"
      "v_mov_b32 v16, v0\n\t"
      "v_bfe_u32 v19, v0, 3, 3\n\t"
      "v_and_b32 v20, 7, v0\n\t"
      "s_lshr_b32 s39, s39, 6\n\t"
      "s_lshr_b32 s48, s2, 2\n\t"
      "s_and_b32 s48, s48, 6\n\t"
      "s_add_u32 s48, s48, s39\n\t"
      "s_and_b32 s48, s48, 7\n\t"
      "s_lshl_b32 s49, s2, 1\n\t"
      "s_and_b32 s49, s49, 8\n\t"
      "s_lshr_b32 s50, s2, 5\n\t"
      "s_add_u32 s49, s49, s50\n\t"
      "s_lshl_b32 s49, s49, 3\n\t"
      "s_add_u32 s49, s49, s48\n\t"
      "s_mul_i32 s49, s49, 18432\n\t"
      "s_add_u32 s50, s49, 9216\n\t"
      "v_mul_u32_u24 v18, 0x480, v19\n\t"
      "v_lshl_or_b32 v18, v20, 4, v18\n\t"
      "s_mov_b32 s90, 0x240000\n\t"
      "s_mov_b32 s91, 0x20000\n\t"
      "s_lshl_b32 s2, s2, 3\n\t"
      "s_add_u32 s2, s2, s39\n\t"
      "s_mul_i32 s36, s39, 248832\n\t"
      "s_mul_i32 s37, s39, 663552\n\t"
      "s_waitcnt lgkmcnt(0)\n\t"
      "s_mov_b32 s88, s68\n\t"
      "s_mov_b32 s89, s69\n\t"
      "s_setprio 3\n\t"
      "buffer_load_dwordx4 v[0:3], v18, s[88:91], s49 offen offset:0\n\t"
      "buffer_load_dwordx4 v[8:11], v18, s[88:91], s49 offen offset:128\n\t"
      "s_load_dword s1, s[70:71], s36\n\t"
      "s_load_dword s1, s[72:73], s37\n\t"
      "s_load_dword s1, s[84:85], 0x0\n\t"
      "buffer_load_dwordx4 v[32:35], v18, s[88:91], s49 offen offset:256\n\t"
      "buffer_load_dwordx4 v[40:43], v18, s[88:91], s49 offen offset:384\n\t"
      "buffer_load_dwordx4 v[48:51], v18, s[88:91], s49 offen offset:512\n\t"
      "buffer_load_dwordx4 v[80:83], v18, s[88:91], s49 offen offset:640\n\t"
      "buffer_load_dwordx4 v[88:91], v18, s[88:91], s49 offen offset:768\n\t"
      "buffer_load_dwordx4 v[112:115], v18, s[88:91], s49 offen offset:896\n\t"
      "v_and_b32 v17, 63, v16\n\t"
      "v_lshlrev_b32 v240, 4, v17\n\t"
      "v_lshlrev_b32 v246, 2, v17\n\t"
      "v_bfe_u32 v21, v16, 4, 2\n\t"
      "v_and_b32 v22, 48, v240\n\t"
      "v_mul_u32_u24 v241, 0x2400, v19\n\t"
      "v_or_b32 v241, v241, v22\n\t"
      "v_mul_u32_u24 v242, 0xd80, v19\n\t"
      "v_or_b32 v242, v242, v22\n\t"
      "v_lshrrev_b32 v23, 2, v16\n\t"
      "v_xor_b32 v23, v23, v21\n\t"
      "v_bfe_i32 v243, v23, 0, 1\n\t"
      "v_xor_b32 v23, v20, v21\n\t"
      "buffer_load_dwordx4 v[120:123], v18, s[88:91], s49 offen offset:1024\n\t"
      "buffer_load_dwordx4 v[4:7], v18, s[88:91], s50 offen offset:0\n\t"
      "v_lshlrev_b32 v23, 4, v23\n\t"
      "v_lshl_or_b32 v23, v19, 7, v23\n\t"
      "s_lshl_b32 s51, s48, 11\n\t"
      "s_add_u32 s51, s51, 90112\n\t"
      "v_add_u32 v164, s51, v23\n\t"
      "v_xor_b32 v165, 64, v164\n\t"
      "v_add_u32 v165, 1024, v165\n\t"
      "s_lshl_b32 s51, s48, 6\n\t"
      "s_add_u32 s51, s51, 157696\n\t"
      "v_lshl_add_u32 v167, v19, 2, s51\n\t"
      "v_bfe_u32 v24, v16, 5, 1\n\t"
      "v_and_b32 v25, 31, v16\n\t"
      "s_and_b32 s51, s39, 3\n\t"
      "buffer_load_dwordx4 v[12:15], v18, s[88:91], s50 offen offset:128\n\t"
      "buffer_load_dwordx4 v[36:39], v18, s[88:91], s50 offen offset:256\n\t"
      "s_lshl_b32 s51, s51, 5\n\t"
      "v_add_u32 v25, s51, v25\n\t"
      "v_bfe_u32 v23, v16, 1, 3\n\t"
      "v_xor_b32 v23, v23, v24\n\t"
      "v_lshlrev_b32 v23, 4, v23\n\t"
      "v_lshl_or_b32 v23, v25, 7, v23\n\t"
      "v_add_u32 v160, 90112, v23\n\t"
      "v_xor_b32 v161, 32, v160\n\t"
      "v_xor_b32 v162, 64, v160\n\t"
      "v_xor_b32 v163, 0x60, v160\n\t"
      "v_lshlrev_b32 v168, 2, v25\n\t"
      "v_add_u32 v168, 157696, v168\n\t"
      "v_lshlrev_b32 v169, 1, v16\n\t"
      "buffer_load_dwordx4 v[44:47], v18, s[88:91], s50 offen offset:384\n\t"
      "buffer_load_dwordx4 v[52:55], v18, s[88:91], s50 offen offset:512\n\t"
      "v_lshlrev_b32 v166, 2, v16\n\t"
      "v_add_u32 v166, 155648, v166\n\t"
      "s_lshr_b32 s51, s39, 2\n\t"
      "s_lshl_b32 s52, s51, 7\n\t"
      "s_add_u32 s52, s52, 155648\n\t"
      "v_lshl_add_u32 v245, v24, 4, s52\n\t"
      "v_bfe_u32 v23, v16, 2, 2\n\t"
      "v_lshl_or_b32 v23, v24, 3, v23\n\t"
      "v_lshlrev_b32 v232, 7, v23\n\t"
      "v_and_b32 v23, 3, v16\n\t"
      "v_lshl_or_b32 v232, v23, 3, v232\n\t"
      "v_and_b32 v23, 1, v21\n\t"
      "v_lshl_or_b32 v232, v23, 5, v232\n\t"
      "buffer_load_dwordx4 v[84:87], v18, s[88:91], s50 offen offset:640\n\t"
      "buffer_load_dwordx4 v[92:95], v18, s[88:91], s50 offen offset:768\n\t"
      "v_xor_b32 v23, s51, v19\n\t"
      "v_and_b32 v23, 1, v23\n\t"
      "v_lshl_or_b32 v232, v23, 6, v232\n\t"
      "v_mov_b32 v26, 0x3f803f80\n\t"
      "v_mov_b32 v27, 0x3f803f80\n\t"
      "v_lshlrev_b32 v23, 3, v17\n\t"
      "v_add_u32 v23, 158208, v23\n\t"
      "ds_write_b64 v23, v[26:27]\n\t"
      "v_lshrrev_b32 v23, 1, v17\n\t"
      "v_mul_u32_u24 v171, 0xd80, v23\n\t"
      "v_lshlrev_b32 v239, 7, v17\n\t"
      "buffer_load_dwordx4 v[116:119], v18, s[88:91], s50 offen offset:896\n\t"
      "buffer_load_dwordx4 v[124:127], v18, s[88:91], s50 offen offset:1024\n\t"
      "s_getpc_b64 s[86:87]\n\t"
      "s_add_u32 s86, s86, fk_vtab@rel32@lo+4\n\t"
      "s_addc_u32 s87, s87, fk_vtab@rel32@hi+12\n\t"
      "s_mov_b32 s43, 0x20000\n\t"
      "s_mov_b32 s47, 0x20000\n\t"
      "s_mov_b32 s95, 0x20000\n\t"
      "s_mov_b32 s44, s84\n\t"
      "s_mov_b32 s45, s85\n\t"
      "s_mov_b32 s46, 39845888\n\t"
      "s_mov_b32 s66, 0x1e6000\n\t"
      "s_mov_b32 s67, 27648\n\t"
      "s_mul_i32 s36, s39, 221184\n\t"
      "s_mul_i32 s37, s39, 82944\n\t"
      "s_sub_u32 s37, s37, 9216\n\t"
      "s_mul_i32 s38, s39, 3072\n\t"
      "s_mov_b32 s50, s38\n\t"
      "s_mov_b32 s53, 0x3f803f80\n\t"
      "v_mov_b32 v216, 0\n\t"
      "v_mov_b32 v217, 0\n\t"
      "v_mov_b32 v218, 0\n\t"
      "v_mov_b32 v219, 0\n\t"
      "v_add_u32 v233, 73728, v232\n\t"
      "v_add_u32 v236, 49152, v232\n\t"
      "v_mov_b32 v244, 0xc0135761\n\t"
      "v_subrev_u32 v192, 16384, v164\n\t"
      "v_subrev_u32 v193, 16384, v165\n\t"
      "v_subrev_u32 v194, 16384, v160\n\t"
      "v_subrev_u32 v195, 16384, v161\n\t"
      "v_subrev_u32 v196, 16384, v162\n\t"
      "v_subrev_u32 v197, 16384, v163\n\t"
      "s_getpc_b64 s[92:93]\n\t"
      "Lbeg%=:\n\t"
      "s_and_b32 s93, s93, 0xffff\n\t"
      "s_mov_b32 s94, Lend%=-Lbeg%=\n\t"
      "s_mov_b32 s88, s86\n\t"
      "s_mov_b32 s89, s87\n\t"
      "s_mov_b32 s90, 33792\n\t"
      "v_mov_b32 v28, 0x00003f80\n\t"
      "v_mov_b32 v29, 0x3f800000\n\t"
      "s_setprio 0\n\t"
      "s_mov_b64 s[64:65], s[60:61]\n\t"
      "s_mov_b32 s3, s59\n\t"
      "s_mov_b32 s52, s58\n\t"
      "s_mov_b32 s51, s57\n\t"
      "s_mov_b32 s58, 0xbdd2d3e8\n\t"
      "s_mov_b32 s59, 0xffff0000\n\t"
      "s_mov_b32 s61, 0x40000\n\t"
      "s_waitcnt vmcnt(17)\n\t"
      "v_dot2c_f32_bf16 v216, s53, v0\n\t"
      "v_dot2c_f32_bf16 v218, v0, v0\n\t"
      "v_dot2c_f32_bf16 v216, s53, v1\n\t"
      "v_dot2c_f32_bf16 v218, v1, v1\n\t"
      "v_dot2c_f32_bf16 v216, s53, v2\n\t"
      "v_dot2c_f32_bf16 v218, v2, v2\n\t"
      "v_dot2c_f32_bf16 v216, s53, v3\n\t"
      "v_dot2c_f32_bf16 v218, v3, v3\n\t"
      "s_waitcnt vmcnt(16)\n\t"
      "v_dot2c_f32_bf16 v216, s53, v8\n\t"
      "v_dot2c_f32_bf16 v218, v8, v8\n\t"
      "v_dot2c_f32_bf16 v216, s53, v9\n\t"
      "v_dot2c_f32_bf16 v218, v9, v9\n\t"
      "v_dot2c_f32_bf16 v216, s53, v10\n\t"
      "v_dot2c_f32_bf16 v218, v10, v10\n\t"
      "v_dot2c_f32_bf16 v216, s53, v11\n\t"
      "v_dot2c_f32_bf16 v218, v11, v11\n\t"
      "s_waitcnt vmcnt(15)\n\t"
      "v_dot2c_f32_bf16 v216, s53, v32\n\t"
      "v_dot2c_f32_bf16 v218, v32, v32\n\t"
      "v_dot2c_f32_bf16 v216, s53, v33\n\t"
      "v_dot2c_f32_bf16 v218, v33, v33\n\t"
      "v_dot2c_f32_bf16 v216, s53, v34\n\t"
      "v_dot2c_f32_bf16 v218, v34, v34\n\t"
      "v_dot2c_f32_bf16 v216, s53, v35\n\t"
      "v_dot2c_f32_bf16 v218, v35, v35\n\t"
      "s_waitcnt vmcnt(14)\n\t"
      "v_dot2c_f32_bf16 v216, s53, v40\n\t"
      "v_dot2c_f32_bf16 v218, v40, v40\n\t"
      "v_dot2c_f32_bf16 v216, s53, v41\n\t"
      "v_dot2c_f32_bf16 v218, v41, v41\n\t"
      "v_dot2c_f32_bf16 v216, s53, v42\n\t"
      "v_dot2c_f32_bf16 v218, v42, v42\n\t"
      "v_dot2c_f32_bf16 v216, s53, v43\n\t"
      "v_dot2c_f32_bf16 v218, v43, v43\n\t"
      "s_waitcnt vmcnt(13)\n\t"
      "v_dot2c_f32_bf16 v216, s53, v48\n\t"
      "v_dot2c_f32_bf16 v218, v48, v48\n\t"
      "v_dot2c_f32_bf16 v216, s53, v49\n\t"
      "v_dot2c_f32_bf16 v218, v49, v49\n\t"
      "v_dot2c_f32_bf16 v216, s53, v50\n\t"
      "v_dot2c_f32_bf16 v218, v50, v50\n\t"
      "v_dot2c_f32_bf16 v216, s53, v51\n\t"
      "v_dot2c_f32_bf16 v218, v51, v51\n\t"
      "s_waitcnt vmcnt(12)\n\t"
      "v_dot2c_f32_bf16 v216, s53, v80\n\t"
      "v_dot2c_f32_bf16 v218, v80, v80\n\t"
      "v_dot2c_f32_bf16 v216, s53, v81\n\t"
      "v_dot2c_f32_bf16 v218, v81, v81\n\t"
      "v_dot2c_f32_bf16 v216, s53, v82\n\t"
      "v_dot2c_f32_bf16 v218, v82, v82\n\t"
      "v_dot2c_f32_bf16 v216, s53, v83\n\t"
      "v_dot2c_f32_bf16 v218, v83, v83\n\t"
      "s_waitcnt vmcnt(11)\n\t"
      "v_dot2c_f32_bf16 v216, s53, v88\n\t"
      "v_dot2c_f32_bf16 v218, v88, v88\n\t"
      "v_dot2c_f32_bf16 v216, s53, v89\n\t"
      "v_dot2c_f32_bf16 v218, v89, v89\n\t"
      "v_dot2c_f32_bf16 v216, s53, v90\n\t"
      "v_dot2c_f32_bf16 v218, v90, v90\n\t"
      "v_dot2c_f32_bf16 v216, s53, v91\n\t"
      "v_dot2c_f32_bf16 v218, v91, v91\n\t"
      "s_waitcnt vmcnt(10)\n\t"
      "v_dot2c_f32_bf16 v216, s53, v112\n\t"
      "v_dot2c_f32_bf16 v218, v112, v112\n\t"
      "v_dot2c_f32_bf16 v216, s53, v113\n\t"
      "v_dot2c_f32_bf16 v218, v113, v113\n\t"
      "v_dot2c_f32_bf16 v216, s53, v114\n\t"
      "v_dot2c_f32_bf16 v218, v114, v114\n\t"
      "v_dot2c_f32_bf16 v216, s53, v115\n\t"
      "v_dot2c_f32_bf16 v218, v115, v115\n\t"
      "s_waitcnt vmcnt(9)\n\t"
      "v_dot2c_f32_bf16 v216, s53, v120\n\t"
      "v_dot2c_f32_bf16 v218, v120, v120\n\t"
      "v_dot2c_f32_bf16 v216, s53, v121\n\t"
      "v_dot2c_f32_bf16 v218, v121, v121\n\t"
      "v_dot2c_f32_bf16 v216, s53, v122\n\t"
      "v_dot2c_f32_bf16 v218, v122, v122\n\t"
      "v_dot2c_f32_bf16 v216, s53, v123\n\t"
      "v_dot2c_f32_bf16 v218, v123, v123\n\t"
      "s_nop 2\n\t"
      "v_add_f32_dpp v216, v216, v216 quad_perm:[1,0,3,2] row_mask:0xf bank_mask:0xf\n\t"
      "v_add_f32_dpp v218, v218, v218 quad_perm:[1,0,3,2] row_mask:0xf bank_mask:0xf\n\t"
      "s_nop 0\n\t"
      "v_add_f32_dpp v216, v216, v216 quad_perm:[2,3,0,1] row_mask:0xf bank_mask:0xf\n\t"
      "v_add_f32_dpp v218, v218, v218 quad_perm:[2,3,0,1] row_mask:0xf bank_mask:0xf\n\t"
      "s_nop 0\n\t"
      "v_add_f32_dpp v216, v216, v216 row_half_mirror row_mask:0xf bank_mask:0xf\n\t"
      "v_add_f32_dpp v218, v218, v218 row_half_mirror row_mask:0xf bank_mask:0xf\n\t"
      "s_nop 0\n\t"
      "v_mul_f32 v216, 0x3ae38e39, v216\n\t"
      "v_cvt_pk_bf16_f32 v220, v216, v216\n\t"
      "v_and_b32 v220, 0xffff0000, v220\n\t"
      "v_xor_b32 v220, 0x80000000, v220\n\t"
      "v_dot2_f32_bf16 v224, v0, v28, v220\n\t"
      "v_dot2_f32_bf16 v225, v0, v29, v220\n\t"
      "v_dot2_f32_bf16 v226, v1, v28, v220\n\t"
      "v_dot2_f32_bf16 v227, v1, v29, v220\n\t"
      "v_dot2_f32_bf16 v228, v2, v28, v220\n\t"
      "v_dot2_f32_bf16 v229, v2, v29, v220\n\t"
      "v_dot2_f32_bf16 v230, v3, v28, v220\n\t"
      "v_dot2_f32_bf16 v231, v3, v29, v220\n\t"
      "v_cvt_pk_bf16_f32 v0, v224, v225\n\t"
      "v_cvt_pk_bf16_f32 v1, v226, v227\n\t"
      "v_cvt_pk_bf16_f32 v2, v228, v229\n\t"
      "v_cvt_pk_bf16_f32 v3, v230, v231\n\t"
      "ds_write_b128 v192, v[0:3]\n\t"
      "v_dot2_f32_bf16 v224, v8, v28, v220\n\t"
      "v_dot2_f32_bf16 v225, v8, v29, v220\n\t"
      "v_dot2_f32_bf16 v226, v9, v28, v220\n\t"
      "v_dot2_f32_bf16 v227, v9, v29, v220\n\t"
      "v_dot2_f32_bf16 v228, v10, v28, v220\n\t"
      "v_dot2_f32_bf16 v229, v10, v29, v220\n\t"
      "v_dot2_f32_bf16 v230, v11, v28, v220\n\t"
      "v_dot2_f32_bf16 v231, v11, v29, v220\n\t"
      "v_cvt_pk_bf16_f32 v8, v224, v225\n\t"
      "v_cvt_pk_bf16_f32 v9, v226, v227\n\t"
      "v_cvt_pk_bf16_f32 v10, v228, v229\n\t"
      "v_cvt_pk_bf16_f32 v11, v230, v231\n\t"
      "ds_write_b128 v164, v[8:11] offset:0\n\t"
      "v_dot2_f32_bf16 v224, v32, v28, v220\n\t"
      "v_dot2_f32_bf16 v225, v32, v29, v220\n\t"
      "v_dot2_f32_bf16 v226, v33, v28, v220\n\t"
      "v_dot2_f32_bf16 v227, v33, v29, v220\n\t"
      "v_dot2_f32_bf16 v228, v34, v28, v220\n\t"
      "v_dot2_f32_bf16 v229, v34, v29, v220\n\t"
      "v_dot2_f32_bf16 v230, v35, v28, v220\n\t"
      "v_dot2_f32_bf16 v231, v35, v29, v220\n\t"
      "v_cvt_pk_bf16_f32 v32, v224, v225\n\t"
      "v_cvt_pk_bf16_f32 v33, v226, v227\n\t"
      "v_cvt_pk_bf16_f32 v34, v228, v229\n\t"
      "v_cvt_pk_bf16_f32 v35, v230, v231\n\t"
      "ds_write_b128 v164, v[32:35] offset:16384\n\t"
      "v_dot2_f32_bf16 v224, v40, v28, v220\n\t"
      "v_dot2_f32_bf16 v225, v40, v29, v220\n\t"
      "v_dot2_f32_bf16 v226, v41, v28, v220\n\t"
      "v_dot2_f32_bf16 v227, v41, v29, v220\n\t"
      "v_dot2_f32_bf16 v228, v42, v28, v220\n\t"
      "v_dot2_f32_bf16 v229, v42, v29, v220\n\t"
      "v_dot2_f32_bf16 v230, v43, v28, v220\n\t"
      "v_dot2_f32_bf16 v231, v43, v29, v220\n\t"
      "v_cvt_pk_bf16_f32 v40, v224, v225\n\t"
      "v_cvt_pk_bf16_f32 v41, v226, v227\n\t"
      "v_cvt_pk_bf16_f32 v42, v228, v229\n\t"
      "v_cvt_pk_bf16_f32 v43, v230, v231\n\t"
      "ds_write_b128 v164, v[40:43] offset:32768\n\t"
      "v_dot2_f32_bf16 v224, v48, v28, v220\n\t"
      "v_dot2_f32_bf16 v225, v48, v29, v220\n\t"
      "v_dot2_f32_bf16 v226, v49, v28, v220\n\t"
      "v_dot2_f32_bf16 v227, v49, v29, v220\n\t"
      "v_dot2_f32_bf16 v228, v50, v28, v220\n\t"
      "v_dot2_f32_bf16 v229, v50, v29, v220\n\t"
      "v_dot2_f32_bf16 v230, v51, v28, v220\n\t"
      "v_dot2_f32_bf16 v231, v51, v29, v220\n\t"
      "v_cvt_pk_bf16_f32 v48, v224, v225\n\t"
      "v_cvt_pk_bf16_f32 v49, v226, v227\n\t"
      "v_cvt_pk_bf16_f32 v50, v228, v229\n\t"
      "v_cvt_pk_bf16_f32 v51, v230, v231\n\t"
      "ds_write_b128 v164, v[48:51] offset:49152\n\t"
      "v_dot2_f32_bf16 v224, v80, v28, v220\n\t"
      "v_dot2_f32_bf16 v225, v80, v29, v220\n\t"
      "v_dot2_f32_bf16 v226, v81, v28, v220\n\t"
      "v_dot2_f32_bf16 v227, v81, v29, v220\n\t"
      "v_dot2_f32_bf16 v228, v82, v28, v220\n\t"
      "v_dot2_f32_bf16 v229, v82, v29, v220\n\t"
      "v_dot2_f32_bf16 v230, v83, v28, v220\n\t"
      "v_dot2_f32_bf16 v231, v83, v29, v220\n\t"
      "v_cvt_pk_bf16_f32 v80, v224, v225\n\t"
      "v_cvt_pk_bf16_f32 v81, v226, v227\n\t"
      "v_cvt_pk_bf16_f32 v82, v228, v229\n\t"
      "v_cvt_pk_bf16_f32 v83, v230, v231\n\t"
      "v_dot2_f32_bf16 v224, v88, v28, v220\n\t"
      "v_dot2_f32_bf16 v225, v88, v29, v220\n\t"
      "v_dot2_f32_bf16 v226, v89, v28, v220\n\t"
      "v_dot2_f32_bf16 v227, v89, v29, v220\n\t"
      "v_dot2_f32_bf16 v228, v90, v28, v220\n\t"
      "v_dot2_f32_bf16 v229, v90, v29, v220\n\t"
      "v_dot2_f32_bf16 v230, v91, v28, v220\n\t"
      "v_dot2_f32_bf16 v231, v91, v29, v220\n\t"
      "v_cvt_pk_bf16_f32 v88, v224, v225\n\t"
      "v_cvt_pk_bf16_f32 v89, v226, v227\n\t"
      "v_cvt_pk_bf16_f32 v90, v228, v229\n\t"
      "v_cvt_pk_bf16_f32 v91, v230, v231\n\t"
      "v_dot2_f32_bf16 v224, v112, v28, v220\n\t"
      "v_dot2_f32_bf16 v225, v112, v29, v220\n\t"
      "v_dot2_f32_bf16 v226, v113, v28, v220\n\t"
      "v_dot2_f32_bf16 v227, v113, v29, v220\n\t"
      "v_dot2_f32_bf16 v228, v114, v28, v220\n\t"
      "v_dot2_f32_bf16 v229, v114, v29, v220\n\t"
      "v_dot2_f32_bf16 v230, v115, v28, v220\n\t"
      "v_dot2_f32_bf16 v231, v115, v29, v220\n\t"
      "v_cvt_pk_bf16_f32 v112, v224, v225\n\t"
      "v_cvt_pk_bf16_f32 v113, v226, v227\n\t"
      "v_cvt_pk_bf16_f32 v114, v228, v229\n\t"
      "v_cvt_pk_bf16_f32 v115, v230, v231\n\t"
      "v_dot2_f32_bf16 v224, v120, v28, v220\n\t"
      "v_dot2_f32_bf16 v225, v120, v29, v220\n\t"
      "v_dot2_f32_bf16 v226, v121, v28, v220\n\t"
      "v_dot2_f32_bf16 v227, v121, v29, v220\n\t"
      "v_dot2_f32_bf16 v228, v122, v28, v220\n\t"
      "v_dot2_f32_bf16 v229, v122, v29, v220\n\t"
      "v_dot2_f32_bf16 v230, v123, v28, v220\n\t"
      "v_dot2_f32_bf16 v231, v123, v29, v220\n\t"
      "v_cvt_pk_bf16_f32 v120, v224, v225\n\t"
      "v_cvt_pk_bf16_f32 v121, v226, v227\n\t"
      "v_cvt_pk_bf16_f32 v122, v228, v229\n\t"
      "v_cvt_pk_bf16_f32 v123, v230, v231\n\t"
      "s_cmp_ge_u32 s39, 4\n\t"
      "s_cbranch_scc1 Lyoung%=\n\t"
      "s_and_b32 s54, s62, 255\n\t"
      "s_and_b32 s55, s64, 255\n\t"
      "s_lshr_b64 s[62:63], s[62:63], 8\n\t"
      "s_lshr_b64 s[64:65], s[64:65], 8\n\t"
      "s_cmp_eq_u32 s55, 255\n\t"
      "s_cselect_b32 s55, 0x40000000, 64\n\t"
      "s_cmp_lt_u32 s54, 144\n\t"
      "s_cselect_b32 s40, s72, s70\n\t"
      "s_cselect_b32 s41, s73, s71\n\t"
      "s_cselect_b32 s42, 0x510000, s66\n\t"
      "s_cselect_b32 s49, 73728, s67\n\t"
      "s_cselect_b32 s48, s36, s37\n\t"
      "s_cselect_b64 vcc, -1, 0\n\t"
      "v_cndmask_b32 v234, v242, v241, vcc\n\t"
      "s_cmp_eq_u32 s54, 255\n\t"
      "s_cselect_b32 s42, 0, s42\n\t"
      "s_lshl_b32 s54, s54, 6\n\t"
      "s_add_u32 s48, s48, s54\n\t"
      "v_and_b32 v223, s55, v243\n\t"
      "v_add_u32 v234, v234, v223\n\t"
      "s_add_u32 m0, s50, 12288\n\t"
      "s_add_u32 s55, s48, 331776\n\t"
      "buffer_load_dwordx4 v234, s[40:43], s55 offen lds\n\t"
      "s_mov_b32 m0, s50\n\t"
      "s_add_u32 s50, s50, 1024\n\t"
      "buffer_load_dwordx4 v234, s[40:43], s48 offen lds\n\t"
      "s_add_u32 s48, s48, s49\n\t"
      "s_add_u32 m0, s50, 12288\n\t"
      "s_add_u32 s55, s48, 331776\n\t"
      "buffer_load_dwordx4 v234, s[40:43], s55 offen lds\n\t"
      "s_mov_b32 m0, s50\n\t"
      "s_add_u32 s50, s50, 1024\n\t"
      "buffer_load_dwordx4 v234, s[40:43], s48 offen lds\n\t"
      "s_add_u32 s48, s48, s49\n\t"
      "s_add_u32 m0, s50, 12288\n\t"
      "s_add_u32 s55, s48, 331776\n\t"
      "buffer_load_dwordx4 v234, s[40:43], s55 offen lds\n\t"
      "s_mov_b32 m0, s50\n\t"
      "s_add_u32 s50, s50, 1024\n\t"
      "buffer_load_dwordx4 v234, s[40:43], s48 offen lds\n\t"
      "s_add_u32 s48, s48, s49\n\t"
      "s_mul_i32 s54, s49, 21\n\t"
      "s_add_u32 s48, s48, s54\n\t"
      "s_add_u32 s50, s50, 21504\n\t"
      "s_cmp_ge_u32 s50, 147456\n\t"
      "s_cselect_b32 s50, s38, s50\n\t"
      "s_waitcnt vmcnt(14)\n\t"
      "v_dot2c_f32_bf16 v217, s53, v4\n\t"
      "v_dot2c_f32_bf16 v219, v4, v4\n\t"
      "v_dot2c_f32_bf16 v217, s53, v5\n\t"
      "v_dot2c_f32_bf16 v219, v5, v5\n\t"
      "v_dot2c_f32_bf16 v217, s53, v6\n\t"
      "v_dot2c_f32_bf16 v219, v6, v6\n\t"
      "v_dot2c_f32_bf16 v217, s53, v7\n\t"
      "v_dot2c_f32_bf16 v219, v7, v7\n\t"
      "s_waitcnt vmcnt(13)\n\t"
      "v_dot2c_f32_bf16 v217, s53, v12\n\t"
      "v_dot2c_f32_bf16 v219, v12, v12\n\t"
      "v_dot2c_f32_bf16 v217, s53, v13\n\t"
      "v_dot2c_f32_bf16 v219, v13, v13\n\t"
      "v_dot2c_f32_bf16 v217, s53, v14\n\t"
      "v_dot2c_f32_bf16 v219, v14, v14\n\t"
      "v_dot2c_f32_bf16 v217, s53, v15\n\t"
      "v_dot2c_f32_bf16 v219, v15, v15\n\t"
      "s_waitcnt vmcnt(12)\n\t"
      "v_dot2c_f32_bf16 v217, s53, v36\n\t"
      "v_dot2c_f32_bf16 v219, v36, v36\n\t"
      "v_dot2c_f32_bf16 v217, s53, v37\n\t"
      "v_dot2c_f32_bf16 v219, v37, v37\n\t"
      "v_dot2c_f32_bf16 v217, s53, v38\n\t"
      "v_dot2c_f32_bf16 v219, v38, v38\n\t"
      "v_dot2c_f32_bf16 v217, s53, v39\n\t"
      "v_dot2c_f32_bf16 v219, v39, v39\n\t"
      "s_waitcnt vmcnt(11)\n\t"
      "v_dot2c_f32_bf16 v217, s53, v44\n\t"
      "v_dot2c_f32_bf16 v219, v44, v44\n\t"
      "v_dot2c_f32_bf16 v217, s53, v45\n\t"
      "v_dot2c_f32_bf16 v219, v45, v45\n\t"
      "v_dot2c_f32_bf16 v217, s53, v46\n\t"
      "v_dot2c_f32_bf16 v219, v46, v46\n\t"
      "v_dot2c_f32_bf16 v217, s53, v47\n\t"
      "v_dot2c_f32_bf16 v219, v47, v47\n\t"
      "s_waitcnt vmcnt(10)\n\t"
      "v_dot2c_f32_bf16 v217, s53, v52\n\t"
      "v_dot2c_f32_bf16 v219, v52, v52\n\t"
      "v_dot2c_f32_bf16 v217, s53, v53\n\t"
      "v_dot2c_f32_bf16 v219, v53, v53\n\t"
      "v_dot2c_f32_bf16 v217, s53, v54\n\t"
      "v_dot2c_f32_bf16 v219, v54, v54\n\t"
      "v_dot2c_f32_bf16 v217, s53, v55\n\t"
      "v_dot2c_f32_bf16 v219, v55, v55\n\t"
      "s_waitcnt vmcnt(9)\n\t"
      "v_dot2c_f32_bf16 v217, s53, v84\n\t"
      "v_dot2c_f32_bf16 v219, v84, v84\n\t"
      "v_dot2c_f32_bf16 v217, s53, v85\n\t"
      "v_dot2c_f32_bf16 v219, v85, v85\n\t"
      "v_dot2c_f32_bf16 v217, s53, v86\n\t"
      "v_dot2c_f32_bf16 v219, v86, v86\n\t"
      "v_dot2c_f32_bf16 v217, s53, v87\n\t"
      "v_dot2c_f32_bf16 v219, v87, v87\n\t"
      "s_waitcnt vmcnt(8)\n\t"
      "v_dot2c_f32_bf16 v217, s53, v92\n\t"
      "v_dot2c_f32_bf16 v219, v92, v92\n\t"
      "v_dot2c_f32_bf16 v217, s53, v93\n\t"
      "v_dot2c_f32_bf16 v219, v93, v93\n\t"
      "v_dot2c_f32_bf16 v217, s53, v94\n\t"
      "v_dot2c_f32_bf16 v219, v94, v94\n\t"
      "v_dot2c_f32_bf16 v217, s53, v95\n\t"
      "v_dot2c_f32_bf16 v219, v95, v95\n\t"
      "s_waitcnt vmcnt(7)\n\t"
      "v_dot2c_f32_bf16 v217, s53, v116\n\t"
      "v_dot2c_f32_bf16 v219, v116, v116\n\t"
      "v_dot2c_f32_bf16 v217, s53, v117\n\t"
      "v_dot2c_f32_bf16 v219, v117, v117\n\t"
      "v_dot2c_f32_bf16 v217, s53, v118\n\t"
      "v_dot2c_f32_bf16 v219, v118, v118\n\t"
      "v_dot2c_f32_bf16 v217, s53, v119\n\t"
      "v_dot2c_f32_bf16 v219, v119, v119\n\t"
      "s_waitcnt vmcnt(6)\n\t"
      "v_dot2c_f32_bf16 v217, s53, v124\n\t"
      "v_dot2c_f32_bf16 v219, v124, v124\n\t"
      "v_dot2c_f32_bf16 v217, s53, v125\n\t"
      "v_dot2c_f32_bf16 v219, v125, v125\n\t"
      "v_dot2c_f32_bf16 v217, s53, v126\n\t"
      "v_dot2c_f32_bf16 v219, v126, v126\n\t"
      "v_dot2c_f32_bf16 v217, s53, v127\n\t"
      "v_dot2c_f32_bf16 v219, v127, v127\n\t"
      "s_nop 2\n\t"
      "v_add_f32_dpp v217, v217, v217 quad_perm:[1,0,3,2] row_mask:0xf bank_mask:0xf\n\t"
      "v_add_f32_dpp v219, v219, v219 quad_perm:[1,0,3,2] row_mask:0xf bank_mask:0xf\n\t"
      "s_nop 0\n\t"
      "v_add_f32_dpp v217, v217, v217 quad_perm:[2,3,0,1] row_mask:0xf bank_mask:0xf\n\t"
      "v_add_f32_dpp v219, v219, v219 quad_perm:[2,3,0,1] row_mask:0xf bank_mask:0xf\n\t"
      "s_nop 0\n\t"
      "v_add_f32_dpp v217, v217, v217 row_half_mirror row_mask:0xf bank_mask:0xf\n\t"
      "v_add_f32_dpp v219, v219, v219 row_half_mirror row_mask:0xf bank_mask:0xf\n\t"
      "s_nop 0\n\t"
      "v_mul_f32 v217, 0x3ae38e39, v217\n\t"
      "v_cvt_pk_bf16_f32 v222, v217, v217\n\t"
      "v_and_b32 v222, 0xffff0000, v222\n\t"
      "v_xor_b32 v222, 0x80000000, v222\n\t"
      "v_dot2_f32_bf16 v224, v4, v28, v222\n\t"
      "v_dot2_f32_bf16 v225, v4, v29, v222\n\t"
      "v_dot2_f32_bf16 v226, v5, v28, v222\n\t"
      "v_dot2_f32_bf16 v227, v5, v29, v222\n\t"
      "v_dot2_f32_bf16 v228, v6, v28, v222\n\t"
      "v_dot2_f32_bf16 v229, v6, v29, v222\n\t"
      "v_dot2_f32_bf16 v230, v7, v28, v222\n\t"
      "v_dot2_f32_bf16 v231, v7, v29, v222\n\t"
      "v_cvt_pk_bf16_f32 v4, v224, v225\n\t"
      "v_cvt_pk_bf16_f32 v5, v226, v227\n\t"
      "v_cvt_pk_bf16_f32 v6, v228, v229\n\t"
      "v_cvt_pk_bf16_f32 v7, v230, v231\n\t"
      "ds_write_b128 v193, v[4:7]\n\t"
      "v_dot2_f32_bf16 v224, v12, v28, v222\n\t"
      "v_dot2_f32_bf16 v225, v12, v29, v222\n\t"
      "v_dot2_f32_bf16 v226, v13, v28, v222\n\t"
      "v_dot2_f32_bf16 v227, v13, v29, v222\n\t"
      "v_dot2_f32_bf16 v228, v14, v28, v222\n\t"
      "v_dot2_f32_bf16 v229, v14, v29, v222\n\t"
      "v_dot2_f32_bf16 v230, v15, v28, v222\n\t"
      "v_dot2_f32_bf16 v231, v15, v29, v222\n\t"
      "v_cvt_pk_bf16_f32 v12, v224, v225\n\t"
      "v_cvt_pk_bf16_f32 v13, v226, v227\n\t"
      "v_cvt_pk_bf16_f32 v14, v228, v229\n\t"
      "v_cvt_pk_bf16_f32 v15, v230, v231\n\t"
      "ds_write_b128 v165, v[12:15] offset:0\n\t"
      "v_dot2_f32_bf16 v224, v36, v28, v222\n\t"
      "v_dot2_f32_bf16 v225, v36, v29, v222\n\t"
      "v_dot2_f32_bf16 v226, v37, v28, v222\n\t"
      "v_dot2_f32_bf16 v227, v37, v29, v222\n\t"
      "v_dot2_f32_bf16 v228, v38, v28, v222\n\t"
      "v_dot2_f32_bf16 v229, v38, v29, v222\n\t"
      "v_dot2_f32_bf16 v230, v39, v28, v222\n\t"
      "v_dot2_f32_bf16 v231, v39, v29, v222\n\t"
      "v_cvt_pk_bf16_f32 v36, v224, v225\n\t"
      "v_cvt_pk_bf16_f32 v37, v226, v227\n\t"
      "v_cvt_pk_bf16_f32 v38, v228, v229\n\t"
      "v_cvt_pk_bf16_f32 v39, v230, v231\n\t"
      "ds_write_b128 v165, v[36:39] offset:16384\n\t"
      "v_dot2_f32_bf16 v224, v44, v28, v222\n\t"
      "v_dot2_f32_bf16 v225, v44, v29, v222\n\t"
      "v_dot2_f32_bf16 v226, v45, v28, v222\n\t"
      "v_dot2_f32_bf16 v227, v45, v29, v222\n\t"
      "v_dot2_f32_bf16 v228, v46, v28, v222\n\t"
      "v_dot2_f32_bf16 v229, v46, v29, v222\n\t"
      "v_dot2_f32_bf16 v230, v47, v28, v222\n\t"
      "v_dot2_f32_bf16 v231, v47, v29, v222\n\t"
      "v_cvt_pk_bf16_f32 v44, v224, v225\n\t"
      "v_cvt_pk_bf16_f32 v45, v226, v227\n\t"
      "v_cvt_pk_bf16_f32 v46, v228, v229\n\t"
      "v_cvt_pk_bf16_f32 v47, v230, v231\n\t"
      "ds_write_b128 v165, v[44:47] offset:32768\n\t"
      "v_dot2_f32_bf16 v224, v52, v28, v222\n\t"
      "v_dot2_f32_bf16 v225, v52, v29, v222\n\t"
      "v_dot2_f32_bf16 v226, v53, v28, v222\n\t"
      "v_dot2_f32_bf16 v227, v53, v29, v222\n\t"
      "v_dot2_f32_bf16 v228, v54, v28, v222\n\t"
      "v_dot2_f32_bf16 v229, v54, v29, v222\n\t"
      "v_dot2_f32_bf16 v230, v55, v28, v222\n\t"
      "v_dot2_f32_bf16 v231, v55, v29, v222\n\t"
      "v_cvt_pk_bf16_f32 v52, v224, v225\n\t"
      "v_cvt_pk_bf16_f32 v53, v226, v227\n\t"
      "v_cvt_pk_bf16_f32 v54, v228, v229\n\t"
      "v_cvt_pk_bf16_f32 v55, v230, v231\n\t"
      "ds_write_b128 v165, v[52:55] offset:49152\n\t"
      "v_dot2_f32_bf16 v224, v84, v28, v222\n\t"
      "v_dot2_f32_bf16 v225, v84, v29, v222\n\t"
      "v_dot2_f32_bf16 v226, v85, v28, v222\n\t"
      "v_dot2_f32_bf16 v227, v85, v29, v222\n\t"
      "v_dot2_f32_bf16 v228, v86, v28, v222\n\t"
      "v_dot2_f32_bf16 v229, v86, v29, v222\n\t"
      "v_dot2_f32_bf16 v230, v87, v28, v222\n\t"
      "v_dot2_f32_bf16 v231, v87, v29, v222\n\t"
      "v_cvt_pk_bf16_f32 v84, v224, v225\n\t"
      "v_cvt_pk_bf16_f32 v85, v226, v227\n\t"
      "v_cvt_pk_bf16_f32 v86, v228, v229\n\t"
      "v_cvt_pk_bf16_f32 v87, v230, v231\n\t"
      "v_dot2_f32_bf16 v224, v92, v28, v222\n\t"
      "v_dot2_f32_bf16 v225, v92, v29, v222\n\t"
      "v_dot2_f32_bf16 v226, v93, v28, v222\n\t"
      "v_dot2_f32_bf16 v227, v93, v29, v222\n\t"
      "v_dot2_f32_bf16 v228, v94, v28, v222\n\t"
      "v_dot2_f32_bf16 v229, v94, v29, v222\n\t"
      "v_dot2_f32_bf16 v230, v95, v28, v222\n\t"
      "v_dot2_f32_bf16 v231, v95, v29, v222\n\t"
      "v_cvt_pk_bf16_f32 v92, v224, v225\n\t"
      "v_cvt_pk_bf16_f32 v93, v226, v227\n\t"
      "v_cvt_pk_bf16_f32 v94, v228, v229\n\t"
      "v_cvt_pk_bf16_f32 v95, v230, v231\n\t"
      "v_dot2_f32_bf16 v224, v116, v28, v222\n\t"
      "v_dot2_f32_bf16 v225, v116, v29, v222\n\t"
      "v_dot2_f32_bf16 v226, v117, v28, v222\n\t"
      "v_dot2_f32_bf16 v227, v117, v29, v222\n\t"
      "v_dot2_f32_bf16 v228, v118, v28, v222\n\t"
      "v_dot2_f32_bf16 v229, v118, v29, v222\n\t"
      "v_dot2_f32_bf16 v230, v119, v28, v222\n\t"
      "v_dot2_f32_bf16 v231, v119, v29, v222\n\t"
      "v_cvt_pk_bf16_f32 v116, v224, v225\n\t"
      "v_cvt_pk_bf16_f32 v117, v226, v227\n\t"
      "v_cvt_pk_bf16_f32 v118, v228, v229\n\t"
      "v_cvt_pk_bf16_f32 v119, v230, v231\n\t"
      "v_dot2_f32_bf16 v224, v124, v28, v222\n\t"
      "v_dot2_f32_bf16 v225, v124, v29, v222\n\t"
      "v_dot2_f32_bf16 v226, v125, v28, v222\n\t"
      "v_dot2_f32_bf16 v227, v125, v29, v222\n\t"
      "v_dot2_f32_bf16 v228, v126, v28, v222\n\t"
      "v_dot2_f32_bf16 v229, v126, v29, v222\n\t"
      "v_dot2_f32_bf16 v230, v127, v28, v222\n\t"
      "v_dot2_f32_bf16 v231, v127, v29, v222\n\t"
      "v_cvt_pk_bf16_f32 v124, v224, v225\n\t"
      "v_cvt_pk_bf16_f32 v125, v226, v227\n\t"
      "v_cvt_pk_bf16_f32 v126, v228, v229\n\t"
      "v_cvt_pk_bf16_f32 v127, v230, v231\n\t"
      "s_add_u32 m0, s50, 12288\n\t"
      "s_add_u32 s55, s48, 331776\n\t"
      "buffer_load_dwordx4 v234, s[40:43], s55 offen lds\n\t"
      "s_mov_b32 m0, s50\n\t"
      "s_add_u32 s50, s50, 1024\n\t"
      "buffer_load_dwordx4 v234, s[40:43], s48 offen lds\n\t"
      "s_add_u32 s48, s48, s49\n\t"
      "s_add_u32 m0, s50, 12288\n\t"
      "s_add_u32 s55, s48, 331776\n\t"
      "buffer_load_dwordx4 v234, s[40:43], s55 offen lds\n\t"
      "s_mov_b32 m0, s50\n\t"
      "s_add_u32 s50, s50, 1024\n\t"
      "buffer_load_dwordx4 v234, s[40:43], s48 offen lds\n\t"
      "s_add_u32 s48, s48, s49\n\t"
      "s_add_u32 m0, s50, 12288\n\t"
      "s_add_u32 s55, s48, 331776\n\t"
      "buffer_load_dwordx4 v234, s[40:43], s55 offen lds\n\t"
      "s_mov_b32 m0, s50\n\t"
      "s_add_u32 s50, s50, 1024\n\t"
      "buffer_load_dwordx4 v234, s[40:43], s48 offen lds\n\t"
      "s_add_u32 s48, s48, s49\n\t"
      "s_mul_i32 s54, s49, 21\n\t"
      "s_add_u32 s48, s48, s54\n\t"
      "s_add_u32 s50, s50, 21504\n\t"
      "s_cmp_ge_u32 s50, 147456\n\t"
      "s_cselect_b32 s50, s38, s50\n\t"
      "s_add_u32 m0, s50, 12288\n\t"
      "s_add_u32 s55, s48, 331776\n\t"
      "buffer_load_dwordx4 v234, s[40:43], s55 offen lds\n\t"
      "s_mov_b32 m0, s50\n\t"
      "s_add_u32 s50, s50, 1024\n\t"
      "buffer_load_dwordx4 v234, s[40:43], s48 offen lds\n\t"
      "s_add_u32 s48, s48, s49\n\t"
      "s_add_u32 m0, s50, 12288\n\t"
      "s_add_u32 s55, s48, 331776\n\t"
      "buffer_load_dwordx4 v234, s[40:43], s55 offen lds\n\t"
      "s_mov_b32 m0, s50\n\t"
      "s_add_u32 s50, s50, 1024\n\t"
      "buffer_load_dwordx4 v234, s[40:43], s48 offen lds\n\t"
      "s_add_u32 s48, s48, s49\n\t"
      "s_add_u32 m0, s50, 12288\n\t"
      "s_add_u32 s55, s48, 331776\n\t"
      "buffer_load_dwordx4 v234, s[40:43], s55 offen lds\n\t"
      "s_mov_b32 m0, s50\n\t"
      "s_add_u32 s50, s50, 1024\n\t"
      "buffer_load_dwordx4 v234, s[40:43], s48 offen lds\n\t"
      "s_add_u32 s48, s48, s49\n\t"
      "s_mul_i32 s54, s49, 21\n\t"
      "s_add_u32 s48, s48, s54\n\t"
      "s_add_u32 s50, s50, 21504\n\t"
      "s_cmp_ge_u32 s50, 147456\n\t"
      "s_cselect_b32 s50, s38, s50\n\t"
      "s_mov_b64 exec, 0x3ff\n\t"
      "buffer_load_dword v239, v239, s[92:95], s52 offen\n\t"
      "s_mov_b64 exec, -1\n\t"
      "v_and_b32 v172, 0x70, v240\n\t"
      "v_lshl_add_u32 v172, v172, 3, v241\n\t"
      "s_and_b32 s54, s62, 255\n\t"
      "s_bfe_u32 s55, s62, 0x80008\n\t"
      "s_sub_u32 s54, s55, s54\n\t"
      "s_lshl_b32 s54, s54, 6\n\t"
      "s_cmp_ge_u32 s55, 144\n\t"
      "s_cselect_b32 s54, s54, 0\n\t"
      "s_mov_b32 exec_lo, 0xaaaaaaaa\n\t"
      "s_mov_b32 exec_hi, 0xa\n\t"
      "v_add_u32 v171, s54, v171\n\t"
      "s_mov_b32 exec_lo, -1\n\t"
      "s_mov_b32 exec_hi, 0xf\n\t"
      "buffer_load_dword v251, v171, s[40:43], s51 offen\n\t"
      "s_mov_b32 s92, s72\n\t"
      "s_mov_b32 s93, s73\n\t"
      "s_mov_b32 s94, 5308416\n\t"
      "s_mov_b32 exec_lo, 0x1f1f1f1f\n\t"
      "s_mov_b32 exec_hi, 0x1f1f1f1f\n\t"
      "s_add_u32 s54, s56, 73728\n\t"
      "buffer_load_dword v251, v172, s[92:95], s56 offen\n\t"
      "buffer_load_dword v251, v172, s[92:95], s54 offen\n\t"
      "s_mov_b64 exec, 0x1f1f\n\t"
      "s_add_u32 s54, s56, 147456\n\t"
      "buffer_load_dword v251, v172, s[92:95], s54 offen\n\t"
      "s_mov_b64 exec, -1\n\t"
      "s_branch Ljoin%=\n\t"
      "Lyoung%=:\n\t"
      "s_waitcnt vmcnt(8)\n\t"
      "v_dot2c_f32_bf16 v217, s53, v4\n\t"
      "v_dot2c_f32_bf16 v219, v4, v4\n\t"
      "v_dot2c_f32_bf16 v217, s53, v5\n\t"
      "v_dot2c_f32_bf16 v219, v5, v5\n\t"
      "v_dot2c_f32_bf16 v217, s53, v6\n\t"
      "v_dot2c_f32_bf16 v219, v6, v6\n\t"
      "v_dot2c_f32_bf16 v217, s53, v7\n\t"
      "v_dot2c_f32_bf16 v219, v7, v7\n\t"
      "s_waitcnt vmcnt(7)\n\t"
      "v_dot2c_f32_bf16 v217, s53, v12\n\t"
      "v_dot2c_f32_bf16 v219, v12, v12\n\t"
      "v_dot2c_f32_bf16 v217, s53, v13\n\t"
      "v_dot2c_f32_bf16 v219, v13, v13\n\t"
      "v_dot2c_f32_bf16 v217, s53, v14\n\t"
      "v_dot2c_f32_bf16 v219, v14, v14\n\t"
      "v_dot2c_f32_bf16 v217, s53, v15\n\t"
      "v_dot2c_f32_bf16 v219, v15, v15\n\t"
      "s_waitcnt vmcnt(6)\n\t"
      "v_dot2c_f32_bf16 v217, s53, v36\n\t"
      "v_dot2c_f32_bf16 v219, v36, v36\n\t"
      "v_dot2c_f32_bf16 v217, s53, v37\n\t"
      "v_dot2c_f32_bf16 v219, v37, v37\n\t"
      "v_dot2c_f32_bf16 v217, s53, v38\n\t"
      "v_dot2c_f32_bf16 v219, v38, v38\n\t"
      "v_dot2c_f32_bf16 v217, s53, v39\n\t"
      "v_dot2c_f32_bf16 v219, v39, v39\n\t"
      "s_waitcnt vmcnt(5)\n\t"
      "v_dot2c_f32_bf16 v217, s53, v44\n\t"
      "v_dot2c_f32_bf16 v219, v44, v44\n\t"
      "v_dot2c_f32_bf16 v217, s53, v45\n\t"
      "v_dot2c_f32_bf16 v219, v45, v45\n\t"
      "v_dot2c_f32_bf16 v217, s53, v46\n\t"
      "v_dot2c_f32_bf16 v219, v46, v46\n\t"
      "v_dot2c_f32_bf16 v217, s53, v47\n\t"
      "v_dot2c_f32_bf16 v219, v47, v47\n\t"
      "s_waitcnt vmcnt(4)\n\t"
      "v_dot2c_f32_bf16 v217, s53, v52\n\t"
      "v_dot2c_f32_bf16 v219, v52, v52\n\t"
      "v_dot2c_f32_bf16 v217, s53, v53\n\t"
      "v_dot2c_f32_bf16 v219, v53, v53\n\t"
      "v_dot2c_f32_bf16 v217, s53, v54\n\t"
      "v_dot2c_f32_bf16 v219, v54, v54\n\t"
      "v_dot2c_f32_bf16 v217, s53, v55\n\t"
      "v_dot2c_f32_bf16 v219, v55, v55\n\t"
      "s_waitcnt vmcnt(3)\n\t"
      "v_dot2c_f32_bf16 v217, s53, v84\n\t"
      "v_dot2c_f32_bf16 v219, v84, v84\n\t"
      "v_dot2c_f32_bf16 v217, s53, v85\n\t"
      "v_dot2c_f32_bf16 v219, v85, v85\n\t"
      "v_dot2c_f32_bf16 v217, s53, v86\n\t"
      "v_dot2c_f32_bf16 v219, v86, v86\n\t"
      "v_dot2c_f32_bf16 v217, s53, v87\n\t"
      "v_dot2c_f32_bf16 v219, v87, v87\n\t"
      "s_waitcnt vmcnt(2)\n\t"
      "v_dot2c_f32_bf16 v217, s53, v92\n\t"
      "v_dot2c_f32_bf16 v219, v92, v92\n\t"
      "v_dot2c_f32_bf16 v217, s53, v93\n\t"
      "v_dot2c_f32_bf16 v219, v93, v93\n\t"
      "v_dot2c_f32_bf16 v217, s53, v94\n\t"
      "v_dot2c_f32_bf16 v219, v94, v94\n\t"
      "v_dot2c_f32_bf16 v217, s53, v95\n\t"
      "v_dot2c_f32_bf16 v219, v95, v95\n\t"
      "s_waitcnt vmcnt(1)\n\t"
      "v_dot2c_f32_bf16 v217, s53, v116\n\t"
      "v_dot2c_f32_bf16 v219, v116, v116\n\t"
      "v_dot2c_f32_bf16 v217, s53, v117\n\t"
      "v_dot2c_f32_bf16 v219, v117, v117\n\t"
      "v_dot2c_f32_bf16 v217, s53, v118\n\t"
      "v_dot2c_f32_bf16 v219, v118, v118\n\t"
      "v_dot2c_f32_bf16 v217, s53, v119\n\t"
      "v_dot2c_f32_bf16 v219, v119, v119\n\t"
      "s_waitcnt vmcnt(0)\n\t"
      "v_dot2c_f32_bf16 v217, s53, v124\n\t"
      "v_dot2c_f32_bf16 v219, v124, v124\n\t"
      "v_dot2c_f32_bf16 v217, s53, v125\n\t"
      "v_dot2c_f32_bf16 v219, v125, v125\n\t"
      "v_dot2c_f32_bf16 v217, s53, v126\n\t"
      "v_dot2c_f32_bf16 v219, v126, v126\n\t"
      "v_dot2c_f32_bf16 v217, s53, v127\n\t"
      "v_dot2c_f32_bf16 v219, v127, v127\n\t"
      "s_nop 2\n\t"
      "v_add_f32_dpp v217, v217, v217 quad_perm:[1,0,3,2] row_mask:0xf bank_mask:0xf\n\t"
      "v_add_f32_dpp v219, v219, v219 quad_perm:[1,0,3,2] row_mask:0xf bank_mask:0xf\n\t"
      "s_nop 0\n\t"
      "v_add_f32_dpp v217, v217, v217 quad_perm:[2,3,0,1] row_mask:0xf bank_mask:0xf\n\t"
      "v_add_f32_dpp v219, v219, v219 quad_perm:[2,3,0,1] row_mask:0xf bank_mask:0xf\n\t"
      "s_nop 0\n\t"
      "v_add_f32_dpp v217, v217, v217 row_half_mirror row_mask:0xf bank_mask:0xf\n\t"
      "v_add_f32_dpp v219, v219, v219 row_half_mirror row_mask:0xf bank_mask:0xf\n\t"
      "s_nop 0\n\t"
      "v_mul_f32 v217, 0x3ae38e39, v217\n\t"
      "v_cvt_pk_bf16_f32 v222, v217, v217\n\t"
      "v_and_b32 v222, 0xffff0000, v222\n\t"
      "v_xor_b32 v222, 0x80000000, v222\n\t"
      "v_dot2_f32_bf16 v224, v4, v28, v222\n\t"
      "v_dot2_f32_bf16 v225, v4, v29, v222\n\t"
      "v_dot2_f32_bf16 v226, v5, v28, v222\n\t"
      "v_dot2_f32_bf16 v227, v5, v29, v222\n\t"
      "v_dot2_f32_bf16 v228, v6, v28, v222\n\t"
      "v_dot2_f32_bf16 v229, v6, v29, v222\n\t"
      "v_dot2_f32_bf16 v230, v7, v28, v222\n\t"
      "v_dot2_f32_bf16 v231, v7, v29, v222\n\t"
      "v_cvt_pk_bf16_f32 v4, v224, v225\n\t"
      "v_cvt_pk_bf16_f32 v5, v226, v227\n\t"
      "v_cvt_pk_bf16_f32 v6, v228, v229\n\t"
      "v_cvt_pk_bf16_f32 v7, v230, v231\n\t"
      "ds_write_b128 v193, v[4:7]\n\t"
      "v_dot2_f32_bf16 v224, v12, v28, v222\n\t"
      "v_dot2_f32_bf16 v225, v12, v29, v222\n\t"
      "v_dot2_f32_bf16 v226, v13, v28, v222\n\t"
      "v_dot2_f32_bf16 v227, v13, v29, v222\n\t"
      "v_dot2_f32_bf16 v228, v14, v28, v222\n\t"
      "v_dot2_f32_bf16 v229, v14, v29, v222\n\t"
      "v_dot2_f32_bf16 v230, v15, v28, v222\n\t"
      "v_dot2_f32_bf16 v231, v15, v29, v222\n\t"
      "v_cvt_pk_bf16_f32 v12, v224, v225\n\t"
      "v_cvt_pk_bf16_f32 v13, v226, v227\n\t"
      "v_cvt_pk_bf16_f32 v14, v228, v229\n\t"
      "v_cvt_pk_bf16_f32 v15, v230, v231\n\t"
      "ds_write_b128 v165, v[12:15] offset:0\n\t"
      "v_dot2_f32_bf16 v224, v36, v28, v222\n\t"
      "v_dot2_f32_bf16 v225, v36, v29, v222\n\t"
      "v_dot2_f32_bf16 v226, v37, v28, v222\n\t"
      "v_dot2_f32_bf16 v227, v37, v29, v222\n\t"
      "v_dot2_f32_bf16 v228, v38, v28, v222\n\t"
      "v_dot2_f32_bf16 v229, v38, v29, v222\n\t"
      "v_dot2_f32_bf16 v230, v39, v28, v222\n\t"
      "v_dot2_f32_bf16 v231, v39, v29, v222\n\t"
      "v_cvt_pk_bf16_f32 v36, v224, v225\n\t"
      "v_cvt_pk_bf16_f32 v37, v226, v227\n\t"
      "v_cvt_pk_bf16_f32 v38, v228, v229\n\t"
      "v_cvt_pk_bf16_f32 v39, v230, v231\n\t"
      "ds_write_b128 v165, v[36:39] offset:16384\n\t"
      "v_dot2_f32_bf16 v224, v44, v28, v222\n\t"
      "v_dot2_f32_bf16 v225, v44, v29, v222\n\t"
      "v_dot2_f32_bf16 v226, v45, v28, v222\n\t"
      "v_dot2_f32_bf16 v227, v45, v29, v222\n\t"
      "v_dot2_f32_bf16 v228, v46, v28, v222\n\t"
      "v_dot2_f32_bf16 v229, v46, v29, v222\n\t"
      "v_dot2_f32_bf16 v230, v47, v28, v222\n\t"
      "v_dot2_f32_bf16 v231, v47, v29, v222\n\t"
      "v_cvt_pk_bf16_f32 v44, v224, v225\n\t"
      "v_cvt_pk_bf16_f32 v45, v226, v227\n\t"
      "v_cvt_pk_bf16_f32 v46, v228, v229\n\t"
      "v_cvt_pk_bf16_f32 v47, v230, v231\n\t"
      "ds_write_b128 v165, v[44:47] offset:32768\n\t"
      "v_dot2_f32_bf16 v224, v52, v28, v222\n\t"
      "v_dot2_f32_bf16 v225, v52, v29, v222\n\t"
      "v_dot2_f32_bf16 v226, v53, v28, v222\n\t"
      "v_dot2_f32_bf16 v227, v53, v29, v222\n\t"
      "v_dot2_f32_bf16 v228, v54, v28, v222\n\t"
      "v_dot2_f32_bf16 v229, v54, v29, v222\n\t"
      "v_dot2_f32_bf16 v230, v55, v28, v222\n\t"
      "v_dot2_f32_bf16 v231, v55, v29, v222\n\t"
      "v_cvt_pk_bf16_f32 v52, v224, v225\n\t"
      "v_cvt_pk_bf16_f32 v53, v226, v227\n\t"
      "v_cvt_pk_bf16_f32 v54, v228, v229\n\t"
      "v_cvt_pk_bf16_f32 v55, v230, v231\n\t"
      "ds_write_b128 v165, v[52:55] offset:49152\n\t"
      "v_dot2_f32_bf16 v224, v84, v28, v222\n\t"
      "v_dot2_f32_bf16 v225, v84, v29, v222\n\t"
      "v_dot2_f32_bf16 v226, v85, v28, v222\n\t"
      "v_dot2_f32_bf16 v227, v85, v29, v222\n\t"
      "v_dot2_f32_bf16 v228, v86, v28, v222\n\t"
      "v_dot2_f32_bf16 v229, v86, v29, v222\n\t"
      "v_dot2_f32_bf16 v230, v87, v28, v222\n\t"
      "v_dot2_f32_bf16 v231, v87, v29, v222\n\t"
      "v_cvt_pk_bf16_f32 v84, v224, v225\n\t"
      "v_cvt_pk_bf16_f32 v85, v226, v227\n\t"
      "v_cvt_pk_bf16_f32 v86, v228, v229\n\t"
      "v_cvt_pk_bf16_f32 v87, v230, v231\n\t"
      "v_dot2_f32_bf16 v224, v92, v28, v222\n\t"
      "v_dot2_f32_bf16 v225, v92, v29, v222\n\t"
      "v_dot2_f32_bf16 v226, v93, v28, v222\n\t"
      "v_dot2_f32_bf16 v227, v93, v29, v222\n\t"
      "v_dot2_f32_bf16 v228, v94, v28, v222\n\t"
      "v_dot2_f32_bf16 v229, v94, v29, v222\n\t"
      "v_dot2_f32_bf16 v230, v95, v28, v222\n\t"
      "v_dot2_f32_bf16 v231, v95, v29, v222\n\t"
      "v_cvt_pk_bf16_f32 v92, v224, v225\n\t"
      "v_cvt_pk_bf16_f32 v93, v226, v227\n\t"
      "v_cvt_pk_bf16_f32 v94, v228, v229\n\t"
      "v_cvt_pk_bf16_f32 v95, v230, v231\n\t"
      "v_dot2_f32_bf16 v224, v116, v28, v222\n\t"
      "v_dot2_f32_bf16 v225, v116, v29, v222\n\t"
      "v_dot2_f32_bf16 v226, v117, v28, v222\n\t"
      "v_dot2_f32_bf16 v227, v117, v29, v222\n\t"
      "v_dot2_f32_bf16 v228, v118, v28, v222\n\t"
      "v_dot2_f32_bf16 v229, v118, v29, v222\n\t"
      "v_dot2_f32_bf16 v230, v119, v28, v222\n\t"
      "v_dot2_f32_bf16 v231, v119, v29, v222\n\t"
      "v_cvt_pk_bf16_f32 v116, v224, v225\n\t"
      "v_cvt_pk_bf16_f32 v117, v226, v227\n\t"
      "v_cvt_pk_bf16_f32 v118, v228, v229\n\t"
      "v_cvt_pk_bf16_f32 v119, v230, v231\n\t"
      "v_dot2_f32_bf16 v224, v124, v28, v222\n\t"
      "v_dot2_f32_bf16 v225, v124, v29, v222\n\t"
      "v_dot2_f32_bf16 v226, v125, v28, v222\n\t"
      "v_dot2_f32_bf16 v227, v125, v29, v222\n\t"
      "v_dot2_f32_bf16 v228, v126, v28, v222\n\t"
      "v_dot2_f32_bf16 v229, v126, v29, v222\n\t"
      "v_dot2_f32_bf16 v230, v127, v28, v222\n\t"
      "v_dot2_f32_bf16 v231, v127, v29, v222\n\t"
      "v_cvt_pk_bf16_f32 v124, v224, v225\n\t"
      "v_cvt_pk_bf16_f32 v125, v226, v227\n\t"
      "v_cvt_pk_bf16_f32 v126, v228, v229\n\t"
      "v_cvt_pk_bf16_f32 v127, v230, v231\n\t"
      "s_and_b32 s54, s62, 255\n\t"
      "s_and_b32 s55, s64, 255\n\t"
      "s_lshr_b64 s[62:63], s[62:63], 8\n\t"
      "s_lshr_b64 s[64:65], s[64:65], 8\n\t"
      "s_cmp_eq_u32 s55, 255\n\t"
      "s_cselect_b32 s55, 0x40000000, 64\n\t"
      "s_cmp_lt_u32 s54, 144\n\t"
      "s_cselect_b32 s40, s72, s70\n\t"
      "s_cselect_b32 s41, s73, s71\n\t"
      "s_cselect_b32 s42, 0x510000, s66\n\t"
      "s_cselect_b32 s49, 73728, s67\n\t"
      "s_cselect_b32 s48, s36, s37\n\t"
      "s_cselect_b64 vcc, -1, 0\n\t"
      "v_cndmask_b32 v234, v242, v241, vcc\n\t"
      "s_cmp_eq_u32 s54, 255\n\t"
      "s_cselect_b32 s42, 0, s42\n\t"
      "s_lshl_b32 s54, s54, 6\n\t"
      "s_add_u32 s48, s48, s54\n\t"
      "v_and_b32 v223, s55, v243\n\t"
      "v_add_u32 v234, v234, v223\n\t"
      "s_mov_b32 m0, s50\n\t"
      "s_add_u32 s50, s50, 1024\n\t"
      "s_add_u32 s48, s48, s49\n\t"
      "s_mov_b32 m0, s50\n\t"
      "s_add_u32 s50, s50, 1024\n\t"
      "s_add_u32 s48, s48, s49\n\t"
      "s_mov_b32 m0, s50\n\t"
      "s_add_u32 s50, s50, 1024\n\t"
      "s_add_u32 s48, s48, s49\n\t"
      "s_mul_i32 s54, s49, 21\n\t"
      "s_add_u32 s48, s48, s54\n\t"
      "s_add_u32 s50, s50, 21504\n\t"
      "s_cmp_ge_u32 s50, 147456\n\t"
      "s_cselect_b32 s50, s38, s50\n\t"
      "s_mov_b32 m0, s50\n\t"
      "s_add_u32 s50, s50, 1024\n\t"
      "s_add_u32 s48, s48, s49\n\t"
      "s_mov_b32 m0, s50\n\t"
      "s_add_u32 s50, s50, 1024\n\t"
      "s_add_u32 s48, s48, s49\n\t"
      "s_mov_b32 m0, s50\n\t"
      "s_add_u32 s50, s50, 1024\n\t"
      "s_add_u32 s48, s48, s49\n\t"
      "s_mul_i32 s54, s49, 21\n\t"
      "s_add_u32 s48, s48, s54\n\t"
      "s_add_u32 s50, s50, 21504\n\t"
      "s_cmp_ge_u32 s50, 147456\n\t"
      "s_cselect_b32 s50, s38, s50\n\t"
      "s_mov_b32 m0, s50\n\t"
      "s_add_u32 s50, s50, 1024\n\t"
      "s_add_u32 s48, s48, s49\n\t"
      "s_mov_b32 m0, s50\n\t"
      "s_add_u32 s50, s50, 1024\n\t"
      "s_add_u32 s48, s48, s49\n\t"
      "s_mov_b32 m0, s50\n\t"
      "s_add_u32 s50, s50, 1024\n\t"
      "s_add_u32 s48, s48, s49\n\t"
      "s_mul_i32 s54, s49, 21\n\t"
      "s_add_u32 s48, s48, s54\n\t"
      "s_add_u32 s50, s50, 21504\n\t"
      "s_cmp_ge_u32 s50, 147456\n\t"
      "s_cselect_b32 s50, s38, s50\n\t"
      "s_setprio 0\n\t"
      "Ljoin%=:\n\t"
      "s_waitcnt vmcnt(17)\n\t"
      "s_waitcnt lgkmcnt(0)\n\t"
      "s_barrier\n\t"
      "s_mov_b32 s53, 0xa2000\n\t"
      "s_mov_b32 s51, s8\n\t"
      "s_lshl_b32 s54, s51, 8\n\t"
      "v_add_u32 v235, s54, v245\n\t"
      "ds_read_b128 v[0:3], v194\n\t"
      "ds_read_b128 v[4:7], v195\n\t"
      "ds_read_b128 v[8:11], v196\n\t"
      "ds_read_b128 v[12:15], v197\n\t"
      "ds_read_b64_tr_b16 v[176:177], v232 offset:0\n\t"
      "ds_read_b64_tr_b16 v[178:179], v232 offset:512\n\t"
      "ds_read_b64_tr_b16 v[180:181], v232 offset:2048\n\t"
      "ds_read_b64_tr_b16 v[182:183], v232 offset:2560\n\t"
      "ds_read_b64_tr_b16 v[184:185], v232 offset:4096\n\t"
      "ds_read_b64_tr_b16 v[186:187], v232 offset:4608\n\t"
      "ds_read_b64_tr_b16 v[188:189], v232 offset:6144\n\t"
      "ds_read_b64_tr_b16 v[190:191], v232 offset:6656\n\t"
      "ds_read_b64_tr_b16 v[192:193], v232 offset:8192\n\t"
      "ds_read_b64_tr_b16 v[194:195], v232 offset:8704\n\t"
      "s_waitcnt lgkmcnt(8)\n\t"
      "v_mfma_f32_32x32x16_bf16 v[144:159], v[176:179], v[0:3], 0\n\t"
      "ds_read_b64_tr_b16 v[196:197], v232 offset:10240\n\t"
      "ds_read_b64_tr_b16 v[198:199], v232 offset:10752\n\t"
      "ds_read_b128 v[16:19], v160 offset:0\n\t"
      "ds_read_b128 v[20:23], v161 offset:0\n\t"
      "ds_read_b128 v[24:27], v162 offset:0\n\t"
      "ds_read_b128 v[28:31], v163 offset:0\n\t"
      "s_waitcnt lgkmcnt(12)\n\t"
      "v_mfma_f32_32x32x16_bf16 v[144:159], v[180:183], v[4:7], v[144:159]\n\t"
      "ds_read_b64_tr_b16 v[176:177], v232 offset:12288\n\t"
      "ds_read_b64_tr_b16 v[178:179], v232 offset:12800\n\t"
      "ds_read_b128 v[32:35], v160 offset:16384\n\t"
      "s_waitcnt lgkmcnt(13)\n\t"
      "ds_read_b128 v[36:39], v161 offset:16384\n\t"
      "ds_read_b128 v[40:43], v162 offset:16384\n\t"
      "s_waitcnt lgkmcnt(13)\n\t"
      "ds_read_b128 v[44:47], v163 offset:16384\n\t"
      "v_mfma_f32_32x32x16_bf16 v[144:159], v[184:187], v[8:11], v[144:159]\n\t"
      "ds_read_b64_tr_b16 v[180:181], v232 offset:14336\n\t"
      "s_waitcnt lgkmcnt(13)\n\t"
      "ds_read_b64_tr_b16 v[182:183], v232 offset:14848\n\t"
      "ds_read_b128 v[48:51], v160 offset:32768\n\t"
      "s_waitcnt lgkmcnt(13)\n\t"
      "ds_read_b128 v[52:55], v161 offset:32768\n\t"
      "ds_read_b128 v[56:59], v162 offset:32768\n\t"
      "s_waitcnt lgkmcnt(13)\n\t"
      "ds_read_b128 v[60:63], v163 offset:32768\n\t"
      "v_mfma_f32_32x32x16_bf16 v[144:159], v[188:191], v[12:15], v[144:159]\n\t"
      "ds_read_b64_tr_b16 v[184:185], v232 offset:16384\n\t"
      "s_waitcnt lgkmcnt(13)\n\t"
      "ds_read_b64_tr_b16 v[186:187], v232 offset:16896\n\t"
      "ds_read_b128 v[64:67], v160 offset:49152\n\t"
      "s_waitcnt lgkmcnt(13)\n\t"
      "ds_read_b128 v[68:71], v161 offset:49152\n\t"
      "ds_read_b128 v[72:75], v162 offset:49152\n\t"
      "s_waitcnt lgkmcnt(13)\n\t"
      "ds_read_b128 v[76:79], v163 offset:49152\n\t"
      "v_mfma_f32_32x32x16_bf16 v[144:159], v[192:195], v[16:19], v[144:159]\n\t"
      "ds_read_b64_tr_b16 v[188:189], v232 offset:18432\n\t"
      "s_waitcnt lgkmcnt(13)\n\t"
      "ds_read_b64_tr_b16 v[190:191], v232 offset:18944\n\t"
      "v_mul_f32 v216, v216, v216\n\t"
      "v_mul_f32 v218, 0x3ae38e39, v218\n\t"
      "v_sub_f32 v218, v218, v216\n\t"
      "v_max_f32 v218, 0, v218\n\t"
      "v_cvt_pk_bf16_f32 v218, v218, v218\n\t"
      "v_and_b32 v218, s59, v218\n\t"
      "v_add_f32 v218, 0x3727c5ac, v218\n\t"
      "v_cvt_pk_bf16_f32 v218, v218, v218\n\t"
      "v_and_b32 v218, s59, v218\n\t"
      "v_sqrt_f32 v218, v218\n\t"
      "v_mfma_f32_32x32x16_bf16 v[144:159], v[196:199], v[20:23], v[144:159]\n\t"
      "ds_read_b64_tr_b16 v[192:193], v232 offset:20480\n\t"
      "s_waitcnt lgkmcnt(13)\n\t"
      "ds_read_b64_tr_b16 v[194:195], v232 offset:20992\n\t"
      "s_nop 0\n\t"
      "v_cvt_pk_bf16_f32 v218, v218, v218\n\t"
      "v_and_b32 v218, s59, v218\n\t"
      "v_rcp_f32 v218, v218\n\t"
      "s_nop 0\n\t"
      "ds_write_b32 v167, v218 offset:0\n\t"
      "v_mul_f32 v217, v217, v217\n\t"
      "v_mul_f32 v219, 0x3ae38e39, v219\n\t"
      "v_sub_f32 v219, v219, v217\n\t"
      "v_max_f32 v219, 0, v219\n\t"
      "v_cvt_pk_bf16_f32 v219, v219, v219\n\t"
      "v_mfma_f32_32x32x16_bf16 v[144:159], v[176:179], v[24:27], v[144:159]\n\t"
      "s_waitcnt lgkmcnt(13)\n\t"
      "ds_read_b64_tr_b16 v[196:197], v232 offset:22528\n\t"
      "ds_read_b64_tr_b16 v[198:199], v232 offset:23040\n\t"
      "v_and_b32 v219, s59, v219\n\t"
      "v_add_f32 v219, 0x3727c5ac, v219\n\t"
      "v_cvt_pk_bf16_f32 v219, v219, v219\n\t"
      "v_and_b32 v219, s59, v219\n\t"
      "v_sqrt_f32 v219, v219\n\t"
      "s_nop 0\n\t"
      "v_cvt_pk_bf16_f32 v219, v219, v219\n\t"
      "v_and_b32 v219, s59, v219\n\t"
      "v_rcp_f32 v219, v219\n\t"
      "s_nop 0\n\t"
      "s_waitcnt lgkmcnt(13)\n\t"
      "ds_write_b32 v167, v219 offset:32\n\t"
      "s_waitcnt vmcnt(11)\n\t"
      "s_waitcnt lgkmcnt(0)\n\t"
      "s_barrier\n\t"
      "v_mfma_f32_32x32x16_bf16 v[144:159], v[180:183], v[28:31], v[144:159]\n\t"
      "ds_read_b64_tr_b16 v[176:177], v232 offset:24576\n\t"
      "ds_read_b64_tr_b16 v[178:179], v232 offset:25088\n\t"
      "ds_write_b128 v164, v[80:83] offset:0\n\t"
      "ds_write_b128 v165, v[84:87] offset:0\n\t"
      "ds_write_b128 v164, v[88:91] offset:16384\n\t"
      "v_mfma_f32_32x32x16_bf16 v[144:159], v[184:187], v[32:35], v[144:159]\n\t"
      "ds_read_b64_tr_b16 v[180:181], v232 offset:26624\n\t"
      "ds_read_b64_tr_b16 v[182:183], v232 offset:27136\n\t"
      "ds_write_b128 v165, v[92:95] offset:16384\n\t"
      "ds_write_b128 v164, v[112:115] offset:32768\n\t"
      "ds_write_b128 v165, v[116:119] offset:32768\n\t"
      "v_mfma_f32_32x32x16_bf16 v[144:159], v[188:191], v[36:39], v[144:159]\n\t"
      "ds_read_b64_tr_b16 v[184:185], v232 offset:28672\n\t"
      "ds_read_b64_tr_b16 v[186:187], v232 offset:29184\n\t"
      "ds_write_b128 v164, v[120:123] offset:49152\n\t"
      "ds_write_b128 v165, v[124:127] offset:49152\n\t"
      "v_mfma_f32_32x32x16_bf16 v[144:159], v[192:195], v[40:43], v[144:159]\n\t"
      "ds_read_b64_tr_b16 v[188:189], v232 offset:30720\n\t"
      "s_waitcnt lgkmcnt(13)\n\t"
      "ds_read_b64_tr_b16 v[190:191], v232 offset:31232\n\t"
      "s_waitcnt lgkmcnt(0)\n\t"
      "s_barrier\n\t"
      "v_mfma_f32_32x32x16_bf16 v[144:159], v[196:199], v[44:47], v[144:159]\n\t"
      "ds_read_b64_tr_b16 v[192:193], v232 offset:32768\n\t"
      "ds_read_b64_tr_b16 v[194:195], v232 offset:33280\n\t"
      "ds_read_b128 v[80:83], v160 offset:0\n\t"
      "ds_read_b128 v[84:87], v161 offset:0\n\t"
      "ds_read_b128 v[88:91], v162 offset:0\n\t"
      "ds_read_b128 v[92:95], v163 offset:0\n\t"
      "v_mfma_f32_32x32x16_bf16 v[144:159], v[176:179], v[48:51], v[144:159]\n\t"
      "ds_read_b64_tr_b16 v[196:197], v232 offset:34816\n\t"
      "ds_read_b64_tr_b16 v[198:199], v232 offset:35328\n\t"
      "ds_read_b128 v[96:99], v160 offset:16384\n\t"
      "ds_read_b128 v[100:103], v161 offset:16384\n\t"
      "ds_read_b128 v[104:107], v162 offset:16384\n\t"
      "ds_read_b128 v[108:111], v163 offset:16384\n\t"
      "ds_read_b128 v[112:115], v160 offset:32768\n\t"
      "v_mfma_f32_32x32x16_bf16 v[144:159], v[180:183], v[52:55], v[144:159]\n\t"
      "ds_read_b64_tr_b16 v[176:177], v232 offset:36864\n\t"
      "ds_read_b64_tr_b16 v[178:179], v232 offset:37376\n\t"
      "s_waitcnt lgkmcnt(13)\n\t"
      "ds_read_b128 v[116:119], v161 offset:32768\n\t"
      "ds_read_b128 v[120:123], v162 offset:32768\n\t"
      "s_waitcnt lgkmcnt(13)\n\t"
      "ds_read_b128 v[124:127], v163 offset:32768\n\t"
      "ds_read_b128 v[128:131], v160 offset:49152\n\t"
      "v_mfma_f32_32x32x16_bf16 v[144:159], v[184:187], v[56:59], v[144:159]\n\t"
      "s_waitcnt lgkmcnt(13)\n\t"
      "ds_read_b64_tr_b16 v[180:181], v232 offset:38912\n\t"
      "ds_read_b64_tr_b16 v[182:183], v232 offset:39424\n\t"
      "s_waitcnt lgkmcnt(13)\n\t"
      "ds_read_b128 v[132:135], v161 offset:49152\n\t"
      "ds_read_b128 v[136:139], v162 offset:49152\n\t"
      "s_waitcnt lgkmcnt(13)\n\t"
      "ds_read_b128 v[140:143], v163 offset:49152\n\t"
      "ds_read_b32 v237, v168\n\t"
      "v_mfma_f32_32x32x16_bf16 v[144:159], v[188:191], v[60:63], v[144:159]\n\t"
      "s_waitcnt lgkmcnt(13)\n\t"
      "ds_read_b64_tr_b16 v[184:185], v232 offset:40960\n\t"
      "ds_read_b64_tr_b16 v[186:187], v232 offset:41472\n\t"
      "buffer_load_dword v238, off, s[44:47], 0 sc1\n\t"
      "buffer_load_dwordx3 v[252:254], v240, s[88:91], s19 offen\n\t"
      "v_mfma_f32_32x32x16_bf16 v[144:159], v[192:195], v[64:67], v[144:159]\n\t"
      "s_waitcnt lgkmcnt(13)\n\t"
      "ds_read_b64_tr_b16 v[188:189], v232 offset:43008\n\t"
      "ds_read_b64_tr_b16 v[190:191], v232 offset:43520\n\t"
      "s_mov_b32 s88, s74\n\t"
      "s_mov_b32 s89, s75\n\t"
      "s_mov_b32 s90, 9216\n\t"
      "buffer_load_ushort v200, v169, s[88:91], s15 offen\n\t"
      "s_waitcnt vmcnt(8)\n\t"
      "s_waitcnt lgkmcnt(0)\n\t"
      "s_barrier\n\t"
      "v_mfma_f32_32x32x16_bf16 v[144:159], v[196:199], v[68:71], v[144:159]\n\t"
      "ds_read_b64_tr_b16 v[192:193], v232 offset:45056\n\t"
      "ds_read_b64_tr_b16 v[194:195], v232 offset:45568\n\t"
      "s_and_b32 s54, s62, 255\n\t"
      "s_and_b32 s55, s64, 255\n\t"
      "s_lshr_b64 s[62:63], s[62:63], 8\n\t"
      "s_lshr_b64 s[64:65], s[64:65], 8\n\t"
      "s_cmp_eq_u32 s55, 255\n\t"
      "s_cselect_b32 s55, 0x40000000, 64\n\t"
      "s_cmp_lt_u32 s54, 144\n\t"
      "s_cselect_b32 s40, s72, s70\n\t"
      "s_cselect_b32 s41, s73, s71\n\t"
      "s_cselect_b32 s42, 0x510000, s66\n\t"
      "s_cselect_b32 s49, 73728, s67\n\t"
      "s_cselect_b32 s48, s36, s37\n\t"
      "s_cselect_b64 vcc, -1, 0\n\t"
      "v_cndmask_b32 v234, v242, v241, vcc\n\t"
      "s_cmp_eq_u32 s54, 255\n\t"
      "s_cselect_b32 s42, 0, s42\n\t"
      "s_lshl_b32 s54, s54, 6\n\t"
      "s_add_u32 s48, s48, s54\n\t"
      "v_and_b32 v223, s55, v243\n\t"
      "v_add_u32 v234, v234, v223\n\t"
      "s_mov_b32 m0, s50\n\t"
      "s_add_u32 s50, s50, 1024\n\t"
      "buffer_load_dwordx4 v234, s[40:43], s48 offen lds\n\t"
      "s_add_u32 s48, s48, s49\n\t"
      "v_mfma_f32_32x32x16_bf16 v[144:159], v[176:179], v[72:75], v[144:159]\n\t"
      "ds_read_b64_tr_b16 v[196:197], v232 offset:47104\n\t"
      "ds_read_b64_tr_b16 v[198:199], v232 offset:47616\n\t"
      "s_mov_b32 m0, s50\n\t"
      "s_add_u32 s50, s50, 1024\n\t"
      "buffer_load_dwordx4 v234, s[40:43], s48 offen lds\n\t"
      "s_add_u32 s48, s48, s49\n\t"
      "v_mfma_f32_32x32x16_bf16 v[144:159], v[180:183], v[76:79], v[144:159]\n\t"
      "ds_read_b64_tr_b16 v[176:177], v236 offset:0\n\t"
      "ds_read_b64_tr_b16 v[178:179], v236 offset:512\n\t"
      "s_mov_b32 m0, s50\n\t"
      "s_add_u32 s50, s50, 1024\n\t"
      "buffer_load_dwordx4 v234, s[40:43], s48 offen lds\n\t"
      "s_add_u32 s48, s48, s49\n\t"
      "s_mul_i32 s54, s49, 21\n\t"
      "s_add_u32 s48, s48, s54\n\t"
      "s_add_u32 s50, s50, 21504\n\t"
      "s_cmp_ge_u32 s50, 147456\n\t"
      "s_cselect_b32 s50, s38, s50\n\t"
      "v_mfma_f32_32x32x16_bf16 v[144:159], v[184:187], v[80:83], v[144:159]\n\t"
      "ds_read_b64_tr_b16 v[180:181], v236 offset:2048\n\t"
      "ds_read_b64_tr_b16 v[182:183], v236 offset:2560\n\t"
      "v_mfma_f32_32x32x16_bf16 v[144:159], v[188:191], v[84:87], v[144:159]\n\t"
      "ds_read_b64_tr_b16 v[184:185], v236 offset:4096\n\t"
      "ds_read_b64_tr_b16 v[186:187], v236 offset:4608\n\t"
      "s_mov_b32 m0, s50\n\t"
      "s_add_u32 s50, s50, 1024\n\t"
      "buffer_load_dwordx4 v234, s[40:43], s48 offen lds\n\t"
      "s_add_u32 s48, s48, s49\n\t"
      "s_waitcnt lgkmcnt(8)\n\t"
      "v_mfma_f32_32x32x16_bf16 v[144:159], v[192:195], v[88:91], v[144:159]\n\t"
      "ds_read_b64_tr_b16 v[188:189], v236 offset:6144\n\t"
      "ds_read_b64_tr_b16 v[190:191], v236 offset:6656\n\t"
      "s_waitcnt lgkmcnt(8)\n\t"
      "v_mfma_f32_32x32x16_bf16 v[144:159], v[196:199], v[92:95], v[144:159]\n\t"
      "ds_read_b64_tr_b16 v[192:193], v236 offset:8192\n\t"
      "ds_read_b64_tr_b16 v[194:195], v236 offset:8704\n\t"
      "s_mov_b32 m0, s50\n\t"
      "s_add_u32 s50, s50, 1024\n\t"
      "buffer_load_dwordx4 v234, s[40:43], s48 offen lds\n\t"
      "s_add_u32 s48, s48, s49\n\t"
      "s_waitcnt lgkmcnt(8)\n\t"
      "v_mfma_f32_32x32x16_bf16 v[144:159], v[176:179], v[96:99], v[144:159]\n\t"
      "ds_read_b64_tr_b16 v[196:197], v236 offset:10240\n\t"
      "ds_read_b64_tr_b16 v[198:199], v236 offset:10752\n\t"
      "s_waitcnt lgkmcnt(8)\n\t"
      "v_mfma_f32_32x32x16_bf16 v[144:159], v[180:183], v[100:103], v[144:159]\n\t"
      "ds_read_b64_tr_b16 v[176:177], v236 offset:12288\n\t"
      "ds_read_b64_tr_b16 v[178:179], v236 offset:12800\n\t"
      "s_mov_b32 m0, s50\n\t"
      "s_add_u32 s50, s50, 1024\n\t"
      "buffer_load_dwordx4 v234, s[40:43], s48 offen lds\n\t"
      "s_add_u32 s48, s48, s49\n\t"
      "s_mul_i32 s54, s49, 21\n\t"
      "s_add_u32 s48, s48, s54\n\t"
      "s_add_u32 s50, s50, 21504\n\t"
      "s_cmp_ge_u32 s50, 147456\n\t"
      "s_cselect_b32 s50, s38, s50\n\t"
      "s_waitcnt lgkmcnt(8)\n\t"
      "v_mfma_f32_32x32x16_bf16 v[144:159], v[184:187], v[104:107], v[144:159]\n\t"
      "ds_read_b64_tr_b16 v[180:181], v236 offset:14336\n\t"
      "ds_read_b64_tr_b16 v[182:183], v236 offset:14848\n\t"
      "s_waitcnt lgkmcnt(8)\n\t"
      "v_mfma_f32_32x32x16_bf16 v[144:159], v[188:191], v[108:111], v[144:159]\n\t"
      "ds_read_b64_tr_b16 v[184:185], v236 offset:16384\n\t"
      "ds_read_b64_tr_b16 v[186:187], v236 offset:16896\n\t"
      "s_mov_b32 m0, s50\n\t"
      "s_add_u32 s50, s50, 1024\n\t"
      "buffer_load_dwordx4 v234, s[40:43], s48 offen lds\n\t"
      "s_add_u32 s48, s48, s49\n\t"
      "s_waitcnt lgkmcnt(8)\n\t"
      "v_mfma_f32_32x32x16_bf16 v[144:159], v[192:195], v[112:115], v[144:159]\n\t"
      "ds_read_b64_tr_b16 v[188:189], v236 offset:18432\n\t"
      "ds_read_b64_tr_b16 v[190:191], v236 offset:18944\n\t"
      "s_waitcnt lgkmcnt(8)\n\t"
      "v_mfma_f32_32x32x16_bf16 v[144:159], v[196:199], v[116:119], v[144:159]\n\t"
      "ds_read_b64_tr_b16 v[192:193], v236 offset:20480\n\t"
      "ds_read_b64_tr_b16 v[194:195], v236 offset:20992\n\t"
      "s_mov_b32 m0, s50\n\t"
      "s_add_u32 s50, s50, 1024\n\t"
      "buffer_load_dwordx4 v234, s[40:43], s48 offen lds\n\t"
      "s_add_u32 s48, s48, s49\n\t"
      "s_waitcnt lgkmcnt(8)\n\t"
      "v_mfma_f32_32x32x16_bf16 v[144:159], v[176:179], v[120:123], v[144:159]\n\t"
      "ds_read_b64_tr_b16 v[196:197], v236 offset:22528\n\t"
      "ds_read_b64_tr_b16 v[198:199], v236 offset:23040\n\t"
      "s_mov_b32 m0, s50\n\t"
      "s_add_u32 s50, s50, 1024\n\t"
      "buffer_load_dwordx4 v234, s[40:43], s48 offen lds\n\t"
      "s_add_u32 s48, s48, s49\n\t"
      "s_mul_i32 s54, s49, 21\n\t"
      "s_add_u32 s48, s48, s54\n\t"
      "s_add_u32 s50, s50, 21504\n\t"
      "s_cmp_ge_u32 s50, 147456\n\t"
      "s_cselect_b32 s50, s38, s50\n\t"
      "s_waitcnt vmcnt(6)\n\t"
      "s_barrier\n\t"
      "s_waitcnt lgkmcnt(8)\n\t"
      "v_mfma_f32_32x32x16_bf16 v[144:159], v[180:183], v[124:127], v[144:159]\n\t"
      "ds_read_b64_tr_b16 v[176:177], v233 offset:0\n\t"
      "ds_read_b64_tr_b16 v[178:179], v233 offset:512\n\t"
      "v_lshlrev_b32 v200, 16, v200\n\t"
      "ds_write_b32 v166, v200\n\t"
      "s_waitcnt lgkmcnt(9)\n\t"
      "v_mfma_f32_32x32x16_bf16 v[144:159], v[184:187], v[128:131], v[144:159]\n\t"
      "ds_read_b64_tr_b16 v[180:181], v233 offset:2048\n\t"
      "ds_read_b64_tr_b16 v[182:183], v233 offset:2560\n\t"
      "s_waitcnt lgkmcnt(9)\n\t"
      "v_mfma_f32_32x32x16_bf16 v[144:159], v[188:191], v[132:135], v[144:159]\n\t"
      "ds_read_b64_tr_b16 v[184:185], v233 offset:4096\n\t"
      "ds_read_b64_tr_b16 v[186:187], v233 offset:4608\n\t"
      "s_waitcnt lgkmcnt(9)\n\t"
      "v_mfma_f32_32x32x16_bf16 v[144:159], v[192:195], v[136:139], v[144:159]\n\t"
      "ds_read_b64_tr_b16 v[188:189], v233 offset:6144\n\t"
      "ds_read_b64_tr_b16 v[190:191], v233 offset:6656\n\t"
      "s_waitcnt lgkmcnt(9)\n\t"
      "v_mfma_f32_32x32x16_bf16 v[160:175], v[196:199], v[140:143], v[144:159]\n\t"
      "ds_read_b64_tr_b16 v[192:193], v233 offset:8192\n\t"
      "ds_read_b64_tr_b16 v[194:195], v233 offset:8704\n\t"
      "v_swap_b32 v232, v233\n\t"
      "v_add_u32 v236, 49152, v232\n\t"
      "s_mov_b32 s52, s11\n\t"
      "v_mul_f32 v230, s13, v237\n\t"
      "s_mov_b32 s92, s84\n\t"
      "s_mov_b32 s93, s85\n\t"
      "s_mov_b32 s94, s7\n\t"
      "v_readfirstlane_b32 s54, v238\n\t"
      "s_nop 0\n\t"
      "s_mul_i32 s54, s54, 1664525\n\t"
      "s_add_u32 s54, s54, 1013904223\n\t"
      "v_mov_b32 v238, s54\n\t"
      "s_mov_b32 s53, 0xa2000\n\t"
      "s_cmp_lt_u32 s7, 0x510000\n\t"
      "s_cselect_b32 s92, s78, s76\n\t"
      "s_cselect_b32 s93, s79, s77\n\t"
      "s_cselect_b32 s94, 0x510000, s53\n\t"
      "s_cselect_b32 s54, 0, 0x510000\n\t"
      "s_sub_u32 s60, s7, s54\n\t"
      "s_setprio 3\n\t"
      "s_waitcnt lgkmcnt(8)\n\t"
      "v_mfma_f32_32x32x16_bf16 v[144:159], v[176:179], v[0:3], 0\n\t"
      "ds_read_b64_tr_b16 v[196:197], v232 offset:10240\n\t"
      "ds_read_b64_tr_b16 v[198:199], v232 offset:10752\n\t"
      "s_and_b32 s54, s62, 255\n\t"
      "s_and_b32 s55, s64, 255\n\t"
      "s_lshr_b64 s[62:63], s[62:63], 8\n\t"
      "s_lshr_b64 s[64:65], s[64:65], 8\n\t"
      "s_cmp_eq_u32 s55, 255\n\t"
      "s_cselect_b32 s55, 0x40000000, 64\n\t"
      "s_cmp_lt_u32 s54, 144\n\t"
      "s_cselect_b32 s40, s72, s70\n\t"
      "s_cselect_b32 s41, s73, s71\n\t"
      "s_cselect_b32 s42, 0x510000, s66\n\t"
      "s_cselect_b32 s49, 73728, s67\n\t"
      "s_cselect_b32 s48, s36, s37\n\t"
      "s_cselect_b64 vcc, -1, 0\n\t"
      "v_cndmask_b32 v234, v242, v241, vcc\n\t"
      "s_cmp_eq_u32 s54, 255\n\t"
      "s_cselect_b32 s42, 0, s42\n\t"
      "s_lshl_b32 s54, s54, 6\n\t"
      "s_add_u32 s48, s48, s54\n\t"
      "v_and_b32 v223, s55, v243\n\t"
      "v_add_u32 v234, v234, v223\n\t"
      "s_mov_b32 m0, s50\n\t"
      "s_add_u32 s50, s50, 1024\n\t"
      "buffer_load_dwordx4 v234, s[40:43], s48 offen lds\n\t"
      "s_add_u32 s48, s48, s49\n\t"
      "s_waitcnt lgkmcnt(8)\n\t"
      "v_mfma_f32_32x32x16_bf16 v[144:159], v[180:183], v[4:7], v[144:159]\n\t"
      "ds_read_b64_tr_b16 v[176:177], v232 offset:12288\n\t"
      "ds_read_b64_tr_b16 v[178:179], v232 offset:12800\n\t"
      "v_mul_f32 v160, v160, v230\n\t"
      "s_waitcnt lgkmcnt(8)\n\t"
      "v_mfma_f32_32x32x16_bf16 v[144:159], v[184:187], v[8:11], v[144:159]\n\t"
      "ds_read_b64_tr_b16 v[180:181], v232 offset:14336\n\t"
      "ds_read_b64_tr_b16 v[182:183], v232 offset:14848\n\t"
      "s_setprio 2\n\t"
      "s_waitcnt lgkmcnt(8)\n\t"
      "v_mfma_f32_32x32x16_bf16 v[144:159], v[188:191], v[12:15], v[144:159]\n\t"
      "ds_read_b64_tr_b16 v[184:185], v232 offset:16384\n\t"
      "ds_read_b64_tr_b16 v[186:187], v232 offset:16896\n\t"
      "v_mul_f32 v161, v161, v230\n\t"
      "s_waitcnt lgkmcnt(8)\n\t"
      "v_mfma_f32_32x32x16_bf16 v[144:159], v[192:195], v[16:19], v[144:159]\n\t"
      "ds_read_b64_tr_b16 v[188:189], v232 offset:18432\n\t"
      "ds_read_b64_tr_b16 v[190:191], v232 offset:18944\n\t"
      "v_cvt_pk_bf16_f32 v216, v160, v161\n\t"
      "s_mov_b32 m0, s50\n\t"
      "s_add_u32 s50, s50, 1024\n\t"
      "buffer_load_dwordx4 v234, s[40:43], s48 offen lds\n\t"
      "s_add_u32 s48, s48, s49\n\t"
      "s_waitcnt lgkmcnt(8)\n\t"
      "v_mfma_f32_32x32x16_bf16 v[144:159], v[196:199], v[20:23], v[144:159]\n\t"
      "ds_read_b64_tr_b16 v[192:193], v232 offset:20480\n\t"
      "ds_read_b64_tr_b16 v[194:195], v232 offset:20992\n\t"
      "s_setprio 1\n\t"
      "s_waitcnt lgkmcnt(8)\n\t"
      "v_mfma_f32_32x32x16_bf16 v[144:159], v[176:179], v[24:27], v[144:159]\n\t"
      "ds_read_b64_tr_b16 v[196:197], v232 offset:22528\n\t"
      "ds_read_b64_tr_b16 v[198:199], v232 offset:23040\n\t"
      "v_mul_f32 v162, v162, v230\n\t"
      "s_waitcnt vmcnt(5)\n\t"
      "s_barrier\n\t"
      "s_waitcnt lgkmcnt(8)\n\t"
      "v_mfma_f32_32x32x16_bf16 v[144:159], v[180:183], v[28:31], v[144:159]\n\t"
      "ds_read_b64_tr_b16 v[176:177], v232 offset:24576\n\t"
      "ds_read_b64_tr_b16 v[178:179], v232 offset:25088\n\t"
      "v_mul_f32 v163, v163, v230\n\t"
      "s_waitcnt lgkmcnt(8)\n\t"
      "v_mfma_f32_32x32x16_bf16 v[144:159], v[184:187], v[32:35], v[144:159]\n\t"
      "ds_read_b64_tr_b16 v[180:181], v232 offset:26624\n\t"
      "ds_read_b64_tr_b16 v[182:183], v232 offset:27136\n\t"
      "s_mov_b32 m0, s50\n\t"
      "s_add_u32 s50, s50, 1024\n\t"
      "buffer_load_dwordx4 v234, s[40:43], s48 offen lds\n\t"
      "s_add_u32 s48, s48, s49\n\t"
      "s_mul_i32 s54, s49, 21\n\t"
      "s_add_u32 s48, s48, s54\n\t"
      "s_add_u32 s50, s50, 21504\n\t"
      "s_cmp_ge_u32 s50, 147456\n\t"
      "s_cselect_b32 s50, s38, s50\n\t"
      "s_setprio 0\n\t"
      "s_waitcnt lgkmcnt(8)\n\t"
      "v_mfma_f32_32x32x16_bf16 v[144:159], v[188:191], v[36:39], v[144:159]\n\t"
      "ds_read_b64_tr_b16 v[184:185], v232 offset:28672\n\t"
      "ds_read_b64_tr_b16 v[186:187], v232 offset:29184\n\t"
      "v_cvt_pk_bf16_f32 v217, v162, v163\n\t"
      "buffer_load_dwordx4 v[248:251], v240, s[92:95], s60 offen\n\t"
      "s_add_u32 s60, s60, 1024\n\t"
      "v_not_b32 v239, v238\n\t"
      "s_mov_b64 exec, 1\n\t"
      "buffer_store_dword v239, off, s[44:47], s30 sc1\n\t"
      "buffer_store_dword v239, off, s[44:47], s31 sc1\n\t"
      "buffer_store_dword v239, off, s[44:47], s3 sc1\n\t"
      "buffer_store_dword v239, off, s[44:47], s17 sc1\n\t"
      "s_mov_b64 exec, -1\n\t"
      "s_waitcnt lgkmcnt(8)\n\t"
      "v_mfma_f32_32x32x16_bf16 v[144:159], v[192:195], v[40:43], v[144:159]\n\t"
      "ds_read_b64_tr_b16 v[188:189], v232 offset:30720\n\t"
      "ds_read_b64_tr_b16 v[190:191], v232 offset:31232\n\t"
      "v_mul_f32 v164, v164, v230\n\t"
      "s_waitcnt lgkmcnt(8)\n\t"
      "v_mfma_f32_32x32x16_bf16 v[144:159], v[196:199], v[44:47], v[144:159]\n\t"
      "ds_read_b64_tr_b16 v[192:193], v232 offset:32768\n\t"
      "ds_read_b64_tr_b16 v[194:195], v232 offset:33280\n\t"
      "s_setprio 3\n\t"
      "s_waitcnt lgkmcnt(8)\n\t"
      "v_mfma_f32_32x32x16_bf16 v[144:159], v[176:179], v[48:51], v[144:159]\n\t"
      "ds_read_b64_tr_b16 v[196:197], v232 offset:34816\n\t"
      "ds_read_b64_tr_b16 v[198:199], v232 offset:35328\n\t"
      "v_mul_f32 v165, v165, v230\n\t"
      "s_mov_b32 m0, s50\n\t"
      "s_add_u32 s50, s50, 1024\n\t"
      "buffer_load_dwordx4 v234, s[40:43], s48 offen lds\n\t"
      "s_add_u32 s48, s48, s49\n\t"
      "s_waitcnt lgkmcnt(8)\n\t"
      "v_mfma_f32_32x32x16_bf16 v[144:159], v[180:183], v[52:55], v[144:159]\n\t"
      "ds_read_b64_tr_b16 v[176:177], v232 offset:36864\n\t"
      "ds_read_b64_tr_b16 v[178:179], v232 offset:37376\n\t"
      "v_cvt_pk_bf16_f32 v218, v164, v165\n\t"
      "s_waitcnt lgkmcnt(8)\n\t"
      "v_mfma_f32_32x32x16_bf16 v[144:159], v[184:187], v[56:59], v[144:159]\n\t"
      "ds_read_b64_tr_b16 v[180:181], v232 offset:38912\n\t"
      "ds_read_b64_tr_b16 v[182:183], v232 offset:39424\n\t"
      "s_setprio 2\n\t"
      "s_waitcnt lgkmcnt(8)\n\t"
      "v_mfma_f32_32x32x16_bf16 v[144:159], v[188:191], v[60:63], v[144:159]\n\t"
      "ds_read_b64_tr_b16 v[184:185], v232 offset:40960\n\t"
      "ds_read_b64_tr_b16 v[186:187], v232 offset:41472\n\t"
      "v_mul_f32 v166, v166, v230\n\t"
      "s_waitcnt lgkmcnt(8)\n\t"
      "v_mfma_f32_32x32x16_bf16 v[144:159], v[192:195], v[64:67], v[144:159]\n\t"
      "ds_read_b64_tr_b16 v[188:189], v232 offset:43008\n\t"
      "ds_read_b64_tr_b16 v[190:191], v232 offset:43520\n\t"
      "v_mul_f32 v167, v167, v230\n\t"
      "s_mov_b32 m0, s50\n\t"
      "s_add_u32 s50, s50, 1024\n\t"
      "buffer_load_dwordx4 v234, s[40:43], s48 offen lds\n\t"
      "s_add_u32 s48, s48, s49\n\t"
      "s_waitcnt lgkmcnt(8)\n\t"
      "v_mfma_f32_32x32x16_bf16 v[144:159], v[196:199], v[68:71], v[144:159]\n\t"
      "ds_read_b64_tr_b16 v[192:193], v232 offset:45056\n\t"
      "ds_read_b64_tr_b16 v[194:195], v232 offset:45568\n\t"
      "s_setprio 1\n\t"
      "s_waitcnt lgkmcnt(8)\n\t"
      "v_mfma_f32_32x32x16_bf16 v[144:159], v[176:179], v[72:75], v[144:159]\n\t"
      "ds_read_b64_tr_b16 v[196:197], v232 offset:47104\n\t"
      "ds_read_b64_tr_b16 v[198:199], v232 offset:47616\n\t"
      "v_cvt_pk_bf16_f32 v219, v166, v167\n\t"
      "buffer_store_dwordx4 v[216:219], v240, s[44:47], s52 offen offset:0 sc1\n\t"
      "s_waitcnt vmcnt(11)\n\t"
      "s_barrier\n\t"
      "s_waitcnt lgkmcnt(8)\n\t"
      "v_mfma_f32_32x32x16_bf16 v[144:159], v[180:183], v[76:79], v[144:159]\n\t"
      "ds_read_b64_tr_b16 v[176:177], v236 offset:0\n\t"
      "ds_read_b64_tr_b16 v[178:179], v236 offset:512\n\t"
      "v_mul_f32 v168, v168, v230\n\t"
      "s_waitcnt lgkmcnt(8)\n\t"
      "v_mfma_f32_32x32x16_bf16 v[144:159], v[184:187], v[80:83], v[144:159]\n\t"
      "ds_read_b64_tr_b16 v[180:181], v236 offset:2048\n\t"
      "ds_read_b64_tr_b16 v[182:183], v236 offset:2560\n\t"
      "v_mul_f32 v169, v169, v230\n\t"
      "s_mov_b32 m0, s50\n\t"
      "s_add_u32 s50, s50, 1024\n\t"
      "buffer_load_dwordx4 v234, s[40:43], s48 offen lds\n\t"
      "s_add_u32 s48, s48, s49\n\t"
      "s_mul_i32 s54, s49, 21\n\t"
      "s_add_u32 s48, s48, s54\n\t"
      "s_add_u32 s50, s50, 21504\n\t"
      "s_cmp_ge_u32 s50, 147456\n\t"
      "s_cselect_b32 s50, s38, s50\n\t"
      "s_setprio 0\n\t"
      "s_waitcnt lgkmcnt(8)\n\t"
      "v_mfma_f32_32x32x16_bf16 v[144:159], v[188:191], v[84:87], v[144:159]\n\t"
      "ds_read_b64_tr_b16 v[184:185], v236 offset:4096\n\t"
      "ds_read_b64_tr_b16 v[186:187], v236 offset:4608\n\t"
      "v_cvt_pk_bf16_f32 v220, v168, v169\n\t"
      "s_waitcnt lgkmcnt(8)\n\t"
      "v_mfma_f32_32x32x16_bf16 v[144:159], v[192:195], v[88:91], v[144:159]\n\t"
      "ds_read_b64_tr_b16 v[188:189], v236 offset:6144\n\t"
      "ds_read_b64_tr_b16 v[190:191], v236 offset:6656\n\t"
      "v_mul_f32 v170, v170, v230\n\t"
      "s_waitcnt lgkmcnt(8)\n\t"
      "v_mfma_f32_32x32x16_bf16 v[144:159], v[196:199], v[92:95], v[144:159]\n\t"
      "ds_read_b64_tr_b16 v[192:193], v236 offset:8192\n\t"
      "ds_read_b64_tr_b16 v[194:195], v236 offset:8704\n\t"
      "v_mul_f32 v171, v171, v230\n\t"
      "s_setprio 3\n\t"
      "s_waitcnt lgkmcnt(8)\n\t"
      "v_mfma_f32_32x32x16_bf16 v[144:159], v[176:179], v[96:99], v[144:159]\n\t"
      "ds_read_b64_tr_b16 v[196:197], v236 offset:10240\n\t"
      "ds_read_b64_tr_b16 v[198:199], v236 offset:10752\n\t"
      "v_cvt_pk_bf16_f32 v221, v170, v171\n\t"
      "s_mov_b32 m0, s50\n\t"
      "s_add_u32 s50, s50, 1024\n\t"
      "buffer_load_dwordx4 v234, s[40:43], s48 offen lds\n\t"
      "s_add_u32 s48, s48, s49\n\t"
      "s_waitcnt lgkmcnt(8)\n\t"
      "v_mfma_f32_32x32x16_bf16 v[144:159], v[180:183], v[100:103], v[144:159]\n\t"
      "ds_read_b64_tr_b16 v[176:177], v236 offset:12288\n\t"
      "ds_read_b64_tr_b16 v[178:179], v236 offset:12800\n\t"
      "v_mul_f32 v172, v172, v230\n\t"
      "s_waitcnt lgkmcnt(8)\n\t"
      "v_mfma_f32_32x32x16_bf16 v[144:159], v[184:187], v[104:107], v[144:159]\n\t"
      "ds_read_b64_tr_b16 v[180:181], v236 offset:14336\n\t"
      "ds_read_b64_tr_b16 v[182:183], v236 offset:14848\n\t"
      "v_mul_f32 v173, v173, v230\n\t"
      "s_setprio 2\n\t"
      "s_waitcnt lgkmcnt(8)\n\t"
      "v_mfma_f32_32x32x16_bf16 v[144:159], v[188:191], v[108:111], v[144:159]\n\t"
      "ds_read_b64_tr_b16 v[184:185], v236 offset:16384\n\t"
      "ds_read_b64_tr_b16 v[186:187], v236 offset:16896\n\t"
      "v_cvt_pk_bf16_f32 v222, v172, v173\n\t"
      "s_waitcnt lgkmcnt(8)\n\t"
      "v_mfma_f32_32x32x16_bf16 v[144:159], v[192:195], v[112:115], v[144:159]\n\t"
      "ds_read_b64_tr_b16 v[188:189], v236 offset:18432\n\t"
      "ds_read_b64_tr_b16 v[190:191], v236 offset:18944\n\t"
      "v_mul_f32 v174, v174, v230\n\t"
      "s_mov_b32 m0, s50\n\t"
      "s_add_u32 s50, s50, 1024\n\t"
      "buffer_load_dwordx4 v234, s[40:43], s48 offen lds\n\t"
      "s_add_u32 s48, s48, s49\n\t"
      "s_waitcnt lgkmcnt(8)\n\t"
      "v_mfma_f32_32x32x16_bf16 v[144:159], v[196:199], v[116:119], v[144:159]\n\t"
      "ds_read_b64_tr_b16 v[192:193], v236 offset:20480\n\t"
      "ds_read_b64_tr_b16 v[194:195], v236 offset:20992\n\t"
      "v_mul_f32 v175, v175, v230\n\t"
      "s_setprio 1\n\t"
      "s_waitcnt lgkmcnt(8)\n\t"
      "v_mfma_f32_32x32x16_bf16 v[144:159], v[176:179], v[120:123], v[144:159]\n\t"
      "ds_read_b64_tr_b16 v[196:197], v236 offset:22528\n\t"
      "ds_read_b64_tr_b16 v[198:199], v236 offset:23040\n\t"
      "v_cvt_pk_bf16_f32 v223, v174, v175\n\t"
      "ds_read_b128 v[200:203], v235 offset:0\n\t"
      "ds_read_b128 v[204:207], v235 offset:32\n\t"
      "ds_read_b128 v[208:211], v235 offset:64\n\t"
      "ds_read_b128 v[212:215], v235 offset:96\n\t"
      "s_waitcnt vmcnt(11)\n\t"
      "s_barrier\n\t"
      "s_waitcnt lgkmcnt(12)\n\t"
      "v_mfma_f32_32x32x16_bf16 v[144:159], v[180:183], v[124:127], v[144:159]\n\t"
      "ds_read_b64_tr_b16 v[176:177], v233 offset:0\n\t"
      "ds_read_b64_tr_b16 v[178:179], v233 offset:512\n\t"
      "s_waitcnt lgkmcnt(12)\n\t"
      "v_mfma_f32_32x32x16_bf16 v[144:159], v[184:187], v[128:131], v[144:159]\n\t"
      "ds_read_b64_tr_b16 v[180:181], v233 offset:2048\n\t"
      "ds_read_b64_tr_b16 v[182:183], v233 offset:2560\n\t"
      "s_mov_b32 m0, s50\n\t"
      "s_add_u32 s50, s50, 1024\n\t"
      "buffer_load_dwordx4 v234, s[40:43], s48 offen lds\n\t"
      "s_add_u32 s48, s48, s49\n\t"
      "s_mul_i32 s54, s49, 21\n\t"
      "s_add_u32 s48, s48, s54\n\t"
      "s_add_u32 s50, s50, 21504\n\t"
      "s_cmp_ge_u32 s50, 147456\n\t"
      "s_cselect_b32 s50, s38, s50\n\t"
      "s_setprio 0\n\t"
      "s_waitcnt lgkmcnt(12)\n\t"
      "v_mfma_f32_32x32x16_bf16 v[144:159], v[188:191], v[132:135], v[144:159]\n\t"
      "ds_read_b64_tr_b16 v[184:185], v233 offset:4096\n\t"
      "ds_read_b64_tr_b16 v[186:187], v233 offset:4608\n\t"
      "s_waitcnt lgkmcnt(12)\n\t"
      "v_mfma_f32_32x32x16_bf16 v[144:159], v[192:195], v[136:139], v[144:159]\n\t"
      "ds_read_b64_tr_b16 v[188:189], v233 offset:6144\n\t"
      "ds_read_b64_tr_b16 v[190:191], v233 offset:6656\n\t"
      "s_waitcnt lgkmcnt(12)\n\t"
      "v_mfma_f32_32x32x16_bf16 v[160:175], v[196:199], v[140:143], v[144:159]\n\t"
      "ds_read_b64_tr_b16 v[192:193], v233 offset:8192\n\t"
      "ds_read_b64_tr_b16 v[194:195], v233 offset:8704\n\t"
      "buffer_store_dwordx4 v[220:223], v240, s[44:47], s52 offen offset:1024 sc1\n\t"
      "v_swap_b32 v232, v233\n\t"
      "v_add_u32 v236, 49152, v232\n\t"
      "s_cmp_eq_u32 s12, 0\n\t"
      "s_cbranch_scc1 Lnol2%=\n\t"
      "s_mov_b32 s52, s12\n\t"
      "v_mul_f32 v230, s14, v237\n\t"
      "s_cmp_eq_u32 s20, 0\n\t"
      "s_cbranch_scc1 Ll2%=\n\t"
      "s_cmp_ge_u32 s39, 4\n\t"
      "s_cbranch_scc1 Lhidle%=\n\t"
      "s_setprio 3\n\t"
      "s_waitcnt lgkmcnt(8)\n\t"
      "v_mfma_f32_32x32x16_bf16 v[144:159], v[176:179], v[0:3], 0\n\t"
      "ds_read_b64_tr_b16 v[196:197], v232 offset:10240\n\t"
      "ds_read_b64_tr_b16 v[198:199], v232 offset:10752\n\t"
      "s_and_b32 s54, s62, 255\n\t"
      "s_and_b32 s55, s64, 255\n\t"
      "s_lshr_b64 s[62:63], s[62:63], 8\n\t"
      "s_lshr_b64 s[64:65], s[64:65], 8\n\t"
      "s_cmp_eq_u32 s55, 255\n\t"
      "s_cselect_b32 s55, 0x40000000, 64\n\t"
      "s_cmp_lt_u32 s54, 144\n\t"
      "s_cselect_b32 s40, s72, s70\n\t"
      "s_cselect_b32 s41, s73, s71\n\t"
      "s_cselect_b32 s42, 0x510000, s66\n\t"
      "s_cselect_b32 s49, 73728, s67\n\t"
      "s_cselect_b32 s48, s36, s37\n\t"
      "s_cselect_b64 vcc, -1, 0\n\t"
      "v_cndmask_b32 v234, v242, v241, vcc\n\t"
      "s_cmp_eq_u32 s54, 255\n\t"
      "s_cselect_b32 s42, 0, s42\n\t"
      "s_lshl_b32 s54, s54, 6\n\t"
      "s_add_u32 s48, s48, s54\n\t"
      "v_and_b32 v223, s55, v243\n\t"
      "v_add_u32 v234, v234, v223\n\t"
      "s_mov_b32 m0, s50\n\t"
      "s_add_u32 s50, s50, 1024\n\t"
      "buffer_load_dwordx4 v234, s[40:43], s48 offen lds\n\t"
      "s_add_u32 s48, s48, s49\n\t"
      "s_waitcnt lgkmcnt(8)\n\t"
      "v_mfma_f32_32x32x16_bf16 v[144:159], v[180:183], v[4:7], v[144:159]\n\t"
      "ds_read_b64_tr_b16 v[176:177], v232 offset:12288\n\t"
      "ds_read_b64_tr_b16 v[178:179], v232 offset:12800\n\t"
      "v_mul_f32 v160, v160, v230\n\t"
      "s_waitcnt lgkmcnt(8)\n\t"
      "v_mfma_f32_32x32x16_bf16 v[144:159], v[184:187], v[8:11], v[144:159]\n\t"
      "ds_read_b64_tr_b16 v[180:181], v232 offset:14336\n\t"
      "ds_read_b64_tr_b16 v[182:183], v232 offset:14848\n\t"
      "s_setprio 2\n\t"
      "s_waitcnt lgkmcnt(8)\n\t"
      "v_mfma_f32_32x32x16_bf16 v[144:159], v[188:191], v[12:15], v[144:159]\n\t"
      "ds_read_b64_tr_b16 v[184:185], v232 offset:16384\n\t"
      "ds_read_b64_tr_b16 v[186:187], v232 offset:16896\n\t"
      "v_mul_f32 v161, v161, v230\n\t"
      "s_waitcnt lgkmcnt(8)\n\t"
      "v_mfma_f32_32x32x16_bf16 v[144:159], v[192:195], v[16:19], v[144:159]\n\t"
      "ds_read_b64_tr_b16 v[188:189], v232 offset:18432\n\t"
      "ds_read_b64_tr_b16 v[190:191], v232 offset:18944\n\t"
      "v_cvt_pk_bf16_f32 v216, v160, v161\n\t"
      "s_mov_b32 m0, s50\n\t"
      "s_add_u32 s50, s50, 1024\n\t"
      "buffer_load_dwordx4 v234, s[40:43], s48 offen lds\n\t"
      "s_add_u32 s48, s48, s49\n\t"
      "s_waitcnt lgkmcnt(8)\n\t"
      "v_mfma_f32_32x32x16_bf16 v[144:159], v[196:199], v[20:23], v[144:159]\n\t"
      "ds_read_b64_tr_b16 v[192:193], v232 offset:20480\n\t"
      "ds_read_b64_tr_b16 v[194:195], v232 offset:20992\n\t"
      "s_setprio 1\n\t"
      "s_waitcnt lgkmcnt(8)\n\t"
      "v_mfma_f32_32x32x16_bf16 v[144:159], v[176:179], v[24:27], v[144:159]\n\t"
      "ds_read_b64_tr_b16 v[196:197], v232 offset:22528\n\t"
      "ds_read_b64_tr_b16 v[198:199], v232 offset:23040\n\t"
      "v_mul_f32 v162, v162, v230\n\t"
      "s_waitcnt vmcnt(6)\n\t"
      "s_barrier\n\t"
      "s_waitcnt lgkmcnt(8)\n\t"
      "v_mfma_f32_32x32x16_bf16 v[144:159], v[180:183], v[28:31], v[144:159]\n\t"
      "ds_read_b64_tr_b16 v[176:177], v232 offset:24576\n\t"
      "ds_read_b64_tr_b16 v[178:179], v232 offset:25088\n\t"
      "v_mul_f32 v163, v163, v230\n\t"
      "s_waitcnt lgkmcnt(8)\n\t"
      "v_mfma_f32_32x32x16_bf16 v[144:159], v[184:187], v[32:35], v[144:159]\n\t"
      "ds_read_b64_tr_b16 v[180:181], v232 offset:26624\n\t"
      "ds_read_b64_tr_b16 v[182:183], v232 offset:27136\n\t"
      "s_mov_b32 m0, s50\n\t"
      "s_add_u32 s50, s50, 1024\n\t"
      "buffer_load_dwordx4 v234, s[40:43], s48 offen lds\n\t"
      "s_add_u32 s48, s48, s49\n\t"
      "s_mul_i32 s54, s49, 21\n\t"
      "s_add_u32 s48, s48, s54\n\t"
      "s_add_u32 s50, s50, 21504\n\t"
      "s_cmp_ge_u32 s50, 147456\n\t"
      "s_cselect_b32 s50, s38, s50\n\t"
      "s_setprio 0\n\t"
      "s_waitcnt lgkmcnt(8)\n\t"
      "v_mfma_f32_32x32x16_bf16 v[144:159], v[188:191], v[36:39], v[144:159]\n\t"
      "ds_read_b64_tr_b16 v[184:185], v232 offset:28672\n\t"
      "ds_read_b64_tr_b16 v[186:187], v232 offset:29184\n\t"
      "v_cvt_pk_bf16_f32 v217, v162, v163\n\t"
      "s_waitcnt lgkmcnt(8)\n\t"
      "v_mfma_f32_32x32x16_bf16 v[144:159], v[192:195], v[40:43], v[144:159]\n\t"
      "ds_read_b64_tr_b16 v[188:189], v232 offset:30720\n\t"
      "ds_read_b64_tr_b16 v[190:191], v232 offset:31232\n\t"
      "v_mul_f32 v164, v164, v230\n\t"
      "s_waitcnt lgkmcnt(8)\n\t"
      "v_mfma_f32_32x32x16_bf16 v[144:159], v[196:199], v[44:47], v[144:159]\n\t"
      "ds_read_b64_tr_b16 v[192:193], v232 offset:32768\n\t"
      "ds_read_b64_tr_b16 v[194:195], v232 offset:33280\n\t"
      "s_setprio 3\n\t"
      "s_waitcnt lgkmcnt(8)\n\t"
      "v_mfma_f32_32x32x16_bf16 v[144:159], v[176:179], v[48:51], v[144:159]\n\t"
      "ds_read_b64_tr_b16 v[196:197], v232 offset:34816\n\t"
      "ds_read_b64_tr_b16 v[198:199], v232 offset:35328\n\t"
      "v_mul_f32 v165, v165, v230\n\t"
      "s_mov_b32 m0, s50\n\t"
      "s_add_u32 s50, s50, 1024\n\t"
      "buffer_load_dwordx4 v234, s[40:43], s48 offen lds\n\t"
      "s_add_u32 s48, s48, s49\n\t"
      "s_waitcnt lgkmcnt(8)\n\t"
      "v_mfma_f32_32x32x16_bf16 v[144:159], v[180:183], v[52:55], v[144:159]\n\t"
      "ds_read_b64_tr_b16 v[176:177], v232 offset:36864\n\t"
      "ds_read_b64_tr_b16 v[178:179], v232 offset:37376\n\t"
      "v_cvt_pk_bf16_f32 v218, v164, v165\n\t"
      "s_waitcnt lgkmcnt(8)\n\t"
      "v_mfma_f32_32x32x16_bf16 v[144:159], v[184:187], v[56:59], v[144:159]\n\t"
      "ds_read_b64_tr_b16 v[180:181], v232 offset:38912\n\t"
      "ds_read_b64_tr_b16 v[182:183], v232 offset:39424\n\t"
      "s_setprio 2\n\t"
      "s_waitcnt lgkmcnt(8)\n\t"
      "v_mfma_f32_32x32x16_bf16 v[144:159], v[188:191], v[60:63], v[144:159]\n\t"
      "ds_read_b64_tr_b16 v[184:185], v232 offset:40960\n\t"
      "ds_read_b64_tr_b16 v[186:187], v232 offset:41472\n\t"
      "v_mul_f32 v166, v166, v230\n\t"
      "s_waitcnt lgkmcnt(8)\n\t"
      "v_mfma_f32_32x32x16_bf16 v[144:159], v[192:195], v[64:67], v[144:159]\n\t"
      "ds_read_b64_tr_b16 v[188:189], v232 offset:43008\n\t"
      "ds_read_b64_tr_b16 v[190:191], v232 offset:43520\n\t"
      "v_mul_f32 v167, v167, v230\n\t"
      "s_mov_b32 m0, s50\n\t"
      "s_add_u32 s50, s50, 1024\n\t"
      "buffer_load_dwordx4 v234, s[40:43], s48 offen lds\n\t"
      "s_add_u32 s48, s48, s49\n\t"
      "s_waitcnt lgkmcnt(8)\n\t"
      "v_mfma_f32_32x32x16_bf16 v[144:159], v[196:199], v[68:71], v[144:159]\n\t"
      "ds_read_b64_tr_b16 v[192:193], v232 offset:45056\n\t"
      "ds_read_b64_tr_b16 v[194:195], v232 offset:45568\n\t"
      "s_setprio 1\n\t"
      "s_waitcnt lgkmcnt(8)\n\t"
      "v_mfma_f32_32x32x16_bf16 v[144:159], v[176:179], v[72:75], v[144:159]\n\t"
      "ds_read_b64_tr_b16 v[196:197], v232 offset:47104\n\t"
      "ds_read_b64_tr_b16 v[198:199], v232 offset:47616\n\t"
      "v_cvt_pk_bf16_f32 v219, v166, v167\n\t"
      "buffer_store_dwordx4 v[216:219], v240, s[44:47], s52 offen offset:0 sc1\n\t"
      "s_waitcnt vmcnt(7)\n\t"
      "s_barrier\n\t"
      "s_waitcnt lgkmcnt(8)\n\t"
      "v_mfma_f32_32x32x16_bf16 v[144:159], v[180:183], v[76:79], v[144:159]\n\t"
      "ds_read_b64_tr_b16 v[176:177], v236 offset:0\n\t"
      "ds_read_b64_tr_b16 v[178:179], v236 offset:512\n\t"
      "v_mul_f32 v168, v168, v230\n\t"
      "s_waitcnt lgkmcnt(8)\n\t"
      "v_mfma_f32_32x32x16_bf16 v[144:159], v[184:187], v[80:83], v[144:159]\n\t"
      "ds_read_b64_tr_b16 v[180:181], v236 offset:2048\n\t"
      "ds_read_b64_tr_b16 v[182:183], v236 offset:2560\n\t"
      "v_mul_f32 v169, v169, v230\n\t"
      "s_mov_b32 m0, s50\n\t"
      "s_add_u32 s50, s50, 1024\n\t"
      "buffer_load_dwordx4 v234, s[40:43], s48 offen lds\n\t"
      "s_add_u32 s48, s48, s49\n\t"
      "s_mul_i32 s54, s49, 21\n\t"
      "s_add_u32 s48, s48, s54\n\t"
      "s_add_u32 s50, s50, 21504\n\t"
      "s_cmp_ge_u32 s50, 147456\n\t"
      "s_cselect_b32 s50, s38, s50\n\t"
      "s_setprio 0\n\t"
      "s_waitcnt lgkmcnt(8)\n\t"
      "v_mfma_f32_32x32x16_bf16 v[144:159], v[188:191], v[84:87], v[144:159]\n\t"
      "ds_read_b64_tr_b16 v[184:185], v236 offset:4096\n\t"
      "ds_read_b64_tr_b16 v[186:187], v236 offset:4608\n\t"
      "v_cvt_pk_bf16_f32 v220, v168, v169\n\t"
      "s_waitcnt lgkmcnt(8)\n\t"
      "v_mfma_f32_32x32x16_bf16 v[144:159], v[192:195], v[88:91], v[144:159]\n\t"
      "ds_read_b64_tr_b16 v[188:189], v236 offset:6144\n\t"
      "ds_read_b64_tr_b16 v[190:191], v236 offset:6656\n\t"
      "v_mul_f32 v170, v170, v230\n\t"
      "s_waitcnt lgkmcnt(8)\n\t"
      "v_mfma_f32_32x32x16_bf16 v[144:159], v[196:199], v[92:95], v[144:159]\n\t"
      "ds_read_b64_tr_b16 v[192:193], v236 offset:8192\n\t"
      "ds_read_b64_tr_b16 v[194:195], v236 offset:8704\n\t"
      "v_mul_f32 v171, v171, v230\n\t"
      "s_setprio 3\n\t"
      "s_waitcnt lgkmcnt(8)\n\t"
      "v_mfma_f32_32x32x16_bf16 v[144:159], v[176:179], v[96:99], v[144:159]\n\t"
      "ds_read_b64_tr_b16 v[196:197], v236 offset:10240\n\t"
      "ds_read_b64_tr_b16 v[198:199], v236 offset:10752\n\t"
      "v_cvt_pk_bf16_f32 v221, v170, v171\n\t"
      "s_mov_b32 m0, s50\n\t"
      "s_add_u32 s50, s50, 1024\n\t"
      "buffer_load_dwordx4 v234, s[40:43], s48 offen lds\n\t"
      "s_add_u32 s48, s48, s49\n\t"
      "s_waitcnt lgkmcnt(8)\n\t"
      "v_mfma_f32_32x32x16_bf16 v[144:159], v[180:183], v[100:103], v[144:159]\n\t"
      "ds_read_b64_tr_b16 v[176:177], v236 offset:12288\n\t"
      "ds_read_b64_tr_b16 v[178:179], v236 offset:12800\n\t"
      "v_mul_f32 v172, v172, v230\n\t"
      "s_waitcnt lgkmcnt(8)\n\t"
      "v_mfma_f32_32x32x16_bf16 v[144:159], v[184:187], v[104:107], v[144:159]\n\t"
      "ds_read_b64_tr_b16 v[180:181], v236 offset:14336\n\t"
      "ds_read_b64_tr_b16 v[182:183], v236 offset:14848\n\t"
      "v_mul_f32 v173, v173, v230\n\t"
      "s_setprio 2\n\t"
      "s_waitcnt lgkmcnt(8)\n\t"
      "v_mfma_f32_32x32x16_bf16 v[144:159], v[188:191], v[108:111], v[144:159]\n\t"
      "ds_read_b64_tr_b16 v[184:185], v236 offset:16384\n\t"
      "ds_read_b64_tr_b16 v[186:187], v236 offset:16896\n\t"
      "v_cvt_pk_bf16_f32 v222, v172, v173\n\t"
      "s_waitcnt lgkmcnt(8)\n\t"
      "v_mfma_f32_32x32x16_bf16 v[144:159], v[192:195], v[112:115], v[144:159]\n\t"
      "ds_read_b64_tr_b16 v[188:189], v236 offset:18432\n\t"
      "ds_read_b64_tr_b16 v[190:191], v236 offset:18944\n\t"
      "v_mul_f32 v174, v174, v230\n\t"
      "s_mov_b32 m0, s50\n\t"
      "s_add_u32 s50, s50, 1024\n\t"
      "buffer_load_dwordx4 v234, s[40:43], s48 offen lds\n\t"
      "s_add_u32 s48, s48, s49\n\t"
      "s_waitcnt lgkmcnt(8)\n\t"
      "v_mfma_f32_32x32x16_bf16 v[144:159], v[196:199], v[116:119], v[144:159]\n\t"
      "ds_read_b64_tr_b16 v[192:193], v236 offset:20480\n\t"
      "ds_read_b64_tr_b16 v[194:195], v236 offset:20992\n\t"
      "v_mul_f32 v175, v175, v230\n\t"
      "s_setprio 1\n\t"
      "s_waitcnt lgkmcnt(8)\n\t"
      "v_mfma_f32_32x32x16_bf16 v[144:159], v[176:179], v[120:123], v[144:159]\n\t"
      "ds_read_b64_tr_b16 v[196:197], v236 offset:22528\n\t"
      "ds_read_b64_tr_b16 v[198:199], v236 offset:23040\n\t"
      "v_cvt_pk_bf16_f32 v223, v174, v175\n\t"
      "ds_read_b128 v[200:203], v235 offset:0\n\t"
      "ds_read_b128 v[204:207], v235 offset:32\n\t"
      "ds_read_b128 v[208:211], v235 offset:64\n\t"
      "ds_read_b128 v[212:215], v235 offset:96\n\t"
      "s_waitcnt vmcnt(6)\n\t"
      "s_barrier\n\t"
      "s_waitcnt lgkmcnt(12)\n\t"
      "v_mfma_f32_32x32x16_bf16 v[144:159], v[180:183], v[124:127], v[144:159]\n\t"
      "ds_read_b64_tr_b16 v[176:177], v233 offset:0\n\t"
      "ds_read_b64_tr_b16 v[178:179], v233 offset:512\n\t"
      "s_waitcnt lgkmcnt(12)\n\t"
      "v_mfma_f32_32x32x16_bf16 v[144:159], v[184:187], v[128:131], v[144:159]\n\t"
      "ds_read_b64_tr_b16 v[180:181], v233 offset:2048\n\t"
      "ds_read_b64_tr_b16 v[182:183], v233 offset:2560\n\t"
      "s_mov_b32 m0, s50\n\t"
      "s_add_u32 s50, s50, 1024\n\t"
      "buffer_load_dwordx4 v234, s[40:43], s48 offen lds\n\t"
      "s_add_u32 s48, s48, s49\n\t"
      "s_mul_i32 s54, s49, 21\n\t"
      "s_add_u32 s48, s48, s54\n\t"
      "s_add_u32 s50, s50, 21504\n\t"
      "s_cmp_ge_u32 s50, 147456\n\t"
      "s_cselect_b32 s50, s38, s50\n\t"
      "s_setprio 0\n\t"
      "s_waitcnt lgkmcnt(12)\n\t"
      "v_mfma_f32_32x32x16_bf16 v[144:159], v[188:191], v[132:135], v[144:159]\n\t"
      "ds_read_b64_tr_b16 v[184:185], v233 offset:4096\n\t"
      "ds_read_b64_tr_b16 v[186:187], v233 offset:4608\n\t"
      "s_waitcnt lgkmcnt(12)\n\t"
      "v_mfma_f32_32x32x16_bf16 v[144:159], v[192:195], v[136:139], v[144:159]\n\t"
      "ds_read_b64_tr_b16 v[188:189], v233 offset:6144\n\t"
      "ds_read_b64_tr_b16 v[190:191], v233 offset:6656\n\t"
      "s_waitcnt lgkmcnt(12)\n\t"
      "v_mfma_f32_32x32x16_bf16 v[160:175], v[196:199], v[140:143], v[144:159]\n\t"
      "ds_read_b64_tr_b16 v[192:193], v233 offset:8192\n\t"
      "ds_read_b64_tr_b16 v[194:195], v233 offset:8704\n\t"
      "buffer_store_dwordx4 v[220:223], v240, s[44:47], s52 offen offset:1024 sc1\n\t"
      "s_branch Lhdone%=\n\t"
      "Lhidle%=:\n\t"
      "s_waitcnt lgkmcnt(0)\n\t"
      "s_setprio 3\n\t"
      "s_and_b32 s54, s62, 255\n\t"
      "s_and_b32 s55, s64, 255\n\t"
      "s_lshr_b64 s[62:63], s[62:63], 8\n\t"
      "s_lshr_b64 s[64:65], s[64:65], 8\n\t"
      "s_cmp_eq_u32 s55, 255\n\t"
      "s_cselect_b32 s55, 0x40000000, 64\n\t"
      "s_cmp_lt_u32 s54, 144\n\t"
      "s_cselect_b32 s40, s72, s70\n\t"
      "s_cselect_b32 s41, s73, s71\n\t"
      "s_cselect_b32 s42, 0x510000, s66\n\t"
      "s_cselect_b32 s49, 73728, s67\n\t"
      "s_cselect_b32 s48, s36, s37\n\t"
      "s_cselect_b64 vcc, -1, 0\n\t"
      "v_cndmask_b32 v234, v242, v241, vcc\n\t"
      "s_cmp_eq_u32 s54, 255\n\t"
      "s_cselect_b32 s42, 0, s42\n\t"
      "s_lshl_b32 s54, s54, 6\n\t"
      "s_add_u32 s48, s48, s54\n\t"
      "v_and_b32 v223, s55, v243\n\t"
      "v_add_u32 v234, v234, v223\n\t"
      "s_mov_b32 m0, s50\n\t"
      "s_add_u32 s50, s50, 1024\n\t"
      "buffer_load_dwordx4 v234, s[40:43], s48 offen lds\n\t"
      "s_add_u32 s48, s48, s49\n\t"
      "v_mul_f32 v160, v160, v230\n\t"
      "s_setprio 2\n\t"
      "v_mul_f32 v161, v161, v230\n\t"
      "v_cvt_pk_bf16_f32 v216, v160, v161\n\t"
      "s_mov_b32 m0, s50\n\t"
      "s_add_u32 s50, s50, 1024\n\t"
      "buffer_load_dwordx4 v234, s[40:43], s48 offen lds\n\t"
      "s_add_u32 s48, s48, s49\n\t"
      "s_setprio 1\n\t"
      "v_mul_f32 v162, v162, v230\n\t"
      "s_waitcnt vmcnt(6)\n\t"
      "s_barrier\n\t"
      "v_mul_f32 v163, v163, v230\n\t"
      "s_mov_b32 m0, s50\n\t"
      "s_add_u32 s50, s50, 1024\n\t"
      "buffer_load_dwordx4 v234, s[40:43], s48 offen lds\n\t"
      "s_add_u32 s48, s48, s49\n\t"
      "s_mul_i32 s54, s49, 21\n\t"
      "s_add_u32 s48, s48, s54\n\t"
      "s_add_u32 s50, s50, 21504\n\t"
      "s_cmp_ge_u32 s50, 147456\n\t"
      "s_cselect_b32 s50, s38, s50\n\t"
      "s_setprio 0\n\t"
      "v_cvt_pk_bf16_f32 v217, v162, v163\n\t"
      "v_mul_f32 v164, v164, v230\n\t"
      "s_setprio 3\n\t"
      "v_mul_f32 v165, v165, v230\n\t"
      "s_mov_b32 m0, s50\n\t"
      "s_add_u32 s50, s50, 1024\n\t"
      "buffer_load_dwordx4 v234, s[40:43], s48 offen lds\n\t"
      "s_add_u32 s48, s48, s49\n\t"
      "v_cvt_pk_bf16_f32 v218, v164, v165\n\t"
      "s_setprio 2\n\t"
      "v_mul_f32 v166, v166, v230\n\t"
      "v_mul_f32 v167, v167, v230\n\t"
      "s_mov_b32 m0, s50\n\t"
      "s_add_u32 s50, s50, 1024\n\t"
      "buffer_load_dwordx4 v234, s[40:43], s48 offen lds\n\t"
      "s_add_u32 s48, s48, s49\n\t"
      "s_setprio 1\n\t"
      "v_cvt_pk_bf16_f32 v219, v166, v167\n\t"
      "buffer_store_dwordx4 v[216:219], v240, s[44:47], s52 offen offset:0 sc1\n\t"
      "s_waitcnt vmcnt(7)\n\t"
      "s_barrier\n\t"
      "v_mul_f32 v168, v168, v230\n\t"
      "v_mul_f32 v169, v169, v230\n\t"
      "s_mov_b32 m0, s50\n\t"
      "s_add_u32 s50, s50, 1024\n\t"
      "buffer_load_dwordx4 v234, s[40:43], s48 offen lds\n\t"
      "s_add_u32 s48, s48, s49\n\t"
      "s_mul_i32 s54, s49, 21\n\t"
      "s_add_u32 s48, s48, s54\n\t"
      "s_add_u32 s50, s50, 21504\n\t"
      "s_cmp_ge_u32 s50, 147456\n\t"
      "s_cselect_b32 s50, s38, s50\n\t"
      "s_setprio 0\n\t"
      "v_cvt_pk_bf16_f32 v220, v168, v169\n\t"
      "v_mul_f32 v170, v170, v230\n\t"
      "v_mul_f32 v171, v171, v230\n\t"
      "s_setprio 3\n\t"
      "v_cvt_pk_bf16_f32 v221, v170, v171\n\t"
      "s_mov_b32 m0, s50\n\t"
      "s_add_u32 s50, s50, 1024\n\t"
      "buffer_load_dwordx4 v234, s[40:43], s48 offen lds\n\t"
      "s_add_u32 s48, s48, s49\n\t"
      "v_mul_f32 v172, v172, v230\n\t"
      "v_mul_f32 v173, v173, v230\n\t"
      "s_setprio 2\n\t"
      "v_cvt_pk_bf16_f32 v222, v172, v173\n\t"
      "v_mul_f32 v174, v174, v230\n\t"
      "s_mov_b32 m0, s50\n\t"
      "s_add_u32 s50, s50, 1024\n\t"
      "buffer_load_dwordx4 v234, s[40:43], s48 offen lds\n\t"
      "s_add_u32 s48, s48, s49\n\t"
      "v_mul_f32 v175, v175, v230\n\t"
      "s_setprio 1\n\t"
      "v_cvt_pk_bf16_f32 v223, v174, v175\n\t"
      "ds_read_b128 v[200:203], v235 offset:0\n\t"
      "ds_read_b128 v[204:207], v235 offset:32\n\t"
      "ds_read_b128 v[208:211], v235 offset:64\n\t"
      "ds_read_b128 v[212:215], v235 offset:96\n\t"
      "s_waitcnt vmcnt(6)\n\t"
      "s_barrier\n\t"
      "ds_read_b64_tr_b16 v[176:177], v233 offset:0\n\t"
      "ds_read_b64_tr_b16 v[178:179], v233 offset:512\n\t"
      "ds_read_b64_tr_b16 v[180:181], v233 offset:2048\n\t"
      "ds_read_b64_tr_b16 v[182:183], v233 offset:2560\n\t"
      "s_mov_b32 m0, s50\n\t"
      "s_add_u32 s50, s50, 1024\n\t"
      "buffer_load_dwordx4 v234, s[40:43], s48 offen lds\n\t"
      "s_add_u32 s48, s48, s49\n\t"
      "s_mul_i32 s54, s49, 21\n\t"
      "s_add_u32 s48, s48, s54\n\t"
      "s_add_u32 s50, s50, 21504\n\t"
      "s_cmp_ge_u32 s50, 147456\n\t"
      "s_cselect_b32 s50, s38, s50\n\t"
      "s_setprio 0\n\t"
      "ds_read_b64_tr_b16 v[184:185], v233 offset:4096\n\t"
      "ds_read_b64_tr_b16 v[186:187], v233 offset:4608\n\t"
      "ds_read_b64_tr_b16 v[188:189], v233 offset:6144\n\t"
      "ds_read_b64_tr_b16 v[190:191], v233 offset:6656\n\t"
      "ds_read_b64_tr_b16 v[192:193], v233 offset:8192\n\t"
      "ds_read_b64_tr_b16 v[194:195], v233 offset:8704\n\t"
      "buffer_store_dwordx4 v[220:223], v240, s[44:47], s52 offen offset:1024 sc1\n\t"
      "Lhdone%=:\n\t"
      "v_swap_b32 v232, v233\n\t"
      "v_add_u32 v236, 49152, v232\n\t"
      "s_mov_b32 s52, s4\n\t"
      "v_mov_b32 v230, v237\n\t"
      "Ll2%=:\n\t"
      "s_setprio 3\n\t"
      "s_waitcnt lgkmcnt(8)\n\t"
      "v_mfma_f32_32x32x16_bf16 v[144:159], v[176:179], v[0:3], 0\n\t"
      "ds_read_b64_tr_b16 v[196:197], v232 offset:10240\n\t"
      "ds_read_b64_tr_b16 v[198:199], v232 offset:10752\n\t"
      "s_and_b32 s54, s62, 255\n\t"
      "s_and_b32 s55, s64, 255\n\t"
      "s_lshr_b64 s[62:63], s[62:63], 8\n\t"
      "s_lshr_b64 s[64:65], s[64:65], 8\n\t"
      "s_cmp_eq_u32 s55, 255\n\t"
      "s_cselect_b32 s55, 0x40000000, 64\n\t"
      "s_cmp_lt_u32 s54, 144\n\t"
      "s_cselect_b32 s40, s72, s70\n\t"
      "s_cselect_b32 s41, s73, s71\n\t"
      "s_cselect_b32 s42, 0x510000, s66\n\t"
      "s_cselect_b32 s49, 73728, s67\n\t"
      "s_cselect_b32 s48, s36, s37\n\t"
      "s_cselect_b64 vcc, -1, 0\n\t"
      "v_cndmask_b32 v234, v242, v241, vcc\n\t"
      "s_cmp_eq_u32 s54, 255\n\t"
      "s_cselect_b32 s42, 0, s42\n\t"
      "s_lshl_b32 s54, s54, 6\n\t"
      "s_add_u32 s48, s48, s54\n\t"
      "v_and_b32 v223, s55, v243\n\t"
      "v_add_u32 v234, v234, v223\n\t"
      "s_mov_b32 m0, s50\n\t"
      "s_add_u32 s50, s50, 1024\n\t"
      "buffer_load_dwordx4 v234, s[40:43], s48 offen lds\n\t"
      "s_add_u32 s48, s48, s49\n\t"
      "s_waitcnt lgkmcnt(8)\n\t"
      "v_mfma_f32_32x32x16_bf16 v[144:159], v[180:183], v[4:7], v[144:159]\n\t"
      "ds_read_b64_tr_b16 v[176:177], v232 offset:12288\n\t"
      "ds_read_b64_tr_b16 v[178:179], v232 offset:12800\n\t"
      "v_mul_f32 v160, v160, v230\n\t"
      "s_waitcnt lgkmcnt(8)\n\t"
      "v_mfma_f32_32x32x16_bf16 v[144:159], v[184:187], v[8:11], v[144:159]\n\t"
      "ds_read_b64_tr_b16 v[180:181], v232 offset:14336\n\t"
      "ds_read_b64_tr_b16 v[182:183], v232 offset:14848\n\t"
      "s_setprio 2\n\t"
      "s_waitcnt lgkmcnt(8)\n\t"
      "v_mfma_f32_32x32x16_bf16 v[144:159], v[188:191], v[12:15], v[144:159]\n\t"
      "ds_read_b64_tr_b16 v[184:185], v232 offset:16384\n\t"
      "ds_read_b64_tr_b16 v[186:187], v232 offset:16896\n\t"
      "v_mul_f32 v161, v161, v230\n\t"
      "s_waitcnt lgkmcnt(8)\n\t"
      "v_mfma_f32_32x32x16_bf16 v[144:159], v[192:195], v[16:19], v[144:159]\n\t"
      "ds_read_b64_tr_b16 v[188:189], v232 offset:18432\n\t"
      "ds_read_b64_tr_b16 v[190:191], v232 offset:18944\n\t"
      "v_cvt_pk_bf16_f32 v216, v160, v161\n\t"
      "s_mov_b32 m0, s50\n\t"
      "s_add_u32 s50, s50, 1024\n\t"
      "buffer_load_dwordx4 v234, s[40:43], s48 offen lds\n\t"
      "s_add_u32 s48, s48, s49\n\t"
      "s_waitcnt lgkmcnt(8)\n\t"
      "v_mfma_f32_32x32x16_bf16 v[144:159], v[196:199], v[20:23], v[144:159]\n\t"
      "ds_read_b64_tr_b16 v[192:193], v232 offset:20480\n\t"
      "ds_read_b64_tr_b16 v[194:195], v232 offset:20992\n\t"
      "s_setprio 1\n\t"
      "s_waitcnt lgkmcnt(8)\n\t"
      "v_mfma_f32_32x32x16_bf16 v[144:159], v[176:179], v[24:27], v[144:159]\n\t"
      "ds_read_b64_tr_b16 v[196:197], v232 offset:22528\n\t"
      "ds_read_b64_tr_b16 v[198:199], v232 offset:23040\n\t"
      "v_mul_f32 v162, v162, v230\n\t"
      "s_waitcnt vmcnt(6)\n\t"
      "s_barrier\n\t"
      "s_waitcnt lgkmcnt(8)\n\t"
      "v_mfma_f32_32x32x16_bf16 v[144:159], v[180:183], v[28:31], v[144:159]\n\t"
      "ds_read_b64_tr_b16 v[176:177], v232 offset:24576\n\t"
      "ds_read_b64_tr_b16 v[178:179], v232 offset:25088\n\t"
      "v_mul_f32 v163, v163, v230\n\t"
      "s_waitcnt lgkmcnt(8)\n\t"
      "v_mfma_f32_32x32x16_bf16 v[144:159], v[184:187], v[32:35], v[144:159]\n\t"
      "ds_read_b64_tr_b16 v[180:181], v232 offset:26624\n\t"
      "ds_read_b64_tr_b16 v[182:183], v232 offset:27136\n\t"
      "s_mov_b32 m0, s50\n\t"
      "s_add_u32 s50, s50, 1024\n\t"
      "buffer_load_dwordx4 v234, s[40:43], s48 offen lds\n\t"
      "s_add_u32 s48, s48, s49\n\t"
      "s_mul_i32 s54, s49, 21\n\t"
      "s_add_u32 s48, s48, s54\n\t"
      "s_add_u32 s50, s50, 21504\n\t"
      "s_cmp_ge_u32 s50, 147456\n\t"
      "s_cselect_b32 s50, s38, s50\n\t"
      "s_setprio 0\n\t"
      "s_waitcnt lgkmcnt(8)\n\t"
      "v_mfma_f32_32x32x16_bf16 v[144:159], v[188:191], v[36:39], v[144:159]\n\t"
      "ds_read_b64_tr_b16 v[184:185], v232 offset:28672\n\t"
      "ds_read_b64_tr_b16 v[186:187], v232 offset:29184\n\t"
      "v_cvt_pk_bf16_f32 v217, v162, v163\n\t"
      "s_waitcnt lgkmcnt(8)\n\t"
      "v_mfma_f32_32x32x16_bf16 v[144:159], v[192:195], v[40:43], v[144:159]\n\t"
      "ds_read_b64_tr_b16 v[188:189], v232 offset:30720\n\t"
      "ds_read_b64_tr_b16 v[190:191], v232 offset:31232\n\t"
      "v_mul_f32 v164, v164, v230\n\t"
      "s_waitcnt lgkmcnt(8)\n\t"
      "v_mfma_f32_32x32x16_bf16 v[144:159], v[196:199], v[44:47], v[144:159]\n\t"
      "ds_read_b64_tr_b16 v[192:193], v232 offset:32768\n\t"
      "ds_read_b64_tr_b16 v[194:195], v232 offset:33280\n\t"
      "s_setprio 3\n\t"
      "s_waitcnt lgkmcnt(8)\n\t"
      "v_mfma_f32_32x32x16_bf16 v[144:159], v[176:179], v[48:51], v[144:159]\n\t"
      "ds_read_b64_tr_b16 v[196:197], v232 offset:34816\n\t"
      "ds_read_b64_tr_b16 v[198:199], v232 offset:35328\n\t"
      "v_mul_f32 v165, v165, v230\n\t"
      "s_mov_b32 m0, s50\n\t"
      "s_add_u32 s50, s50, 1024\n\t"
      "buffer_load_dwordx4 v234, s[40:43], s48 offen lds\n\t"
      "s_add_u32 s48, s48, s49\n\t"
      "s_waitcnt lgkmcnt(8)\n\t"
      "v_mfma_f32_32x32x16_bf16 v[144:159], v[180:183], v[52:55], v[144:159]\n\t"
      "ds_read_b64_tr_b16 v[176:177], v232 offset:36864\n\t"
      "ds_read_b64_tr_b16 v[178:179], v232 offset:37376\n\t"
      "v_cvt_pk_bf16_f32 v218, v164, v165\n\t"
      "s_waitcnt lgkmcnt(8)\n\t"
      "v_mfma_f32_32x32x16_bf16 v[144:159], v[184:187], v[56:59], v[144:159]\n\t"
      "ds_read_b64_tr_b16 v[180:181], v232 offset:38912\n\t"
      "ds_read_b64_tr_b16 v[182:183], v232 offset:39424\n\t"
      "s_setprio 2\n\t"
      "s_waitcnt lgkmcnt(8)\n\t"
      "v_mfma_f32_32x32x16_bf16 v[144:159], v[188:191], v[60:63], v[144:159]\n\t"
      "ds_read_b64_tr_b16 v[184:185], v232 offset:40960\n\t"
      "ds_read_b64_tr_b16 v[186:187], v232 offset:41472\n\t"
      "v_mul_f32 v166, v166, v230\n\t"
      "s_waitcnt lgkmcnt(8)\n\t"
      "v_mfma_f32_32x32x16_bf16 v[144:159], v[192:195], v[64:67], v[144:159]\n\t"
      "ds_read_b64_tr_b16 v[188:189], v232 offset:43008\n\t"
      "ds_read_b64_tr_b16 v[190:191], v232 offset:43520\n\t"
      "v_mul_f32 v167, v167, v230\n\t"
      "s_mov_b32 m0, s50\n\t"
      "s_add_u32 s50, s50, 1024\n\t"
      "buffer_load_dwordx4 v234, s[40:43], s48 offen lds\n\t"
      "s_add_u32 s48, s48, s49\n\t"
      "s_waitcnt lgkmcnt(8)\n\t"
      "v_mfma_f32_32x32x16_bf16 v[144:159], v[196:199], v[68:71], v[144:159]\n\t"
      "ds_read_b64_tr_b16 v[192:193], v232 offset:45056\n\t"
      "ds_read_b64_tr_b16 v[194:195], v232 offset:45568\n\t"
      "s_setprio 1\n\t"
      "s_waitcnt lgkmcnt(8)\n\t"
      "v_mfma_f32_32x32x16_bf16 v[144:159], v[176:179], v[72:75], v[144:159]\n\t"
      "ds_read_b64_tr_b16 v[196:197], v232 offset:47104\n\t"
      "ds_read_b64_tr_b16 v[198:199], v232 offset:47616\n\t"
      "v_cvt_pk_bf16_f32 v219, v166, v167\n\t"
      "buffer_store_dwordx4 v[216:219], v240, s[44:47], s52 offen offset:0 sc1\n\t"
      "s_waitcnt vmcnt(7)\n\t"
      "s_barrier\n\t"
      "s_waitcnt lgkmcnt(8)\n\t"
      "v_mfma_f32_32x32x16_bf16 v[144:159], v[180:183], v[76:79], v[144:159]\n\t"
      "ds_read_b64_tr_b16 v[176:177], v236 offset:0\n\t"
      "ds_read_b64_tr_b16 v[178:179], v236 offset:512\n\t"
      "v_mul_f32 v168, v168, v230\n\t"
      "s_waitcnt lgkmcnt(8)\n\t"
      "v_mfma_f32_32x32x16_bf16 v[144:159], v[184:187], v[80:83], v[144:159]\n\t"
      "ds_read_b64_tr_b16 v[180:181], v236 offset:2048\n\t"
      "ds_read_b64_tr_b16 v[182:183], v236 offset:2560\n\t"
      "v_mul_f32 v169, v169, v230\n\t"
      "s_mov_b32 m0, s50\n\t"
      "s_add_u32 s50, s50, 1024\n\t"
      "buffer_load_dwordx4 v234, s[40:43], s48 offen lds\n\t"
      "s_add_u32 s48, s48, s49\n\t"
      "s_mul_i32 s54, s49, 21\n\t"
      "s_add_u32 s48, s48, s54\n\t"
      "s_add_u32 s50, s50, 21504\n\t"
      "s_cmp_ge_u32 s50, 147456\n\t"
      "s_cselect_b32 s50, s38, s50\n\t"
      "s_setprio 0\n\t"
      "s_waitcnt lgkmcnt(8)\n\t"
      "v_mfma_f32_32x32x16_bf16 v[144:159], v[188:191], v[84:87], v[144:159]\n\t"
      "ds_read_b64_tr_b16 v[184:185], v236 offset:4096\n\t"
      "ds_read_b64_tr_b16 v[186:187], v236 offset:4608\n\t"
      "v_cvt_pk_bf16_f32 v220, v168, v169\n\t"
      "s_waitcnt lgkmcnt(8)\n\t"
      "v_mfma_f32_32x32x16_bf16 v[144:159], v[192:195], v[88:91], v[144:159]\n\t"
      "ds_read_b64_tr_b16 v[188:189], v236 offset:6144\n\t"
      "ds_read_b64_tr_b16 v[190:191], v236 offset:6656\n\t"
      "v_mul_f32 v170, v170, v230\n\t"
      "s_waitcnt lgkmcnt(8)\n\t"
      "v_mfma_f32_32x32x16_bf16 v[144:159], v[196:199], v[92:95], v[144:159]\n\t"
      "ds_read_b64_tr_b16 v[192:193], v236 offset:8192\n\t"
      "ds_read_b64_tr_b16 v[194:195], v236 offset:8704\n\t"
      "v_mul_f32 v171, v171, v230\n\t"
      "s_setprio 3\n\t"
      "s_waitcnt lgkmcnt(8)\n\t"
      "v_mfma_f32_32x32x16_bf16 v[144:159], v[176:179], v[96:99], v[144:159]\n\t"
      "ds_read_b64_tr_b16 v[196:197], v236 offset:10240\n\t"
      "ds_read_b64_tr_b16 v[198:199], v236 offset:10752\n\t"
      "v_cvt_pk_bf16_f32 v221, v170, v171\n\t"
      "s_mov_b32 m0, s50\n\t"
      "s_add_u32 s50, s50, 1024\n\t"
      "buffer_load_dwordx4 v234, s[40:43], s48 offen lds\n\t"
      "s_add_u32 s48, s48, s49\n\t"
      "s_waitcnt lgkmcnt(8)\n\t"
      "v_mfma_f32_32x32x16_bf16 v[144:159], v[180:183], v[100:103], v[144:159]\n\t"
      "ds_read_b64_tr_b16 v[176:177], v236 offset:12288\n\t"
      "ds_read_b64_tr_b16 v[178:179], v236 offset:12800\n\t"
      "v_mul_f32 v172, v172, v230\n\t"
      "s_waitcnt lgkmcnt(8)\n\t"
      "v_mfma_f32_32x32x16_bf16 v[144:159], v[184:187], v[104:107], v[144:159]\n\t"
      "ds_read_b64_tr_b16 v[180:181], v236 offset:14336\n\t"
      "ds_read_b64_tr_b16 v[182:183], v236 offset:14848\n\t"
      "v_mul_f32 v173, v173, v230\n\t"
      "s_setprio 2\n\t"
      "s_waitcnt lgkmcnt(8)\n\t"
      "v_mfma_f32_32x32x16_bf16 v[144:159], v[188:191], v[108:111], v[144:159]\n\t"
      "ds_read_b64_tr_b16 v[184:185], v236 offset:16384\n\t"
      "ds_read_b64_tr_b16 v[186:187], v236 offset:16896\n\t"
      "v_cvt_pk_bf16_f32 v222, v172, v173\n\t"
      "s_waitcnt lgkmcnt(8)\n\t"
      "v_mfma_f32_32x32x16_bf16 v[144:159], v[192:195], v[112:115], v[144:159]\n\t"
      "ds_read_b64_tr_b16 v[188:189], v236 offset:18432\n\t"
      "ds_read_b64_tr_b16 v[190:191], v236 offset:18944\n\t"
      "v_mul_f32 v174, v174, v230\n\t"
      "s_mov_b32 m0, s50\n\t"
      "s_add_u32 s50, s50, 1024\n\t"
      "buffer_load_dwordx4 v234, s[40:43], s48 offen lds\n\t"
      "s_add_u32 s48, s48, s49\n\t"
      "s_waitcnt lgkmcnt(8)\n\t"
      "v_mfma_f32_32x32x16_bf16 v[144:159], v[196:199], v[116:119], v[144:159]\n\t"
      "ds_read_b64_tr_b16 v[192:193], v236 offset:20480\n\t"
      "ds_read_b64_tr_b16 v[194:195], v236 offset:20992\n\t"
      "v_mul_f32 v175, v175, v230\n\t"
      "s_setprio 1\n\t"
      "s_waitcnt lgkmcnt(8)\n\t"
      "v_mfma_f32_32x32x16_bf16 v[144:159], v[176:179], v[120:123], v[144:159]\n\t"
      "ds_read_b64_tr_b16 v[196:197], v236 offset:22528\n\t"
      "ds_read_b64_tr_b16 v[198:199], v236 offset:23040\n\t"
      "v_cvt_pk_bf16_f32 v223, v174, v175\n\t"
      "ds_read_b128 v[200:203], v235 offset:0\n\t"
      "ds_read_b128 v[204:207], v235 offset:32\n\t"
      "ds_read_b128 v[208:211], v235 offset:64\n\t"
      "ds_read_b128 v[212:215], v235 offset:96\n\t"
      "s_waitcnt vmcnt(6)\n\t"
      "s_barrier\n\t"
      "s_waitcnt lgkmcnt(12)\n\t"
      "v_mfma_f32_32x32x16_bf16 v[144:159], v[180:183], v[124:127], v[144:159]\n\t"
      "ds_read_b64_tr_b16 v[176:177], v233 offset:0\n\t"
      "ds_read_b64_tr_b16 v[178:179], v233 offset:512\n\t"
      "s_waitcnt lgkmcnt(12)\n\t"
      "v_mfma_f32_32x32x16_bf16 v[144:159], v[184:187], v[128:131], v[144:159]\n\t"
      "ds_read_b64_tr_b16 v[180:181], v233 offset:2048\n\t"
      "ds_read_b64_tr_b16 v[182:183], v233 offset:2560\n\t"
      "s_mov_b32 m0, s50\n\t"
      "s_add_u32 s50, s50, 1024\n\t"
      "buffer_load_dwordx4 v234, s[40:43], s48 offen lds\n\t"
      "s_add_u32 s48, s48, s49\n\t"
      "s_mul_i32 s54, s49, 21\n\t"
      "s_add_u32 s48, s48, s54\n\t"
      "s_add_u32 s50, s50, 21504\n\t"
      "s_cmp_ge_u32 s50, 147456\n\t"
      "s_cselect_b32 s50, s38, s50\n\t"
      "s_setprio 0\n\t"
      "s_waitcnt lgkmcnt(12)\n\t"
      "v_mfma_f32_32x32x16_bf16 v[144:159], v[188:191], v[132:135], v[144:159]\n\t"
      "ds_read_b64_tr_b16 v[184:185], v233 offset:4096\n\t"
      "ds_read_b64_tr_b16 v[186:187], v233 offset:4608\n\t"
      "s_waitcnt lgkmcnt(12)\n\t"
      "v_mfma_f32_32x32x16_bf16 v[144:159], v[192:195], v[136:139], v[144:159]\n\t"
      "ds_read_b64_tr_b16 v[188:189], v233 offset:6144\n\t"
      "ds_read_b64_tr_b16 v[190:191], v233 offset:6656\n\t"
      "s_waitcnt lgkmcnt(12)\n\t"
      "v_mfma_f32_32x32x16_bf16 v[160:175], v[196:199], v[140:143], v[144:159]\n\t"
      "ds_read_b64_tr_b16 v[192:193], v233 offset:8192\n\t"
      "ds_read_b64_tr_b16 v[194:195], v233 offset:8704\n\t"
      "buffer_store_dwordx4 v[220:223], v240, s[44:47], s52 offen offset:1024 sc1\n\t"
      "v_swap_b32 v232, v233\n\t"
      "v_add_u32 v236, 49152, v232\n\t"
      "Lnol2%=:\n\t"
      "s_cmp_eq_u32 s12, 0\n\t"
      "s_cselect_b32 s53, 3, 2\n\t"
      "s_cmp_lt_u32 s39, 2\n\t"
      "s_cselect_b32 s57, 0xffff, 1\n\t"
      "s_cmp_lg_u32 s26, 0\n\t"
      "s_cselect_b32 s57, s32, s57\n\t"
      "Lk1loop%=:\n\t"
      "s_lshl_b32 s54, s51, 12\n\t"
      "s_add_u32 s52, s10, s54\n\t"
      "s_add_u32 s51, s51, 1\n\t"
      "s_cmp_ge_u32 s51, s9\n\t"
      "s_cselect_b32 s51, 0, s51\n\t"
      "s_lshl_b32 s54, s51, 8\n\t"
      "v_add_u32 v235, s54, v245\n\t"
      "s_setprio 3\n\t"
      "s_waitcnt lgkmcnt(8)\n\t"
      "v_mfma_f32_32x32x16_bf16 v[144:159], v[176:179], v[0:3], 0\n\t"
      "ds_read_b64_tr_b16 v[196:197], v232 offset:10240\n\t"
      "ds_read_b64_tr_b16 v[198:199], v232 offset:10752\n\t"
      "s_and_b32 s54, s62, 255\n\t"
      "s_and_b32 s55, s64, 255\n\t"
      "s_lshr_b64 s[62:63], s[62:63], 8\n\t"
      "s_lshr_b64 s[64:65], s[64:65], 8\n\t"
      "s_cmp_eq_u32 s55, 255\n\t"
      "s_cselect_b32 s55, 0x40000000, 64\n\t"
      "s_cmp_lt_u32 s54, 144\n\t"
      "s_cselect_b32 s40, s72, s70\n\t"
      "s_cselect_b32 s41, s73, s71\n\t"
      "s_cselect_b32 s42, 0x510000, s66\n\t"
      "s_cselect_b32 s49, 73728, s67\n\t"
      "s_cselect_b32 s48, s36, s37\n\t"
      "s_cselect_b64 vcc, -1, 0\n\t"
      "v_cndmask_b32 v234, v242, v241, vcc\n\t"
      "s_cmp_eq_u32 s54, 255\n\t"
      "s_cselect_b32 s42, 0, s42\n\t"
      "s_lshl_b32 s54, s54, 6\n\t"
      "s_add_u32 s48, s48, s54\n\t"
      "v_and_b32 v223, s55, v243\n\t"
      "v_add_u32 v234, v234, v223\n\t"
      "s_mov_b32 m0, s50\n\t"
      "s_add_u32 s50, s50, 1024\n\t"
      "buffer_load_dwordx4 v234, s[40:43], s48 offen lds\n\t"
      "s_add_u32 s48, s48, s49\n\t"
      "s_waitcnt lgkmcnt(8)\n\t"
      "v_mfma_f32_32x32x16_bf16 v[144:159], v[180:183], v[4:7], v[144:159]\n\t"
      "ds_read_b64_tr_b16 v[176:177], v232 offset:12288\n\t"
      "ds_read_b64_tr_b16 v[178:179], v232 offset:12800\n\t"
      "v_fma_f32 v160, v160, v237, v200\n\t"
      "v_mul_f32 v224, v160, v160\n\t"
      "v_fma_f32 v224, v224, s58, v244\n\t"
      "v_mul_f32 v224, v160, v224\n\t"
      "v_exp_f32 v224, v224\n\t"
      "s_waitcnt lgkmcnt(8)\n\t"
      "v_mfma_f32_32x32x16_bf16 v[144:159], v[184:187], v[8:11], v[144:159]\n\t"
      "ds_read_b64_tr_b16 v[180:181], v232 offset:14336\n\t"
      "ds_read_b64_tr_b16 v[182:183], v232 offset:14848\n\t"
      "v_fma_f32 v161, v161, v237, v201\n\t"
      "v_add_f32 v224, 1.0, v224\n\t"
      "v_mul_f32 v225, v161, v161\n\t"
      "v_rcp_f32 v224, v224\n\t"
      "s_setprio 2\n\t"
      "s_waitcnt lgkmcnt(8)\n\t"
      "v_mfma_f32_32x32x16_bf16 v[144:159], v[188:191], v[12:15], v[144:159]\n\t"
      "ds_read_b64_tr_b16 v[184:185], v232 offset:16384\n\t"
      "ds_read_b64_tr_b16 v[186:187], v232 offset:16896\n\t"
      "v_fma_f32 v225, v225, s58, v244\n\t"
      "v_mul_f32 v225, v161, v225\n\t"
      "v_mul_f32 v160, v160, v224\n\t"
      "v_exp_f32 v225, v225\n\t"
      "s_waitcnt lgkmcnt(8)\n\t"
      "v_mfma_f32_32x32x16_bf16 v[144:159], v[192:195], v[16:19], v[144:159]\n\t"
      "ds_read_b64_tr_b16 v[188:189], v232 offset:18432\n\t"
      "ds_read_b64_tr_b16 v[190:191], v232 offset:18944\n\t"
      "v_fma_f32 v162, v162, v237, v202\n\t"
      "v_add_f32 v225, 1.0, v225\n\t"
      "v_mul_f32 v226, v162, v162\n\t"
      "v_rcp_f32 v225, v225\n\t"
      "s_mov_b32 m0, s50\n\t"
      "s_add_u32 s50, s50, 1024\n\t"
      "buffer_load_dwordx4 v234, s[40:43], s48 offen lds\n\t"
      "s_add_u32 s48, s48, s49\n\t"
      "s_waitcnt lgkmcnt(8)\n\t"
      "v_mfma_f32_32x32x16_bf16 v[144:159], v[196:199], v[20:23], v[144:159]\n\t"
      "ds_read_b64_tr_b16 v[192:193], v232 offset:20480\n\t"
      "ds_read_b64_tr_b16 v[194:195], v232 offset:20992\n\t"
      "v_fma_f32 v226, v226, s58, v244\n\t"
      "v_mul_f32 v226, v162, v226\n\t"
      "v_mul_f32 v161, v161, v225\n\t"
      "v_exp_f32 v226, v226\n\t"
      "s_setprio 1\n\t"
      "s_waitcnt lgkmcnt(8)\n\t"
      "v_mfma_f32_32x32x16_bf16 v[144:159], v[176:179], v[24:27], v[144:159]\n\t"
      "ds_read_b64_tr_b16 v[196:197], v232 offset:22528\n\t"
      "ds_read_b64_tr_b16 v[198:199], v232 offset:23040\n\t"
      "v_cvt_pk_bf16_f32 v216, v160, v161\n\t"
      "v_fma_f32 v163, v163, v237, v203\n\t"
      "ds_read_b128 v[200:203], v235 offset:0\n\t"
      "v_add_f32 v226, 1.0, v226\n\t"
      "v_mul_f32 v227, v163, v163\n\t"
      "s_waitcnt vmcnt(6)\n\t"
      "s_barrier\n\t"
      "s_waitcnt lgkmcnt(9)\n\t"
      "v_mfma_f32_32x32x16_bf16 v[144:159], v[180:183], v[28:31], v[144:159]\n\t"
      "ds_read_b64_tr_b16 v[176:177], v232 offset:24576\n\t"
      "ds_read_b64_tr_b16 v[178:179], v232 offset:25088\n\t"
      "v_rcp_f32 v226, v226\n\t"
      "v_fma_f32 v227, v227, s58, v244\n\t"
      "v_mul_f32 v227, v163, v227\n\t"
      "v_mul_f32 v162, v162, v226\n\t"
      "s_waitcnt lgkmcnt(9)\n\t"
      "v_mfma_f32_32x32x16_bf16 v[144:159], v[184:187], v[32:35], v[144:159]\n\t"
      "ds_read_b64_tr_b16 v[180:181], v232 offset:26624\n\t"
      "ds_read_b64_tr_b16 v[182:183], v232 offset:27136\n\t"
      "v_exp_f32 v227, v227\n\t"
      "v_fma_f32 v164, v164, v237, v204\n\t"
      "v_add_f32 v227, 1.0, v227\n\t"
      "v_mul_f32 v224, v164, v164\n\t"
      "s_mov_b32 m0, s50\n\t"
      "s_add_u32 s50, s50, 1024\n\t"
      "buffer_load_dwordx4 v234, s[40:43], s48 offen lds\n\t"
      "s_add_u32 s48, s48, s49\n\t"
      "s_mul_i32 s54, s49, 21\n\t"
      "s_add_u32 s48, s48, s54\n\t"
      "s_add_u32 s50, s50, 21504\n\t"
      "s_cmp_ge_u32 s50, 147456\n\t"
      "s_cselect_b32 s50, s38, s50\n\t"
      "s_setprio 0\n\t"
      "s_waitcnt lgkmcnt(9)\n\t"
      "v_mfma_f32_32x32x16_bf16 v[144:159], v[188:191], v[36:39], v[144:159]\n\t"
      "ds_read_b64_tr_b16 v[184:185], v232 offset:28672\n\t"
      "ds_read_b64_tr_b16 v[186:187], v232 offset:29184\n\t"
      "v_rcp_f32 v227, v227\n\t"
      "v_fma_f32 v224, v224, s58, v244\n\t"
      "v_mul_f32 v224, v164, v224\n\t"
      "v_mul_f32 v163, v163, v227\n\t"
      "buffer_load_dwordx4 v[248:251], v240, s[92:95], s60 offen\n\t"
      "s_add_u32 s60, s60, 1024\n\t"
      "s_waitcnt lgkmcnt(9)\n\t"
      "v_mfma_f32_32x32x16_bf16 v[144:159], v[192:195], v[40:43], v[144:159]\n\t"
      "ds_read_b64_tr_b16 v[188:189], v232 offset:30720\n\t"
      "ds_read_b64_tr_b16 v[190:191], v232 offset:31232\n\t"
      "v_exp_f32 v224, v224\n\t"
      "v_cvt_pk_bf16_f32 v217, v162, v163\n\t"
      "v_fma_f32 v165, v165, v237, v205\n\t"
      "v_add_f32 v224, 1.0, v224\n\t"
      "s_waitcnt lgkmcnt(9)\n\t"
      "v_mfma_f32_32x32x16_bf16 v[144:159], v[196:199], v[44:47], v[144:159]\n\t"
      "ds_read_b64_tr_b16 v[192:193], v232 offset:32768\n\t"
      "ds_read_b64_tr_b16 v[194:195], v232 offset:33280\n\t"
      "v_mul_f32 v225, v165, v165\n\t"
      "v_rcp_f32 v224, v224\n\t"
      "v_fma_f32 v225, v225, s58, v244\n\t"
      "v_mul_f32 v225, v165, v225\n\t"
      "s_setprio 3\n\t"
      "s_waitcnt lgkmcnt(8)\n\t"
      "v_mfma_f32_32x32x16_bf16 v[144:159], v[176:179], v[48:51], v[144:159]\n\t"
      "ds_read_b64_tr_b16 v[196:197], v232 offset:34816\n\t"
      "ds_read_b64_tr_b16 v[198:199], v232 offset:35328\n\t"
      "v_mul_f32 v164, v164, v224\n\t"
      "v_exp_f32 v225, v225\n\t"
      "v_fma_f32 v166, v166, v237, v206\n\t"
      "v_add_f32 v225, 1.0, v225\n\t"
      "s_mov_b32 m0, s50\n\t"
      "s_add_u32 s50, s50, 1024\n\t"
      "buffer_load_dwordx4 v234, s[40:43], s48 offen lds\n\t"
      "s_add_u32 s48, s48, s49\n\t"
      "s_waitcnt lgkmcnt(8)\n\t"
      "v_mfma_f32_32x32x16_bf16 v[144:159], v[180:183], v[52:55], v[144:159]\n\t"
      "ds_read_b64_tr_b16 v[176:177], v232 offset:36864\n\t"
      "ds_read_b64_tr_b16 v[178:179], v232 offset:37376\n\t"
      "v_mul_f32 v226, v166, v166\n\t"
      "v_rcp_f32 v225, v225\n\t"
      "v_fma_f32 v226, v226, s58, v244\n\t"
      "v_mul_f32 v226, v166, v226\n\t"
      "s_waitcnt lgkmcnt(8)\n\t"
      "v_mfma_f32_32x32x16_bf16 v[144:159], v[184:187], v[56:59], v[144:159]\n\t"
      "ds_read_b64_tr_b16 v[180:181], v232 offset:38912\n\t"
      "ds_read_b64_tr_b16 v[182:183], v232 offset:39424\n\t"
      "v_mul_f32 v165, v165, v225\n\t"
      "v_exp_f32 v226, v226\n\t"
      "v_cvt_pk_bf16_f32 v218, v164, v165\n\t"
      "v_fma_f32 v167, v167, v237, v207\n\t"
      "ds_read_b128 v[204:207], v235 offset:32\n\t"
      "s_setprio 2\n\t"
      "s_waitcnt lgkmcnt(9)\n\t"
      "v_mfma_f32_32x32x16_bf16 v[144:159], v[188:191], v[60:63], v[144:159]\n\t"
      "ds_read_b64_tr_b16 v[184:185], v232 offset:40960\n\t"
      "ds_read_b64_tr_b16 v[186:187], v232 offset:41472\n\t"
      "v_add_f32 v226, 1.0, v226\n\t"
      "v_mul_f32 v227, v167, v167\n\t"
      "v_rcp_f32 v226, v226\n\t"
      "v_fma_f32 v227, v227, s58, v244\n\t"
      "s_waitcnt lgkmcnt(9)\n\t"
      "v_mfma_f32_32x32x16_bf16 v[144:159], v[192:195], v[64:67], v[144:159]\n\t"
      "ds_read_b64_tr_b16 v[188:189], v232 offset:43008\n\t"
      "ds_read_b64_tr_b16 v[190:191], v232 offset:43520\n\t"
      "v_mul_f32 v227, v167, v227\n\t"
      "v_mul_f32 v166, v166, v226\n\t"
      "v_exp_f32 v227, v227\n\t"
      "v_fma_f32 v168, v168, v237, v208\n\t"
      "s_mov_b32 m0, s50\n\t"
      "s_add_u32 s50, s50, 1024\n\t"
      "buffer_load_dwordx4 v234, s[40:43], s48 offen lds\n\t"
      "s_add_u32 s48, s48, s49\n\t"
      "s_waitcnt lgkmcnt(9)\n\t"
      "v_mfma_f32_32x32x16_bf16 v[144:159], v[196:199], v[68:71], v[144:159]\n\t"
      "ds_read_b64_tr_b16 v[192:193], v232 offset:45056\n\t"
      "ds_read_b64_tr_b16 v[194:195], v232 offset:45568\n\t"
      "v_add_f32 v227, 1.0, v227\n\t"
      "v_mul_f32 v224, v168, v168\n\t"
      "v_rcp_f32 v227, v227\n\t"
      "v_fma_f32 v224, v224, s58, v244\n\t"
      "s_setprio 1\n\t"
      "s_waitcnt lgkmcnt(9)\n\t"
      "v_mfma_f32_32x32x16_bf16 v[144:159], v[176:179], v[72:75], v[144:159]\n\t"
      "ds_read_b64_tr_b16 v[196:197], v232 offset:47104\n\t"
      "ds_read_b64_tr_b16 v[198:199], v232 offset:47616\n\t"
      "v_mul_f32 v224, v168, v224\n\t"
      "v_mul_f32 v167, v167, v227\n\t"
      "v_exp_f32 v224, v224\n\t"
      "v_cvt_pk_bf16_f32 v219, v166, v167\n\t"
      "buffer_store_dwordx4 v[216:219], v240, s[44:47], s52 offen offset:0 sc1\n\t"
      "s_waitcnt vmcnt(8)\n\t"
      "s_barrier\n\t"
      "s_waitcnt lgkmcnt(9)\n\t"
      "v_mfma_f32_32x32x16_bf16 v[144:159], v[180:183], v[76:79], v[144:159]\n\t"
      "ds_read_b64_tr_b16 v[176:177], v236 offset:0\n\t"
      "ds_read_b64_tr_b16 v[178:179], v236 offset:512\n\t"
      "v_fma_f32 v169, v169, v237, v209\n\t"
      "v_add_f32 v224, 1.0, v224\n\t"
      "v_mul_f32 v225, v169, v169\n\t"
      "v_rcp_f32 v224, v224\n\t"
      "s_waitcnt lgkmcnt(8)\n\t"
      "v_mfma_f32_32x32x16_bf16 v[144:159], v[184:187], v[80:83], v[144:159]\n\t"
      "ds_read_b64_tr_b16 v[180:181], v236 offset:2048\n\t"
      "ds_read_b64_tr_b16 v[182:183], v236 offset:2560\n\t"
      "v_fma_f32 v225, v225, s58, v244\n\t"
      "v_mul_f32 v225, v169, v225\n\t"
      "v_mul_f32 v168, v168, v224\n\t"
      "v_exp_f32 v225, v225\n\t"
      "s_mov_b32 m0, s50\n\t"
      "s_add_u32 s50, s50, 1024\n\t"
      "buffer_load_dwordx4 v234, s[40:43], s48 offen lds\n\t"
      "s_add_u32 s48, s48, s49\n\t"
      "s_mul_i32 s54, s49, 21\n\t"
      "s_add_u32 s48, s48, s54\n\t"
      "s_add_u32 s50, s50, 21504\n\t"
      "s_cmp_ge_u32 s50, 147456\n\t"
      "s_cselect_b32 s50, s38, s50\n\t"
      "s_setprio 0\n\t"
      "s_waitcnt lgkmcnt(8)\n\t"
      "v_mfma_f32_32x32x16_bf16 v[144:159], v[188:191], v[84:87], v[144:159]\n\t"
      "ds_read_b64_tr_b16 v[184:185], v236 offset:4096\n\t"
      "ds_read_b64_tr_b16 v[186:187], v236 offset:4608\n\t"
      "v_fma_f32 v170, v170, v237, v210\n\t"
      "v_add_f32 v225, 1.0, v225\n\t"
      "v_mul_f32 v226, v170, v170\n\t"
      "v_rcp_f32 v225, v225\n\t"
      "s_waitcnt lgkmcnt(8)\n\t"
      "v_mfma_f32_32x32x16_bf16 v[144:159], v[192:195], v[88:91], v[144:159]\n\t"
      "ds_read_b64_tr_b16 v[188:189], v236 offset:6144\n\t"
      "ds_read_b64_tr_b16 v[190:191], v236 offset:6656\n\t"
      "v_fma_f32 v226, v226, s58, v244\n\t"
      "v_mul_f32 v226, v170, v226\n\t"
      "v_mul_f32 v169, v169, v225\n\t"
      "v_exp_f32 v226, v226\n\t"
      "s_waitcnt lgkmcnt(8)\n\t"
      "v_mfma_f32_32x32x16_bf16 v[144:159], v[196:199], v[92:95], v[144:159]\n\t"
      "ds_read_b64_tr_b16 v[192:193], v236 offset:8192\n\t"
      "ds_read_b64_tr_b16 v[194:195], v236 offset:8704\n\t"
      "v_cvt_pk_bf16_f32 v220, v168, v169\n\t"
      "v_fma_f32 v171, v171, v237, v211\n\t"
      "ds_read_b128 v[208:211], v235 offset:64\n\t"
      "v_add_f32 v226, 1.0, v226\n\t"
      "v_mul_f32 v227, v171, v171\n\t"
      "s_setprio 3\n\t"
      "s_waitcnt lgkmcnt(9)\n\t"
      "v_mfma_f32_32x32x16_bf16 v[144:159], v[176:179], v[96:99], v[144:159]\n\t"
      "ds_read_b64_tr_b16 v[196:197], v236 offset:10240\n\t"
      "ds_read_b64_tr_b16 v[198:199], v236 offset:10752\n\t"
      "v_rcp_f32 v226, v226\n\t"
      "v_fma_f32 v227, v227, s58, v244\n\t"
      "v_mul_f32 v227, v171, v227\n\t"
      "v_mul_f32 v170, v170, v226\n\t"
      "s_mov_b32 m0, s50\n\t"
      "s_add_u32 s50, s50, 1024\n\t"
      "buffer_load_dwordx4 v234, s[40:43], s48 offen lds\n\t"
      "s_add_u32 s48, s48, s49\n\t"
      "s_waitcnt lgkmcnt(9)\n\t"
      "v_mfma_f32_32x32x16_bf16 v[144:159], v[180:183], v[100:103], v[144:159]\n\t"
      "ds_read_b64_tr_b16 v[176:177], v236 offset:12288\n\t"
      "ds_read_b64_tr_b16 v[178:179], v236 offset:12800\n\t"
      "v_exp_f32 v227, v227\n\t"
      "v_fma_f32 v172, v172, v237, v212\n\t"
      "v_add_f32 v227, 1.0, v227\n\t"
      "v_mul_f32 v224, v172, v172\n\t"
      "s_waitcnt lgkmcnt(9)\n\t"
      "v_mfma_f32_32x32x16_bf16 v[144:159], v[184:187], v[104:107], v[144:159]\n\t"
      "ds_read_b64_tr_b16 v[180:181], v236 offset:14336\n\t"
      "ds_read_b64_tr_b16 v[182:183], v236 offset:14848\n\t"
      "v_rcp_f32 v227, v227\n\t"
      "v_fma_f32 v224, v224, s58, v244\n\t"
      "v_mul_f32 v224, v172, v224\n\t"
      "v_mul_f32 v171, v171, v227\n\t"
      "s_setprio 2\n\t"
      "s_waitcnt lgkmcnt(9)\n\t"
      "v_mfma_f32_32x32x16_bf16 v[144:159], v[188:191], v[108:111], v[144:159]\n\t"
      "ds_read_b64_tr_b16 v[184:185], v236 offset:16384\n\t"
      "ds_read_b64_tr_b16 v[186:187], v236 offset:16896\n\t"
      "v_exp_f32 v224, v224\n\t"
      "v_cvt_pk_bf16_f32 v221, v170, v171\n\t"
      "v_fma_f32 v173, v173, v237, v213\n\t"
      "v_add_f32 v224, 1.0, v224\n\t"
      "s_waitcnt lgkmcnt(9)\n\t"
      "v_mfma_f32_32x32x16_bf16 v[144:159], v[192:195], v[112:115], v[144:159]\n\t"
      "ds_read_b64_tr_b16 v[188:189], v236 offset:18432\n\t"
      "ds_read_b64_tr_b16 v[190:191], v236 offset:18944\n\t"
      "v_mul_f32 v225, v173, v173\n\t"
      "v_rcp_f32 v224, v224\n\t"
      "v_fma_f32 v225, v225, s58, v244\n\t"
      "v_mul_f32 v225, v173, v225\n\t"
      "s_mov_b32 m0, s50\n\t"
      "s_add_u32 s50, s50, 1024\n\t"
      "buffer_load_dwordx4 v234, s[40:43], s48 offen lds\n\t"
      "s_add_u32 s48, s48, s49\n\t"
      "s_waitcnt lgkmcnt(8)\n\t"
      "v_mfma_f32_32x32x16_bf16 v[144:159], v[196:199], v[116:119], v[144:159]\n\t"
      "ds_read_b64_tr_b16 v[192:193], v236 offset:20480\n\t"
      "ds_read_b64_tr_b16 v[194:195], v236 offset:20992\n\t"
      "v_mul_f32 v172, v172, v224\n\t"
      "v_exp_f32 v225, v225\n\t"
      "v_fma_f32 v174, v174, v237, v214\n\t"
      "v_add_f32 v225, 1.0, v225\n\t"
      "s_setprio 1\n\t"
      "s_waitcnt lgkmcnt(8)\n\t"
      "v_mfma_f32_32x32x16_bf16 v[144:159], v[176:179], v[120:123], v[144:159]\n\t"
      "ds_read_b64_tr_b16 v[196:197], v236 offset:22528\n\t"
      "ds_read_b64_tr_b16 v[198:199], v236 offset:23040\n\t"
      "v_mul_f32 v226, v174, v174\n\t"
      "v_rcp_f32 v225, v225\n\t"
      "v_fma_f32 v226, v226, s58, v244\n\t"
      "v_mul_f32 v226, v174, v226\n\t"
      "s_waitcnt vmcnt(7)\n\t"
      "s_barrier\n\t"
      "s_mov_b64 exec, 1\n\t"
      "buffer_store_dword v238, off, s[44:47], s17 sc1\n\t"
      "s_mov_b64 exec, -1\n\t"
      "s_mov_b32 s17, 0x7ffffff0\n\t"
      "s_waitcnt lgkmcnt(8)\n\t"
      "v_mfma_f32_32x32x16_bf16 v[144:159], v[180:183], v[124:127], v[144:159]\n\t"
      "ds_read_b64_tr_b16 v[176:177], v233 offset:0\n\t"
      "ds_read_b64_tr_b16 v[178:179], v233 offset:512\n\t"
      "v_mul_f32 v173, v173, v225\n\t"
      "v_exp_f32 v226, v226\n\t"
      "v_cvt_pk_bf16_f32 v222, v172, v173\n\t"
      "s_waitcnt lgkmcnt(8)\n\t"
      "v_mfma_f32_32x32x16_bf16 v[144:159], v[184:187], v[128:131], v[144:159]\n\t"
      "ds_read_b64_tr_b16 v[180:181], v233 offset:2048\n\t"
      "ds_read_b64_tr_b16 v[182:183], v233 offset:2560\n\t"
      "v_fma_f32 v175, v175, v237, v215\n\t"
      "ds_read_b128 v[212:215], v235 offset:96\n\t"
      "v_add_f32 v226, 1.0, v226\n\t"
      "v_mul_f32 v227, v175, v175\n\t"
      "v_rcp_f32 v226, v226\n\t"
      "s_mov_b32 m0, s50\n\t"
      "s_add_u32 s50, s50, 1024\n\t"
      "buffer_load_dwordx4 v234, s[40:43], s48 offen lds\n\t"
      "s_add_u32 s48, s48, s49\n\t"
      "s_mul_i32 s54, s49, 21\n\t"
      "s_add_u32 s48, s48, s54\n\t"
      "s_add_u32 s50, s50, 21504\n\t"
      "s_cmp_ge_u32 s50, 147456\n\t"
      "s_cselect_b32 s50, s38, s50\n\t"
      "s_setprio 0\n\t"
      "s_waitcnt lgkmcnt(9)\n\t"
      "v_mfma_f32_32x32x16_bf16 v[144:159], v[188:191], v[132:135], v[144:159]\n\t"
      "ds_read_b64_tr_b16 v[184:185], v233 offset:4096\n\t"
      "ds_read_b64_tr_b16 v[186:187], v233 offset:4608\n\t"
      "v_fma_f32 v227, v227, s58, v244\n\t"
      "v_mul_f32 v227, v175, v227\n\t"
      "v_mul_f32 v174, v174, v226\n\t"
      "v_exp_f32 v227, v227\n\t"
      "s_waitcnt lgkmcnt(9)\n\t"
      "v_mfma_f32_32x32x16_bf16 v[144:159], v[192:195], v[136:139], v[144:159]\n\t"
      "ds_read_b64_tr_b16 v[188:189], v233 offset:6144\n\t"
      "ds_read_b64_tr_b16 v[190:191], v233 offset:6656\n\t"
      "v_add_f32 v227, 1.0, v227\n\t"
      "v_rcp_f32 v227, v227\n\t"
      "s_nop 0\n\t"
      "v_mul_f32 v175, v175, v227\n\t"
      "v_cvt_pk_bf16_f32 v223, v174, v175\n\t"
      "s_waitcnt lgkmcnt(9)\n\t"
      "v_mfma_f32_32x32x16_bf16 v[160:175], v[196:199], v[140:143], v[144:159]\n\t"
      "ds_read_b64_tr_b16 v[192:193], v233 offset:8192\n\t"
      "ds_read_b64_tr_b16 v[194:195], v233 offset:8704\n\t"
      "buffer_store_dwordx4 v[220:223], v240, s[44:47], s52 offen offset:1024 sc1\n\t"
      "v_swap_b32 v232, v233\n\t"
      "v_add_u32 v236, 49152, v232\n\t"
      "s_sub_u32 s53, s53, 1\n\t"
      "s_cmp_lg_u32 s53, 0\n\t"
      "s_cbranch_scc1 Lk1loop%=\n\t"
      "s_lshl_b32 s54, s51, 12\n\t"
      "s_add_u32 s52, s10, s54\n\t"
      "s_add_u32 s51, s51, 1\n\t"
      "s_cmp_ge_u32 s51, s9\n\t"
      "s_cselect_b32 s51, 0, s51\n\t"
      "s_lshl_b32 s54, s51, 8\n\t"
      "v_add_u32 v235, s54, v245\n\t"
      "v_mov_b32 v239, v238\n\t"
      "s_mov_b32 exec_lo, s57\n\t"
      "s_mov_b32 exec_hi, s26\n\t"
      "buffer_load_dword v239, v246, s[44:47], s25 offen sc1\n\t"
      "s_mov_b64 exec, -1\n\t"
      "s_setprio 3\n\t"
      "s_waitcnt lgkmcnt(8)\n\t"
      "v_mfma_f32_32x32x16_bf16 v[144:159], v[176:179], v[0:3], 0\n\t"
      "ds_read_b64_tr_b16 v[196:197], v232 offset:10240\n\t"
      "ds_read_b64_tr_b16 v[198:199], v232 offset:10752\n\t"
      "s_waitcnt lgkmcnt(8)\n\t"
      "v_mfma_f32_32x32x16_bf16 v[144:159], v[180:183], v[4:7], v[144:159]\n\t"
      "ds_read_b64_tr_b16 v[176:177], v232 offset:12288\n\t"
      "ds_read_b64_tr_b16 v[178:179], v232 offset:12800\n\t"
      "v_fma_f32 v160, v160, v237, v200\n\t"
      "v_mul_f32 v224, v160, v160\n\t"
      "v_fma_f32 v224, v224, s58, v244\n\t"
      "v_mul_f32 v224, v160, v224\n\t"
      "v_exp_f32 v224, v224\n\t"
      "s_waitcnt lgkmcnt(8)\n\t"
      "v_mfma_f32_32x32x16_bf16 v[144:159], v[184:187], v[8:11], v[144:159]\n\t"
      "ds_read_b64_tr_b16 v[180:181], v232 offset:14336\n\t"
      "ds_read_b64_tr_b16 v[182:183], v232 offset:14848\n\t"
      "v_fma_f32 v161, v161, v237, v201\n\t"
      "v_add_f32 v224, 1.0, v224\n\t"
      "v_mul_f32 v225, v161, v161\n\t"
      "v_rcp_f32 v224, v224\n\t"
      "s_setprio 2\n\t"
      "s_waitcnt lgkmcnt(8)\n\t"
      "v_mfma_f32_32x32x16_bf16 v[144:159], v[188:191], v[12:15], v[144:159]\n\t"
      "ds_read_b64_tr_b16 v[184:185], v232 offset:16384\n\t"
      "ds_read_b64_tr_b16 v[186:187], v232 offset:16896\n\t"
      "v_fma_f32 v225, v225, s58, v244\n\t"
      "v_mul_f32 v225, v161, v225\n\t"
      "v_mul_f32 v160, v160, v224\n\t"
      "v_exp_f32 v225, v225\n\t"
      "v_lshrrev_b32 v248, 3, v240\n\t"
      "v_and_b32 v248, 64, v248\n\t"
      "v_xor_b32 v248, v248, v240\n\t"
      "v_xor_b32 v249, 128, v248\n\t"
      "s_add_u32 s50, s20, s38\n\t"
      "s_mov_b32 s88, s86\n\t"
      "s_mov_b32 s89, s87\n\t"
      "s_mov_b32 s90, 33792\n\t"
      "s_waitcnt lgkmcnt(8)\n\t"
      "v_mfma_f32_32x32x16_bf16 v[144:159], v[192:195], v[16:19], v[144:159]\n\t"
      "ds_read_b64_tr_b16 v[188:189], v232 offset:18432\n\t"
      "ds_read_b64_tr_b16 v[190:191], v232 offset:18944\n\t"
      "v_fma_f32 v162, v162, v237, v202\n\t"
      "v_add_f32 v225, 1.0, v225\n\t"
      "v_mul_f32 v226, v162, v162\n\t"
      "v_rcp_f32 v225, v225\n\t"
      "buffer_load_dwordx4 v[8:11], v240, s[88:91], s35 offen offset:0\n\t"
      "s_waitcnt lgkmcnt(8)\n\t"
      "v_mfma_f32_32x32x16_bf16 v[144:159], v[196:199], v[20:23], v[144:159]\n\t"
      "ds_read_b64_tr_b16 v[192:193], v232 offset:20480\n\t"
      "ds_read_b64_tr_b16 v[194:195], v232 offset:20992\n\t"
      "v_fma_f32 v226, v226, s58, v244\n\t"
      "v_mul_f32 v226, v162, v226\n\t"
      "v_mul_f32 v161, v161, v225\n\t"
      "v_exp_f32 v226, v226\n\t"
      "buffer_load_dwordx4 v[12:15], v240, s[88:91], s35 offen offset:1024\n\t"
      "s_setprio 1\n\t"
      "s_waitcnt lgkmcnt(8)\n\t"
      "v_mfma_f32_32x32x16_bf16 v[144:159], v[176:179], v[24:27], v[144:159]\n\t"
      "ds_read_b64_tr_b16 v[196:197], v232 offset:22528\n\t"
      "ds_read_b64_tr_b16 v[198:199], v232 offset:23040\n\t"
      "v_cvt_pk_bf16_f32 v216, v160, v161\n\t"
      "v_fma_f32 v163, v163, v237, v203\n\t"
      "ds_read_b128 v[200:203], v235 offset:0\n\t"
      "v_add_f32 v226, 1.0, v226\n\t"
      "v_mul_f32 v227, v163, v163\n\t"
      "s_cmp_ge_u32 s39, 1\n\t"
      "s_cbranch_scc1 Lp0go%=\n\t"
      "s_waitcnt vmcnt(2)\n\t"
      "v_cmp_ne_u32 vcc, v239, v238\n\t"
      "s_nop 1\n\t"
      "s_cmp_lg_u64 vcc, 0\n\t"
      "s_cbranch_scc1 Lsp1_%=\n\t"
      "Lsg1_%=:\n\t"
      "Lp0go%=:\n\t"
      "s_waitcnt vmcnt(8)\n\t"
      "s_barrier\n\t"
      "s_waitcnt lgkmcnt(9)\n\t"
      "v_mfma_f32_32x32x16_bf16 v[144:159], v[180:183], v[28:31], v[144:159]\n\t"
      "ds_read_b64_tr_b16 v[176:177], v232 offset:24576\n\t"
      "ds_read_b64_tr_b16 v[178:179], v232 offset:25088\n\t"
      "v_rcp_f32 v226, v226\n\t"
      "v_fma_f32 v227, v227, s58, v244\n\t"
      "v_mul_f32 v227, v163, v227\n\t"
      "v_mul_f32 v162, v162, v226\n\t"
      "s_add_u32 m0, s50, 0\n\t"
      "s_nop 0\n\t"
      "buffer_load_dwordx4 v248, s[44:47], s21 offen offset:0 sc1 lds\n\t"
      "s_waitcnt lgkmcnt(9)\n\t"
      "v_mfma_f32_32x32x16_bf16 v[144:159], v[184:187], v[32:35], v[144:159]\n\t"
      "ds_read_b64_tr_b16 v[180:181], v232 offset:26624\n\t"
      "ds_read_b64_tr_b16 v[182:183], v232 offset:27136\n\t"
      "v_exp_f32 v227, v227\n\t"
      "v_fma_f32 v164, v164, v237, v204\n\t"
      "v_add_f32 v227, 1.0, v227\n\t"
      "v_mul_f32 v224, v164, v164\n\t"
      "s_add_u32 m0, s50, 0\n\t"
      "s_nop 0\n\t"
      "buffer_load_dwordx4 v249, s[44:47], s21 offen offset:1024 sc1 lds\n\t"
      "s_setprio 0\n\t"
      "s_waitcnt lgkmcnt(9)\n\t"
      "v_mfma_f32_32x32x16_bf16 v[144:159], v[188:191], v[36:39], v[144:159]\n\t"
      "ds_read_b64_tr_b16 v[184:185], v232 offset:28672\n\t"
      "ds_read_b64_tr_b16 v[186:187], v232 offset:29184\n\t"
      "v_rcp_f32 v227, v227\n\t"
      "v_fma_f32 v224, v224, s58, v244\n\t"
      "v_mul_f32 v224, v164, v224\n\t"
      "v_mul_f32 v163, v163, v227\n\t"
      "s_waitcnt lgkmcnt(9)\n\t"
      "v_mfma_f32_32x32x16_bf16 v[144:159], v[192:195], v[40:43], v[144:159]\n\t"
      "ds_read_b64_tr_b16 v[188:189], v232 offset:30720\n\t"
      "ds_read_b64_tr_b16 v[190:191], v232 offset:31232\n\t"
      "v_exp_f32 v224, v224\n\t"
      "v_cvt_pk_bf16_f32 v217, v162, v163\n\t"
      "v_fma_f32 v165, v165, v237, v205\n\t"
      "v_add_f32 v224, 1.0, v224\n\t"
      "s_add_u32 m0, s50, 0\n\t"
      "s_nop 0\n\t"
      "buffer_load_dwordx4 v248, s[44:47], s21 offen offset:2048 sc1 lds\n\t"
      "s_waitcnt lgkmcnt(9)\n\t"
      "v_mfma_f32_32x32x16_bf16 v[144:159], v[196:199], v[44:47], v[144:159]\n\t"
      "ds_read_b64_tr_b16 v[192:193], v232 offset:32768\n\t"
      "ds_read_b64_tr_b16 v[194:195], v232 offset:33280\n\t"
      "v_mul_f32 v225, v165, v165\n\t"
      "v_rcp_f32 v224, v224\n\t"
      "v_fma_f32 v225, v225, s58, v244\n\t"
      "v_mul_f32 v225, v165, v225\n\t"
      "s_add_u32 m0, s50, 24576\n\t"
      "s_nop 0\n\t"
      "buffer_load_dwordx4 v248, s[44:47], s22 offen offset:0 sc1 lds\n\t"
      "s_setprio 3\n\t"
      "s_waitcnt lgkmcnt(8)\n\t"
      "v_mfma_f32_32x32x16_bf16 v[144:159], v[176:179], v[48:51], v[144:159]\n\t"
      "ds_read_b64_tr_b16 v[196:197], v232 offset:34816\n\t"
      "ds_read_b64_tr_b16 v[198:199], v232 offset:35328\n\t"
      "v_mul_f32 v164, v164, v224\n\t"
      "v_exp_f32 v225, v225\n\t"
      "v_fma_f32 v166, v166, v237, v206\n\t"
      "v_add_f32 v225, 1.0, v225\n\t"
      "s_waitcnt lgkmcnt(8)\n\t"
      "v_mfma_f32_32x32x16_bf16 v[144:159], v[180:183], v[52:55], v[144:159]\n\t"
      "ds_read_b64_tr_b16 v[176:177], v232 offset:36864\n\t"
      "ds_read_b64_tr_b16 v[178:179], v232 offset:37376\n\t"
      "v_mul_f32 v226, v166, v166\n\t"
      "v_rcp_f32 v225, v225\n\t"
      "v_fma_f32 v226, v226, s58, v244\n\t"
      "v_mul_f32 v226, v166, v226\n\t"
      "s_add_u32 m0, s50, 24576\n\t"
      "s_nop 0\n\t"
      "buffer_load_dwordx4 v249, s[44:47], s22 offen offset:1024 sc1 lds\n\t"
      "s_waitcnt lgkmcnt(8)\n\t"
      "v_mfma_f32_32x32x16_bf16 v[144:159], v[184:187], v[56:59], v[144:159]\n\t"
      "ds_read_b64_tr_b16 v[180:181], v232 offset:38912\n\t"
      "ds_read_b64_tr_b16 v[182:183], v232 offset:39424\n\t"
      "v_mul_f32 v165, v165, v225\n\t"
      "v_exp_f32 v226, v226\n\t"
      "v_cvt_pk_bf16_f32 v218, v164, v165\n\t"
      "v_fma_f32 v167, v167, v237, v207\n\t"
      "ds_read_b128 v[204:207], v235 offset:32\n\t"
      "s_add_u32 m0, s50, 24576\n\t"
      "s_nop 0\n\t"
      "buffer_load_dwordx4 v248, s[44:47], s22 offen offset:2048 sc1 lds\n\t"
      "s_setprio 2\n\t"
      "s_waitcnt lgkmcnt(9)\n\t"
      "v_mfma_f32_32x32x16_bf16 v[144:159], v[188:191], v[60:63], v[144:159]\n\t"
      "ds_read_b64_tr_b16 v[184:185], v232 offset:40960\n\t"
      "ds_read_b64_tr_b16 v[186:187], v232 offset:41472\n\t"
      "v_add_f32 v226, 1.0, v226\n\t"
      "v_mul_f32 v227, v167, v167\n\t"
      "v_rcp_f32 v226, v226\n\t"
      "v_fma_f32 v227, v227, s58, v244\n\t"
      "s_waitcnt lgkmcnt(9)\n\t"
      "v_mfma_f32_32x32x16_bf16 v[144:159], v[192:195], v[64:67], v[144:159]\n\t"
      "ds_read_b64_tr_b16 v[188:189], v232 offset:43008\n\t"
      "ds_read_b64_tr_b16 v[190:191], v232 offset:43520\n\t"
      "v_mul_f32 v227, v167, v227\n\t"
      "v_mul_f32 v166, v166, v226\n\t"
      "v_exp_f32 v227, v227\n\t"
      "v_fma_f32 v168, v168, v237, v208\n\t"
      "s_add_u32 m0, s50, 49152\n\t"
      "s_nop 0\n\t"
      "buffer_load_dwordx4 v248, s[44:47], s23 offen offset:0 sc1 lds\n\t"
      "s_waitcnt lgkmcnt(9)\n\t"
      "v_mfma_f32_32x32x16_bf16 v[144:159], v[196:199], v[68:71], v[144:159]\n\t"
      "ds_read_b64_tr_b16 v[192:193], v232 offset:45056\n\t"
      "ds_read_b64_tr_b16 v[194:195], v232 offset:45568\n\t"
      "v_add_f32 v227, 1.0, v227\n\t"
      "v_mul_f32 v224, v168, v168\n\t"
      "v_rcp_f32 v227, v227\n\t"
      "v_fma_f32 v224, v224, s58, v244\n\t"
      "s_add_u32 m0, s50, 49152\n\t"
      "s_nop 0\n\t"
      "buffer_load_dwordx4 v249, s[44:47], s23 offen offset:1024 sc1 lds\n\t"
      "s_setprio 1\n\t"
      "s_waitcnt lgkmcnt(9)\n\t"
      "v_mfma_f32_32x32x16_bf16 v[144:159], v[176:179], v[72:75], v[144:159]\n\t"
      "ds_read_b64_tr_b16 v[196:197], v232 offset:47104\n\t"
      "ds_read_b64_tr_b16 v[198:199], v232 offset:47616\n\t"
      "v_mul_f32 v224, v168, v224\n\t"
      "v_mul_f32 v167, v167, v227\n\t"
      "v_exp_f32 v224, v224\n\t"
      "v_cvt_pk_bf16_f32 v219, v166, v167\n\t"
      "buffer_store_dwordx4 v[216:219], v240, s[44:47], s52 offen offset:0 sc1\n\t"
      "s_waitcnt vmcnt(13)\n\t"
      "s_barrier\n\t"
      "s_waitcnt lgkmcnt(9)\n\t"
      "v_mfma_f32_32x32x16_bf16 v[144:159], v[180:183], v[76:79], v[144:159]\n\t"
      "ds_read_b64_tr_b16 v[176:177], v236 offset:0\n\t"
      "ds_read_b64_tr_b16 v[178:179], v236 offset:512\n\t"
      "v_fma_f32 v169, v169, v237, v209\n\t"
      "v_add_f32 v224, 1.0, v224\n\t"
      "v_mul_f32 v225, v169, v169\n\t"
      "v_rcp_f32 v224, v224\n\t"
      "s_add_u32 m0, s50, 49152\n\t"
      "s_nop 0\n\t"
      "buffer_load_dwordx4 v248, s[44:47], s23 offen offset:2048 sc1 lds\n\t"
      "s_waitcnt lgkmcnt(8)\n\t"
      "v_mfma_f32_32x32x16_bf16 v[144:159], v[184:187], v[80:83], v[144:159]\n\t"
      "ds_read_b64_tr_b16 v[180:181], v236 offset:2048\n\t"
      "ds_read_b64_tr_b16 v[182:183], v236 offset:2560\n\t"
      "v_fma_f32 v225, v225, s58, v244\n\t"
      "v_mul_f32 v225, v169, v225\n\t"
      "v_mul_f32 v168, v168, v224\n\t"
      "v_exp_f32 v225, v225\n\t"
      "buffer_load_dwordx4 v[0:3], v240, s[44:47], s24 offen offset:0 sc1\n\t"
      "s_setprio 0\n\t"
      "s_waitcnt lgkmcnt(8)\n\t"
      "v_mfma_f32_32x32x16_bf16 v[144:159], v[188:191], v[84:87], v[144:159]\n\t"
      "ds_read_b64_tr_b16 v[184:185], v236 offset:4096\n\t"
      "ds_read_b64_tr_b16 v[186:187], v236 offset:4608\n\t"
      "v_fma_f32 v170, v170, v237, v210\n\t"
      "v_add_f32 v225, 1.0, v225\n\t"
      "v_mul_f32 v226, v170, v170\n\t"
      "v_rcp_f32 v225, v225\n\t"
      "buffer_load_dwordx4 v[4:7], v240, s[44:47], s24 offen offset:1024 sc1\n\t"
      "s_waitcnt lgkmcnt(8)\n\t"
      "v_mfma_f32_32x32x16_bf16 v[144:159], v[192:195], v[88:91], v[144:159]\n\t"
      "ds_read_b64_tr_b16 v[188:189], v236 offset:6144\n\t"
      "ds_read_b64_tr_b16 v[190:191], v236 offset:6656\n\t"
      "v_fma_f32 v226, v226, s58, v244\n\t"
      "v_mul_f32 v226, v170, v226\n\t"
      "v_mul_f32 v169, v169, v225\n\t"
      "v_exp_f32 v226, v226\n\t"
      "buffer_load_dwordx4 v[16:19], v240, s[88:91], s35 offen offset:2048\n\t"
      "s_waitcnt lgkmcnt(8)\n\t"
      "v_mfma_f32_32x32x16_bf16 v[144:159], v[196:199], v[92:95], v[144:159]\n\t"
      "ds_read_b64_tr_b16 v[192:193], v236 offset:8192\n\t"
      "ds_read_b64_tr_b16 v[194:195], v236 offset:8704\n\t"
      "v_cvt_pk_bf16_f32 v220, v168, v169\n\t"
      "v_fma_f32 v171, v171, v237, v211\n\t"
      "ds_read_b128 v[208:211], v235 offset:64\n\t"
      "v_add_f32 v226, 1.0, v226\n\t"
      "v_mul_f32 v227, v171, v171\n\t"
      "buffer_load_dwordx4 v[20:23], v240, s[88:91], s35 offen offset:3072\n\t"
      "s_setprio 3\n\t"
      "s_waitcnt lgkmcnt(9)\n\t"
      "v_mfma_f32_32x32x16_bf16 v[144:159], v[176:179], v[96:99], v[144:159]\n\t"
      "ds_read_b64_tr_b16 v[196:197], v236 offset:10240\n\t"
      "ds_read_b64_tr_b16 v[198:199], v236 offset:10752\n\t"
      "v_rcp_f32 v226, v226\n\t"
      "v_fma_f32 v227, v227, s58, v244\n\t"
      "v_mul_f32 v227, v171, v227\n\t"
      "v_mul_f32 v170, v170, v226\n\t"
      "s_waitcnt lgkmcnt(9)\n\t"
      "v_mfma_f32_32x32x16_bf16 v[144:159], v[180:183], v[100:103], v[144:159]\n\t"
      "ds_read_b64_tr_b16 v[176:177], v236 offset:12288\n\t"
      "ds_read_b64_tr_b16 v[178:179], v236 offset:12800\n\t"
      "v_exp_f32 v227, v227\n\t"
      "v_fma_f32 v172, v172, v237, v212\n\t"
      "v_add_f32 v227, 1.0, v227\n\t"
      "v_mul_f32 v224, v172, v172\n\t"
      "s_waitcnt lgkmcnt(9)\n\t"
      "v_mfma_f32_32x32x16_bf16 v[144:159], v[184:187], v[104:107], v[144:159]\n\t"
      "ds_read_b64_tr_b16 v[180:181], v236 offset:14336\n\t"
      "ds_read_b64_tr_b16 v[182:183], v236 offset:14848\n\t"
      "v_rcp_f32 v227, v227\n\t"
      "v_fma_f32 v224, v224, s58, v244\n\t"
      "v_mul_f32 v224, v172, v224\n\t"
      "v_mul_f32 v171, v171, v227\n\t"
      "s_setprio 2\n\t"
      "s_waitcnt lgkmcnt(9)\n\t"
      "v_mfma_f32_32x32x16_bf16 v[144:159], v[188:191], v[108:111], v[144:159]\n\t"
      "ds_read_b64_tr_b16 v[184:185], v236 offset:16384\n\t"
      "ds_read_b64_tr_b16 v[186:187], v236 offset:16896\n\t"
      "v_exp_f32 v224, v224\n\t"
      "v_cvt_pk_bf16_f32 v221, v170, v171\n\t"
      "v_fma_f32 v173, v173, v237, v213\n\t"
      "v_add_f32 v224, 1.0, v224\n\t"
      "s_waitcnt lgkmcnt(9)\n\t"
      "v_mfma_f32_32x32x16_bf16 v[144:159], v[192:195], v[112:115], v[144:159]\n\t"
      "ds_read_b64_tr_b16 v[188:189], v236 offset:18432\n\t"
      "ds_read_b64_tr_b16 v[190:191], v236 offset:18944\n\t"
      "v_mul_f32 v225, v173, v173\n\t"
      "v_rcp_f32 v224, v224\n\t"
      "v_fma_f32 v225, v225, s58, v244\n\t"
      "v_mul_f32 v225, v173, v225\n\t"
      "s_waitcnt lgkmcnt(8)\n\t"
      "v_mfma_f32_32x32x16_bf16 v[144:159], v[196:199], v[116:119], v[144:159]\n\t"
      "ds_read_b64_tr_b16 v[192:193], v236 offset:20480\n\t"
      "ds_read_b64_tr_b16 v[194:195], v236 offset:20992\n\t"
      "v_mul_f32 v172, v172, v224\n\t"
      "v_exp_f32 v225, v225\n\t"
      "v_fma_f32 v174, v174, v237, v214\n\t"
      "v_add_f32 v225, 1.0, v225\n\t"
      "s_setprio 1\n\t"
      "s_waitcnt lgkmcnt(8)\n\t"
      "v_mfma_f32_32x32x16_bf16 v[144:159], v[176:179], v[120:123], v[144:159]\n\t"
      "ds_read_b64_tr_b16 v[196:197], v236 offset:22528\n\t"
      "ds_read_b64_tr_b16 v[198:199], v236 offset:23040\n\t"
      "v_mul_f32 v226, v174, v174\n\t"
      "v_rcp_f32 v225, v225\n\t"
      "v_fma_f32 v226, v226, s58, v244\n\t"
      "v_mul_f32 v226, v174, v226\n\t"
      "s_waitcnt vmcnt(4)\n\t"
      "s_barrier\n\t"
      "s_waitcnt lgkmcnt(8)\n\t"
      "v_mfma_f32_32x32x16_bf16 v[144:159], v[180:183], v[124:127], v[144:159]\n\t"
      "v_mul_f32 v173, v173, v225\n\t"
      "v_exp_f32 v226, v226\n\t"
      "v_cvt_pk_bf16_f32 v222, v172, v173\n\t"
      "s_and_b32 s54, s39, 3\n\t"
      "s_mul_i32 s54, s54, 3072\n\t"
      "s_add_u32 s54, s54, s20\n\t"
      "s_cmp_ge_u32 s39, 4\n\t"
      "s_cselect_b64 s[40:41], -1, 0\n\t"
      "s_cselect_b32 s55, 1024, 0\n\t"
      "s_add_u32 s55, s55, s54\n\t"
      "v_cndmask_b32 v250, v248, v249, s[40:41]\n\t"
      "v_xor_b32 v251, 128, v250\n\t"
      "v_add_u32 v250, s55, v250\n\t"
      "v_add_u32 v251, s55, v251\n\t"
      "v_cmp_ne_u32 vcc, 0, v10\n\t"
      "v_mov_b32 v24, s54\n\t"
      "s_nop 0\n\t"
      "v_cndmask_b32 v24, 0, v24, vcc\n\t"
      "v_add_u32 v8, v8, v24\n\t"
      "v_add_u32 v9, v9, v24\n\t"
      "s_waitcnt lgkmcnt(6)\n\t"
      "v_mfma_f32_32x32x16_bf16 v[144:159], v[184:187], v[128:131], v[144:159]\n\t"
      "v_fma_f32 v175, v175, v237, v215\n\t"
      "ds_read_b128 v[212:215], v235 offset:96\n\t"
      "v_add_f32 v226, 1.0, v226\n\t"
      "v_mul_f32 v227, v175, v175\n\t"
      "v_rcp_f32 v226, v226\n\t"
      "ds_read_b128 v[176:179], v250 offset:24576\n\t"
      "ds_read_b128 v[180:183], v251 offset:25600\n\t"
      "s_setprio 0\n\t"
      "s_waitcnt lgkmcnt(7)\n\t"
      "v_mfma_f32_32x32x16_bf16 v[144:159], v[188:191], v[132:135], v[144:159]\n\t"
      "v_fma_f32 v227, v227, s58, v244\n\t"
      "v_mul_f32 v227, v175, v227\n\t"
      "v_mul_f32 v174, v174, v226\n\t"
      "v_exp_f32 v227, v227\n\t"
      "s_waitcnt vmcnt(0)\n\t"
      "v_and_b32 v0, v0, v16\n\t"
      "v_and_b32 v1, v1, v17\n\t"
      "v_and_b32 v2, v2, v18\n\t"
      "v_and_b32 v3, v3, v19\n\t"
      "v_and_b32 v4, v4, v20\n\t"
      "v_and_b32 v5, v5, v21\n\t"
      "v_and_b32 v6, v6, v22\n\t"
      "v_and_b32 v7, v7, v23\n\t"
      "s_waitcnt lgkmcnt(5)\n\t"
      "v_mfma_f32_32x32x16_bf16 v[144:159], v[192:195], v[136:139], v[144:159]\n\t"
      "v_add_f32 v227, 1.0, v227\n\t"
      "v_rcp_f32 v227, v227\n\t"
      "s_nop 0\n\t"
      "v_mul_f32 v175, v175, v227\n\t"
      "v_cvt_pk_bf16_f32 v223, v174, v175\n\t"
      "ds_read_b128 v[184:187], v250 offset:21504\n\t"
      "ds_read_b128 v[188:191], v251 offset:22528\n\t"
      "s_waitcnt lgkmcnt(5)\n\t"
      "v_mfma_f32_32x32x16_bf16 v[160:175], v[196:199], v[140:143], v[144:159]\n\t"
      "buffer_store_dwordx4 v[220:223], v240, s[44:47], s52 offen offset:1024 sc1\n\t"
      "s_lshl_b32 s54, s51, 12\n\t"
      "s_add_u32 s52, s10, s54\n\t"
      "s_add_u32 s51, s51, 1\n\t"
      "s_cmp_ge_u32 s51, s9\n\t"
      "s_cselect_b32 s51, 0, s51\n\t"
      "s_lshl_b32 s54, s51, 8\n\t"
      "v_add_u32 v235, s54, v245\n\t"
      "s_setprio 0\n\t"
      "s_mov_b32 s88, s78\n\t"
      "s_mov_b32 s89, s79\n\t"
      "s_mov_b32 s90, 5308416\n\t"
      "s_mov_b32 s92, s76\n\t"
      "s_mov_b32 s93, s77\n\t"
      "s_mov_b32 s94, 663552\n\t"
      "s_mov_b32 s21, s5\n\t"
      "s_mov_b32 s22, s6\n\t"
      "s_mov_b32 s23, s18\n\t"
      "s_mov_b32 s25, s16\n\t"
      "s_waitcnt lgkmcnt(4)\n\t"
      "ds_read_b128 v[192:195], v250 offset:18432\n\t"
      "ds_read_b128 v[196:199], v251 offset:19456\n\t"
      "s_waitcnt lgkmcnt(4)\n\t"
      "v_mfma_f32_32x32x16_bf16 v[144:159], v[176:179], v[0:3], 0\n\t"
      "v_mfma_f32_32x32x16_bf16 v[144:159], v[180:183], v[4:7], v[144:159]\n\t"
      "ds_read_b128 v[176:179], v250 offset:15360\n\t"
      "ds_read_b128 v[180:183], v251 offset:16384\n\t"
      "v_fma_f32 v160, v160, v237, v200\n\t"
      "v_mul_f32 v224, v160, v160\n\t"
      "v_fma_f32 v224, v224, s58, v244\n\t"
      "v_mul_f32 v224, v160, v224\n\t"
      "v_exp_f32 v224, v224\n\t"
      "v_fma_f32 v161, v161, v237, v201\n\t"
      "v_add_f32 v224, 1.0, v224\n\t"
      "v_mul_f32 v225, v161, v161\n\t"
      "s_waitcnt lgkmcnt(4)\n\t"
      "v_mfma_f32_32x32x16_bf16 v[128:143], v[184:187], v[0:3], 0\n\t"
      "v_rcp_f32 v224, v224\n\t"
      "v_fma_f32 v225, v225, s58, v244\n\t"
      "v_mul_f32 v225, v161, v225\n\t"
      "v_mul_f32 v160, v160, v224\n\t"
      "v_exp_f32 v225, v225\n\t"
      "v_fma_f32 v162, v162, v237, v202\n\t"
      "v_add_f32 v225, 1.0, v225\n\t"
      "v_mul_f32 v226, v162, v162\n\t"
      "v_mfma_f32_32x32x16_bf16 v[128:143], v[188:191], v[4:7], v[128:143]\n\t"
      "ds_read_b128 v[184:187], v250 offset:12288\n\t"
      "ds_read_b128 v[188:191], v251 offset:13312\n\t"
      "v_rcp_f32 v225, v225\n\t"
      "v_fma_f32 v226, v226, s58, v244\n\t"
      "v_mul_f32 v226, v162, v226\n\t"
      "v_mul_f32 v161, v161, v225\n\t"
      "v_exp_f32 v226, v226\n\t"
      "v_cvt_pk_bf16_f32 v216, v160, v161\n\t"
      "v_fma_f32 v163, v163, v237, v203\n\t"
      "v_add_f32 v226, 1.0, v226\n\t"
      "s_waitcnt lgkmcnt(4)\n\t"
      "v_mfma_f32_32x32x16_bf16 v[112:127], v[192:195], v[0:3], 0\n\t"
      "v_mul_f32 v227, v163, v163\n\t"
      "v_rcp_f32 v226, v226\n\t"
      "v_fma_f32 v227, v227, s58, v244\n\t"
      "v_mul_f32 v227, v163, v227\n\t"
      "v_mul_f32 v162, v162, v226\n\t"
      "v_exp_f32 v227, v227\n\t"
      "v_fma_f32 v164, v164, v237, v204\n\t"
      "v_add_f32 v227, 1.0, v227\n\t"
      "v_mfma_f32_32x32x16_bf16 v[112:127], v[196:199], v[4:7], v[112:127]\n\t"
      "ds_read_b128 v[192:195], v250 offset:9216\n\t"
      "ds_read_b128 v[196:199], v251 offset:10240\n\t"
      "v_mul_f32 v224, v164, v164\n\t"
      "v_rcp_f32 v227, v227\n\t"
      "v_fma_f32 v224, v224, s58, v244\n\t"
      "v_mul_f32 v224, v164, v224\n\t"
      "v_mul_f32 v163, v163, v227\n\t"
      "v_exp_f32 v224, v224\n\t"
      "v_cvt_pk_bf16_f32 v217, v162, v163\n\t"
      "v_fma_f32 v165, v165, v237, v205\n\t"
      "s_waitcnt lgkmcnt(4)\n\t"
      "v_mfma_f32_32x32x16_bf16 v[96:111], v[176:179], v[0:3], 0\n\t"
      "v_add_f32 v224, 1.0, v224\n\t"
      "v_mul_f32 v225, v165, v165\n\t"
      "v_rcp_f32 v224, v224\n\t"
      "v_fma_f32 v225, v225, s58, v244\n\t"
      "v_mul_f32 v225, v165, v225\n\t"
      "v_mul_f32 v164, v164, v224\n\t"
      "v_exp_f32 v225, v225\n\t"
      "v_fma_f32 v166, v166, v237, v206\n\t"
      "v_mfma_f32_32x32x16_bf16 v[96:111], v[180:183], v[4:7], v[96:111]\n\t"
      "ds_read_b128 v[176:179], v250 offset:6144\n\t"
      "ds_read_b128 v[180:183], v251 offset:7168\n\t"
      "v_add_f32 v225, 1.0, v225\n\t"
      "v_mul_f32 v226, v166, v166\n\t"
      "v_rcp_f32 v225, v225\n\t"
      "v_fma_f32 v226, v226, s58, v244\n\t"
      "v_mul_f32 v226, v166, v226\n\t"
      "v_mul_f32 v165, v165, v225\n\t"
      "v_exp_f32 v226, v226\n\t"
      "v_cvt_pk_bf16_f32 v218, v164, v165\n\t"
      "s_waitcnt lgkmcnt(4)\n\t"
      "v_mfma_f32_32x32x16_bf16 v[80:95], v[184:187], v[0:3], 0\n\t"
      "v_fma_f32 v167, v167, v237, v207\n\t"
      "v_add_f32 v226, 1.0, v226\n\t"
      "v_mul_f32 v227, v167, v167\n\t"
      "v_rcp_f32 v226, v226\n\t"
      "v_fma_f32 v227, v227, s58, v244\n\t"
      "v_mul_f32 v227, v167, v227\n\t"
      "v_mul_f32 v166, v166, v226\n\t"
      "v_exp_f32 v227, v227\n\t"
      "v_mfma_f32_32x32x16_bf16 v[80:95], v[188:191], v[4:7], v[80:95]\n\t"
      "ds_read_b128 v[184:187], v250 offset:3072\n\t"
      "ds_read_b128 v[188:191], v251 offset:4096\n\t"
      "v_fma_f32 v168, v168, v237, v208\n\t"
      "v_add_f32 v227, 1.0, v227\n\t"
      "v_mul_f32 v224, v168, v168\n\t"
      "v_rcp_f32 v227, v227\n\t"
      "v_fma_f32 v224, v224, s58, v244\n\t"
      "v_mul_f32 v224, v168, v224\n\t"
      "v_mul_f32 v167, v167, v227\n\t"
      "v_exp_f32 v224, v224\n\t"
      "s_waitcnt lgkmcnt(4)\n\t"
      "v_mfma_f32_32x32x16_bf16 v[64:79], v[192:195], v[0:3], 0\n\t"
      "v_cvt_pk_bf16_f32 v219, v166, v167\n\t"
      "buffer_store_dwordx4 v[216:219], v240, s[44:47], s52 offen offset:0 sc1\n\t"
      "v_fma_f32 v169, v169, v237, v209\n\t"
      "v_add_f32 v224, 1.0, v224\n\t"
      "v_mul_f32 v225, v169, v169\n\t"
      "v_rcp_f32 v224, v224\n\t"
      "v_fma_f32 v225, v225, s58, v244\n\t"
      "v_mul_f32 v225, v169, v225\n\t"
      "v_mul_f32 v168, v168, v224\n\t"
      "v_mfma_f32_32x32x16_bf16 v[64:79], v[196:199], v[4:7], v[64:79]\n\t"
      "ds_read_b128 v[192:195], v250 offset:0\n\t"
      "ds_read_b128 v[196:199], v251 offset:1024\n\t"
      "v_exp_f32 v225, v225\n\t"
      "v_fma_f32 v170, v170, v237, v210\n\t"
      "v_add_f32 v225, 1.0, v225\n\t"
      "v_mul_f32 v226, v170, v170\n\t"
      "v_rcp_f32 v225, v225\n\t"
      "v_fma_f32 v226, v226, s58, v244\n\t"
      "v_mul_f32 v226, v170, v226\n\t"
      "v_mul_f32 v169, v169, v225\n\t"
      "s_waitcnt lgkmcnt(4)\n\t"
      "v_mfma_f32_32x32x16_bf16 v[48:63], v[176:179], v[0:3], 0\n\t"
      "v_exp_f32 v226, v226\n\t"
      "v_cvt_pk_bf16_f32 v220, v168, v169\n\t"
      "v_fma_f32 v171, v171, v237, v211\n\t"
      "v_add_f32 v226, 1.0, v226\n\t"
      "v_mul_f32 v227, v171, v171\n\t"
      "v_rcp_f32 v226, v226\n\t"
      "v_fma_f32 v227, v227, s58, v244\n\t"
      "v_mul_f32 v227, v171, v227\n\t"
      "v_mfma_f32_32x32x16_bf16 v[48:63], v[180:183], v[4:7], v[48:63]\n\t"
      "v_mul_f32 v170, v170, v226\n\t"
      "v_exp_f32 v227, v227\n\t"
      "v_fma_f32 v172, v172, v237, v212\n\t"
      "v_add_f32 v227, 1.0, v227\n\t"
      "v_mul_f32 v224, v172, v172\n\t"
      "v_rcp_f32 v227, v227\n\t"
      "v_fma_f32 v224, v224, s58, v244\n\t"
      "v_mul_f32 v224, v172, v224\n\t"
      "s_waitcnt lgkmcnt(2)\n\t"
      "v_mfma_f32_32x32x16_bf16 v[32:47], v[184:187], v[0:3], 0\n\t"
      "v_mul_f32 v171, v171, v227\n\t"
      "v_exp_f32 v224, v224\n\t"
      "v_cvt_pk_bf16_f32 v221, v170, v171\n\t"
      "v_fma_f32 v173, v173, v237, v213\n\t"
      "v_add_f32 v224, 1.0, v224\n\t"
      "v_mul_f32 v225, v173, v173\n\t"
      "v_rcp_f32 v224, v224\n\t"
      "v_fma_f32 v225, v225, s58, v244\n\t"
      "v_mfma_f32_32x32x16_bf16 v[32:47], v[188:191], v[4:7], v[32:47]\n\t"
      "v_mul_f32 v225, v173, v225\n\t"
      "v_mul_f32 v172, v172, v224\n\t"
      "v_exp_f32 v225, v225\n\t"
      "v_fma_f32 v174, v174, v237, v214\n\t"
      "v_add_f32 v225, 1.0, v225\n\t"
      "v_mul_f32 v226, v174, v174\n\t"
      "v_rcp_f32 v225, v225\n\t"
      "v_fma_f32 v226, v226, s58, v244\n\t"
      "s_waitcnt lgkmcnt(0)\n\t"
      "v_mfma_f32_32x32x16_bf16 v[16:31], v[192:195], v[0:3], 0\n\t"
      "v_mul_f32 v226, v174, v226\n\t"
      "v_mul_f32 v173, v173, v225\n\t"
      "v_exp_f32 v226, v226\n\t"
      "v_cvt_pk_bf16_f32 v222, v172, v173\n\t"
      "v_fma_f32 v175, v175, v237, v215\n\t"
      "v_add_f32 v226, 1.0, v226\n\t"
      "v_mul_f32 v227, v175, v175\n\t"
      "v_rcp_f32 v226, v226\n\t"
      "v_mfma_f32_32x32x16_bf16 v[16:31], v[196:199], v[4:7], v[16:31]\n\t"
      "v_fma_f32 v227, v227, s58, v244\n\t"
      "v_mul_f32 v227, v175, v227\n\t"
      "v_mul_f32 v174, v174, v226\n\t"
      "v_exp_f32 v227, v227\n\t"
      "s_nop 0\n\t"
      "v_add_f32 v227, 1.0, v227\n\t"
      "v_rcp_f32 v227, v227\n\t"
      "s_nop 0\n\t"
      "v_mul_f32 v175, v175, v227\n\t"
      "v_cvt_pk_bf16_f32 v223, v174, v175\n\t"
      "buffer_store_dwordx4 v[220:223], v240, s[44:47], s52 offen offset:1024 sc1\n\t"
      "s_barrier\n\t"
      "s_mul_i32 s48, s39, 2304\n\t"
      "s_add_u32 s51, s20, s48\n\t"
      "s_sub_u32 s50, 73728, s20\n\t"
      "s_add_u32 s50, s50, s48\n\t"
      "s_add_u32 m0, s50, 0\n\t"
      "s_add_u32 s49, s27, 0\n\t"
      "buffer_load_dwordx4 v252, s[88:91], s49 offen lds\n\t"
      "s_add_u32 m0, s50, 1024\n\t"
      "s_add_u32 s49, s27, 0\n\t"
      "buffer_load_dwordx4 v253, s[88:91], s49 offen lds\n\t"
      "s_add_u32 m0, s50, 2048\n\t"
      "s_add_u32 s49, s27, 0\n\t"
      "s_mov_b64 exec, 0xffff\n\t"
      "buffer_load_dwordx4 v254, s[88:91], s49 offen lds\n\t"
      "s_mov_b64 exec, -1\n\t"
      "s_lshr_b32 s48, s33, 28\n\t"
      "s_cmp_lg_u32 s48, 0\n\t"
      "s_cbranch_scc1 Lfix%=\n\t"
      "Lfixd%=:\n\t"
      "s_mov_b32 s4, 0xffffffff\n\t"
      "s_mov_b32 s5, 0xfffffff0\n\t"
      "s_mov_b32 s6, 0xfffffffe\n\t"
      "s_mov_b32 s7, 0xffffffe0\n\t"
      "v_max_f32 v248, v128, v129\n\t"
      "v_max_f32 v249, v130, v131\n\t"
      "v_max3_f32 v248, v248, v132, v133\n\t"
      "v_max3_f32 v249, v249, v134, v135\n\t"
      "v_max3_f32 v248, v248, v136, v137\n\t"
      "v_max3_f32 v249, v249, v138, v139\n\t"
      "v_max3_f32 v248, v248, v140, v141\n\t"
      "v_max3_f32 v249, v249, v142, v143\n\t"
      "s_mov_b32 s8, 0xfffffffc\n\t"
      "s_mov_b32 s9, 0xffffffc0\n\t"
      "s_mov_b32 s10, 0xfffffff8\n\t"
      "s_mov_b32 s11, 0xffffff80\n\t"
      "v_max3_f32 v248, v248, v112, v113\n\t"
      "v_max3_f32 v249, v249, v114, v115\n\t"
      "v_max3_f32 v248, v248, v116, v117\n\t"
      "v_max3_f32 v249, v249, v118, v119\n\t"
      "v_max3_f32 v248, v248, v120, v121\n\t"
      "v_max3_f32 v249, v249, v122, v123\n\t"
      "v_max3_f32 v248, v248, v124, v125\n\t"
      "v_max3_f32 v249, v249, v126, v127\n\t"
      "s_mov_b32 s12, 0xffffff00\n\t"
      "s_mov_b32 s13, 0xfffff000\n\t"
      "s_mov_b32 s14, 0xfffffe00\n\t"
      "s_mov_b32 s15, 0xffffe000\n\t"
      "v_max3_f32 v248, v248, v96, v97\n\t"
      "v_max3_f32 v249, v249, v98, v99\n\t"
      "v_max3_f32 v248, v248, v100, v101\n\t"
      "v_max3_f32 v249, v249, v102, v103\n\t"
      "v_max3_f32 v248, v248, v104, v105\n\t"
      "v_max3_f32 v249, v249, v106, v107\n\t"
      "v_max3_f32 v248, v248, v108, v109\n\t"
      "v_max3_f32 v249, v249, v110, v111\n\t"
      "s_mov_b32 s16, 0xfffffc00\n\t"
      "s_mov_b32 s17, 0xffffc000\n\t"
      "s_mov_b32 s18, 0xfffff800\n\t"
      "s_mov_b32 s19, 0xffff8000\n\t"
      "v_max3_f32 v248, v248, v80, v81\n\t"
      "v_max3_f32 v249, v249, v82, v83\n\t"
      "v_max3_f32 v248, v248, v84, v85\n\t"
      "v_max3_f32 v249, v249, v86, v87\n\t"
      "v_max3_f32 v248, v248, v88, v89\n\t"
      "v_max3_f32 v249, v249, v90, v91\n\t"
      "v_max3_f32 v248, v248, v92, v93\n\t"
      "v_max3_f32 v249, v249, v94, v95\n\t"
      "s_mov_b32 s52, 0xffff0000\n\t"
      "s_mov_b32 s53, 0xfff00000\n\t"
      "s_mov_b32 s54, 0xfffe0000\n\t"
      "s_mov_b32 s55, 0xffe00000\n\t"
      "v_max3_f32 v248, v248, v64, v65\n\t"
      "v_max3_f32 v249, v249, v66, v67\n\t"
      "v_max3_f32 v248, v248, v68, v69\n\t"
      "v_max3_f32 v249, v249, v70, v71\n\t"
      "v_max3_f32 v248, v248, v72, v73\n\t"
      "v_max3_f32 v249, v249, v74, v75\n\t"
      "v_max3_f32 v248, v248, v76, v77\n\t"
      "v_max3_f32 v249, v249, v78, v79\n\t"
      "s_mov_b32 s56, 0xfffc0000\n\t"
      "s_mov_b32 s57, 0xffc00000\n\t"
      "s_mov_b32 s58, 0xfff80000\n\t"
      "s_mov_b32 s59, 0xff800000\n\t"
      "v_max3_f32 v248, v248, v48, v49\n\t"
      "v_max3_f32 v249, v249, v50, v51\n\t"
      "v_max3_f32 v248, v248, v52, v53\n\t"
      "v_max3_f32 v249, v249, v54, v55\n\t"
      "v_max3_f32 v248, v248, v56, v57\n\t"
      "v_max3_f32 v249, v249, v58, v59\n\t"
      "v_max3_f32 v248, v248, v60, v61\n\t"
      "v_max3_f32 v249, v249, v62, v63\n\t"
      "s_mov_b32 s62, 0xff000000\n\t"
      "s_mov_b32 s63, 0xf0000000\n\t"
      "s_mov_b32 s64, 0xfe000000\n\t"
      "s_mov_b32 s65, 0xe0000000\n\t"
      "v_max3_f32 v248, v248, v32, v33\n\t"
      "v_max3_f32 v249, v249, v34, v35\n\t"
      "v_max3_f32 v248, v248, v36, v37\n\t"
      "v_max3_f32 v249, v249, v38, v39\n\t"
      "v_max3_f32 v248, v248, v40, v41\n\t"
      "v_max3_f32 v249, v249, v42, v43\n\t"
      "v_max3_f32 v248, v248, v44, v45\n\t"
      "v_max3_f32 v249, v249, v46, v47\n\t"
      "s_mov_b32 s66, 0xfc000000\n\t"
      "s_mov_b32 s67, 0xc0000000\n\t"
      "s_mov_b32 s42, 0xf8000000\n\t"
      "s_mov_b32 s43, 0x80000000\n\t"
      "v_cndmask_b32 v144, v16, v144, s[4:5]\n\t"
      "v_cndmask_b32 v145, v17, v145, s[6:7]\n\t"
      "v_cndmask_b32 v146, v18, v146, s[8:9]\n\t"
      "v_cndmask_b32 v147, v19, v147, s[10:11]\n\t"
      "v_cndmask_b32 v148, v20, v148, s[12:13]\n\t"
      "v_cndmask_b32 v149, v21, v149, s[14:15]\n\t"
      "v_cndmask_b32 v150, v22, v150, s[16:17]\n\t"
      "v_cndmask_b32 v151, v23, v151, s[18:19]\n\t"
      "v_cndmask_b32 v152, v24, v152, s[52:53]\n\t"
      "v_cndmask_b32 v153, v25, v153, s[54:55]\n\t"
      "v_cndmask_b32 v154, v26, v154, s[56:57]\n\t"
      "v_cndmask_b32 v155, v27, v155, s[58:59]\n\t"
      "v_cndmask_b32 v156, v28, v156, s[62:63]\n\t"
      "v_cndmask_b32 v157, v29, v157, s[64:65]\n\t"
      "v_cndmask_b32 v158, v30, v158, s[66:67]\n\t"
      "v_cndmask_b32 v159, v31, v159, s[42:43]\n\t"
      "s_add_u32 m0, s50, 18432\n\t"
      "s_add_u32 s49, s27, 73728\n\t"
      "buffer_load_dwordx4 v252, s[88:91], s49 offen lds\n\t"
      "s_add_u32 m0, s50, 19456\n\t"
      "s_add_u32 s49, s27, 73728\n\t"
      "buffer_load_dwordx4 v253, s[88:91], s49 offen lds\n\t"
      "s_add_u32 m0, s50, 20480\n\t"
      "s_add_u32 s49, s27, 73728\n\t"
      "s_mov_b64 exec, 0xffff\n\t"
      "buffer_load_dwordx4 v254, s[88:91], s49 offen lds\n\t"
      "s_mov_b64 exec, -1\n\t"
      "v_max3_f32 v248, v248, v144, v145\n\t"
      "v_max3_f32 v249, v249, v146, v147\n\t"
      "v_max3_f32 v248, v248, v148, v149\n\t"
      "v_max3_f32 v249, v249, v150, v151\n\t"
      "v_max3_f32 v248, v248, v152, v153\n\t"
      "v_max3_f32 v249, v249, v154, v155\n\t"
      "v_max3_f32 v248, v248, v156, v157\n\t"
      "v_max3_f32 v249, v249, v158, v159\n\t"
      "s_waitcnt vmcnt(6)\n\t"
      "s_mov_b64 exec, 1\n\t"
      "buffer_store_dword v238, off, s[44:47], s30 sc1\n\t"
      "s_mov_b64 exec, -1\n\t"
      "ds_read_b64_tr_b16 v[200:201], v8 offset:0\n\t"
      "ds_read_b64_tr_b16 v[202:203], v9 offset:0\n\t"
      "ds_read_b64_tr_b16 v[204:205], v8 offset:256\n\t"
      "ds_read_b64_tr_b16 v[206:207], v9 offset:256\n\t"
      "v_sub_u32 v8, v8, v10\n\t"
      "v_sub_u32 v9, v9, v10\n\t"
      "ds_read_b64_tr_b16 v[208:209], v8 offset:0\n\t"
      "ds_read_b64_tr_b16 v[210:211], v9 offset:0\n\t"
      "ds_read_b64_tr_b16 v[212:213], v8 offset:256\n\t"
      "ds_read_b64_tr_b16 v[214:215], v9 offset:256\n\t"
      "v_sub_u32 v8, v8, v10\n\t"
      "v_sub_u32 v9, v9, v10\n\t"
      "v_max_f32 v248, v248, v249\n\t"
      "v_mov_b32 v176, 0x3f803f80\n\t"
      "v_mov_b32 v249, v248\n\t"
      "v_mov_b32 v177, 0x3f80\n\t"
      "v_mov_b32 v178, 0\n\t"
      "v_permlane32_swap_b32 v248, v249\n\t"
      "v_mov_b32 v179, 0\n\t"
      "v_mov_b32 v182, 0\n\t"
      "v_max_f32 v248, v248, v249\n\t"
      "v_xor_b32 v248, 0x80000000, v248\n\t"
      "v_and_b32 v249, 0xffff0000, v248\n\t"
      "v_sub_f32 v248, v248, v249\n\t"
      "v_and_b32 v239, 0xffff0000, v248\n\t"
      "v_sub_f32 v248, v248, v239\n\t"
      "v_lshrrev_b32 v249, 16, v249\n\t"
      "v_or_b32 v180, v249, v239\n\t"
      "v_lshrrev_b32 v181, 16, v248\n\t"
      "v_mov_b32 v183, 0\n\t"
      "s_mov_b32 exec_lo, 0\n\t"
      "v_mov_b32 v180, 0\n\t"
      "v_mov_b32 v181, 0\n\t"
      "s_mov_b32 exec_lo, -1\n\t"
      "s_nop 1\n\t"
      "v_mfma_f32_32x32x16_bf16 v[144:159], v[176:179], v[180:183], v[144:159]\n\t"
      "v_mfma_f32_32x32x16_bf16 v[128:143], v[176:179], v[180:183], v[128:143]\n\t"
      "v_mfma_f32_32x32x16_bf16 v[112:127], v[176:179], v[180:183], v[112:127]\n\t"
      "v_mov_b32 v235, v13\n\t"
      "v_mov_b32 v237, v13\n\t"
      "s_mov_b32 exec_lo, 0\n\t"
      "v_subrev_u32 v237, 512, v237\n\t"
      "s_mov_b32 exec_lo, 0xffff0000\n\t"
      "s_mov_b32 exec_hi, 0xffff0000\n\t"
      "v_mov_b32 v237, 0x7ffffff0\n\t"
      "s_mov_b64 exec, -1\n\t"
      "s_sub_u32 s48, 73728, s20\n\t"
      "v_add_u32 v232, s48, v12\n\t"
      "v_add_u32 v236, s20, v12\n\t"
      "v_add_u32 v233, 36864, v232\n\t"
      "v_add_u32 v234, 36864, v236\n\t"
      "v_mov_b32 v241, v14\n\t"
      "v_mov_b32 v242, v15\n\t"
      "v_exp_f32 v144, v144\n\t"
      "v_exp_f32 v145, v145\n\t"
      "v_exp_f32 v146, v146\n\t"
      "v_exp_f32 v147, v147\n\t"
      "v_exp_f32 v148, v148\n\t"
      "v_exp_f32 v149, v149\n\t"
      "v_exp_f32 v150, v150\n\t"
      "v_exp_f32 v151, v151\n\t"
      "v_exp_f32 v152, v152\n\t"
      "v_exp_f32 v153, v153\n\t"
      "v_exp_f32 v154, v154\n\t"
      "v_exp_f32 v155, v155\n\t"
      "v_exp_f32 v156, v156\n\t"
      "v_exp_f32 v157, v157\n\t"
      "v_exp_f32 v158, v158\n\t"
      "v_exp_f32 v159, v159\n\t"
      "v_cvt_pk_bf16_f32 v184, v144, v145\n\t"
      "v_cndmask_b32 v144, 0, v144, s[4:5]\n\t"
      "v_cndmask_b32 v145, 0, v145, s[6:7]\n\t"
      "v_cvt_pk_bf16_f32 v216, v144, v145\n\t"
      "v_xor_b32 v184, v184, v216\n\t"
      "v_cvt_pk_bf16_f32 v185, v146, v147\n\t"
      "v_cndmask_b32 v146, 0, v146, s[8:9]\n\t"
      "v_cndmask_b32 v147, 0, v147, s[10:11]\n\t"
      "v_cvt_pk_bf16_f32 v217, v146, v147\n\t"
      "v_xor_b32 v185, v185, v217\n\t"
      "s_add_u32 m0, s50, 36864\n\t"
      "s_add_u32 s49, s27, 147456\n\t"
      "buffer_load_dwordx4 v252, s[88:91], s49 offen lds\n\t"
      "v_cvt_pk_bf16_f32 v186, v148, v149\n\t"
      "v_cndmask_b32 v148, 0, v148, s[12:13]\n\t"
      "v_cndmask_b32 v149, 0, v149, s[14:15]\n\t"
      "v_cvt_pk_bf16_f32 v218, v148, v149\n\t"
      "v_xor_b32 v186, v186, v218\n\t"
      "v_cvt_pk_bf16_f32 v187, v150, v151\n\t"
      "v_cndmask_b32 v150, 0, v150, s[16:17]\n\t"
      "v_cndmask_b32 v151, 0, v151, s[18:19]\n\t"
      "v_cvt_pk_bf16_f32 v219, v150, v151\n\t"
      "v_xor_b32 v187, v187, v219\n\t"
      "v_cvt_pk_bf16_f32 v188, v152, v153\n\t"
      "v_cndmask_b32 v152, 0, v152, s[52:53]\n\t"
      "v_cndmask_b32 v153, 0, v153, s[54:55]\n\t"
      "v_cvt_pk_bf16_f32 v220, v152, v153\n\t"
      "v_xor_b32 v188, v188, v220\n\t"
      "s_add_u32 m0, s50, 37888\n\t"
      "s_add_u32 s49, s27, 147456\n\t"
      "buffer_load_dwordx4 v253, s[88:91], s49 offen lds\n\t"
      "v_cvt_pk_bf16_f32 v189, v154, v155\n\t"
      "v_cndmask_b32 v154, 0, v154, s[56:57]\n\t"
      "v_cndmask_b32 v155, 0, v155, s[58:59]\n\t"
      "v_cvt_pk_bf16_f32 v221, v154, v155\n\t"
      "v_xor_b32 v189, v189, v221\n\t"
      "v_cvt_pk_bf16_f32 v190, v156, v157\n\t"
      "v_cndmask_b32 v156, 0, v156, s[62:63]\n\t"
      "v_cndmask_b32 v157, 0, v157, s[64:65]\n\t"
      "v_cvt_pk_bf16_f32 v222, v156, v157\n\t"
      "v_xor_b32 v190, v190, v222\n\t"
      "v_cvt_pk_bf16_f32 v191, v158, v159\n\t"
      "v_cndmask_b32 v158, 0, v158, s[66:67]\n\t"
      "v_cndmask_b32 v159, 0, v159, s[42:43]\n\t"
      "v_cvt_pk_bf16_f32 v223, v158, v159\n\t"
      "v_xor_b32 v191, v191, v223\n\t"
      "s_add_u32 m0, s50, 38912\n\t"
      "s_add_u32 s49, s27, 147456\n\t"
      "s_mov_b64 exec, 0xffff\n\t"
      "buffer_load_dwordx4 v254, s[88:91], s49 offen lds\n\t"
      "s_mov_b64 exec, -1\n\t"
      "v_exp_f32 v128, v128\n\t"
      "v_exp_f32 v129, v129\n\t"
      "s_add_u32 m0, s50, 55296\n\t"
      "s_add_u32 s49, s27, 221184\n\t"
      "buffer_load_dwordx4 v252, s[88:91], s49 offen lds\n\t"
      "v_exp_f32 v130, v130\n\t"
      "v_exp_f32 v131, v131\n\t"
      "v_cvt_pk_bf16_f32 v224, v128, v129\n\t"
      "s_waitcnt lgkmcnt(4)\n\t"
      "v_mfma_f32_32x32x16_bf16 v[160:175], v[200:203], v[216:219], 0\n\t"
      "v_exp_f32 v132, v132\n\t"
      "v_exp_f32 v133, v133\n\t"
      "v_cvt_pk_bf16_f32 v225, v130, v131\n\t"
      "s_add_u32 m0, s50, 56320\n\t"
      "s_add_u32 s49, s27, 221184\n\t"
      "buffer_load_dwordx4 v253, s[88:91], s49 offen lds\n\t"
      "v_exp_f32 v134, v134\n\t"
      "v_exp_f32 v135, v135\n\t"
      "v_cvt_pk_bf16_f32 v226, v132, v133\n\t"
      "v_mfma_f32_32x32x16_bf16 v[160:175], v[204:207], v[220:223], v[160:175]\n\t"
      "v_exp_f32 v136, v136\n\t"
      "v_exp_f32 v137, v137\n\t"
      "v_cvt_pk_bf16_f32 v227, v134, v135\n\t"
      "ds_read_b64_tr_b16 v[200:201], v8 offset:0\n\t"
      "ds_read_b64_tr_b16 v[202:203], v9 offset:0\n\t"
      "ds_read_b64_tr_b16 v[204:205], v8 offset:256\n\t"
      "ds_read_b64_tr_b16 v[206:207], v9 offset:256\n\t"
      "v_sub_u32 v8, v8, v10\n\t"
      "v_sub_u32 v9, v9, v10\n\t"
      "v_exp_f32 v138, v138\n\t"
      "v_exp_f32 v139, v139\n\t"
      "v_cvt_pk_bf16_f32 v228, v136, v137\n\t"
      "v_mfma_f32_32x32x16_bf16 v[96:111], v[176:179], v[180:183], v[96:111]\n\t"
      "v_exp_f32 v140, v140\n\t"
      "v_exp_f32 v141, v141\n\t"
      "v_cvt_pk_bf16_f32 v229, v138, v139\n\t"
      "s_add_u32 m0, s50, 57344\n\t"
      "s_add_u32 s49, s27, 221184\n\t"
      "s_mov_b64 exec, 0xffff\n\t"
      "buffer_load_dwordx4 v254, s[88:91], s49 offen lds\n\t"
      "s_mov_b64 exec, -1\n\t"
      "v_exp_f32 v142, v142\n\t"
      "v_exp_f32 v143, v143\n\t"
      "v_cvt_pk_bf16_f32 v230, v140, v141\n\t"
      "v_cvt_pk_bf16_f32 v231, v142, v143\n\t"
      "v_exp_f32 v112, v112\n\t"
      "v_exp_f32 v113, v113\n\t"
      "s_add_u32 m0, s51, 0\n\t"
      "s_add_u32 s49, s27, 294912\n\t"
      "buffer_load_dwordx4 v252, s[88:91], s49 offen lds\n\t"
      "v_exp_f32 v114, v114\n\t"
      "v_exp_f32 v115, v115\n\t"
      "v_cvt_pk_bf16_f32 v216, v112, v113\n\t"
      "s_waitcnt lgkmcnt(4)\n\t"
      "v_mfma_f32_32x32x16_bf16 v[160:175], v[208:211], v[224:227], v[160:175]\n\t"
      "v_exp_f32 v116, v116\n\t"
      "v_exp_f32 v117, v117\n\t"
      "v_cvt_pk_bf16_f32 v217, v114, v115\n\t"
      "s_add_u32 m0, s51, 1024\n\t"
      "s_add_u32 s49, s27, 294912\n\t"
      "buffer_load_dwordx4 v253, s[88:91], s49 offen lds\n\t"
      "v_exp_f32 v118, v118\n\t"
      "v_exp_f32 v119, v119\n\t"
      "v_cvt_pk_bf16_f32 v218, v116, v117\n\t"
      "v_mfma_f32_32x32x16_bf16 v[160:175], v[212:215], v[228:231], v[160:175]\n\t"
      "v_exp_f32 v120, v120\n\t"
      "v_exp_f32 v121, v121\n\t"
      "v_cvt_pk_bf16_f32 v219, v118, v119\n\t"
      "ds_read_b64_tr_b16 v[208:209], v8 offset:0\n\t"
      "ds_read_b64_tr_b16 v[210:211], v9 offset:0\n\t"
      "ds_read_b64_tr_b16 v[212:213], v8 offset:256\n\t"
      "ds_read_b64_tr_b16 v[214:215], v9 offset:256\n\t"
      "v_sub_u32 v8, v8, v10\n\t"
      "v_sub_u32 v9, v9, v10\n\t"
      "v_exp_f32 v122, v122\n\t"
      "v_exp_f32 v123, v123\n\t"
      "v_cvt_pk_bf16_f32 v220, v120, v121\n\t"
      "v_mfma_f32_32x32x16_bf16 v[80:95], v[176:179], v[180:183], v[80:95]\n\t"
      "v_exp_f32 v124, v124\n\t"
      "v_exp_f32 v125, v125\n\t"
      "v_cvt_pk_bf16_f32 v221, v122, v123\n\t"
      "s_add_u32 m0, s51, 2048\n\t"
      "s_add_u32 s49, s27, 294912\n\t"
      "s_mov_b64 exec, 0xffff\n\t"
      "buffer_load_dwordx4 v254, s[88:91], s49 offen lds\n\t"
      "s_mov_b64 exec, -1\n\t"
      "v_exp_f32 v126, v126\n\t"
      "v_exp_f32 v127, v127\n\t"
      "v_cvt_pk_bf16_f32 v222, v124, v125\n\t"
      "v_cvt_pk_bf16_f32 v223, v126, v127\n\t"
      "v_exp_f32 v96, v96\n\t"
      "v_exp_f32 v97, v97\n\t"
      "s_add_u32 m0, s51, 18432\n\t"
      "s_add_u32 s49, s27, 368640\n\t"
      "buffer_load_dwordx4 v252, s[88:91], s49 offen lds\n\t"
      "v_exp_f32 v98, v98\n\t"
      "v_exp_f32 v99, v99\n\t"
      "v_cvt_pk_bf16_f32 v224, v96, v97\n\t"
      "s_waitcnt lgkmcnt(4)\n\t"
      "v_mfma_f32_32x32x16_bf16 v[160:175], v[200:203], v[216:219], v[160:175]\n\t"
      "v_exp_f32 v100, v100\n\t"
      "v_exp_f32 v101, v101\n\t"
      "v_cvt_pk_bf16_f32 v225, v98, v99\n\t"
      "s_add_u32 m0, s51, 19456\n\t"
      "s_add_u32 s49, s27, 368640\n\t"
      "buffer_load_dwordx4 v253, s[88:91], s49 offen lds\n\t"
      "v_exp_f32 v102, v102\n\t"
      "v_exp_f32 v103, v103\n\t"
      "v_cvt_pk_bf16_f32 v226, v100, v101\n\t"
      "v_mfma_f32_32x32x16_bf16 v[160:175], v[204:207], v[220:223], v[160:175]\n\t"
      "v_exp_f32 v104, v104\n\t"
      "v_exp_f32 v105, v105\n\t"
      "v_cvt_pk_bf16_f32 v227, v102, v103\n\t"
      "ds_read_b64_tr_b16 v[200:201], v8 offset:0\n\t"
      "ds_read_b64_tr_b16 v[202:203], v9 offset:0\n\t"
      "ds_read_b64_tr_b16 v[204:205], v8 offset:256\n\t"
      "ds_read_b64_tr_b16 v[206:207], v9 offset:256\n\t"
      "v_sub_u32 v8, v8, v10\n\t"
      "v_sub_u32 v9, v9, v10\n\t"
      "v_exp_f32 v106, v106\n\t"
      "v_exp_f32 v107, v107\n\t"
      "v_cvt_pk_bf16_f32 v228, v104, v105\n\t"
      "v_mfma_f32_32x32x16_bf16 v[64:79], v[176:179], v[180:183], v[64:79]\n\t"
      "v_exp_f32 v108, v108\n\t"
      "v_exp_f32 v109, v109\n\t"
      "v_cvt_pk_bf16_f32 v229, v106, v107\n\t"
      "s_add_u32 m0, s51, 20480\n\t"
      "s_add_u32 s49, s27, 368640\n\t"
      "s_mov_b64 exec, 0xffff\n\t"
      "buffer_load_dwordx4 v254, s[88:91], s49 offen lds\n\t"
      "s_mov_b64 exec, -1\n\t"
      "v_exp_f32 v110, v110\n\t"
      "v_exp_f32 v111, v111\n\t"
      "v_cvt_pk_bf16_f32 v230, v108, v109\n\t"
      "v_cvt_pk_bf16_f32 v231, v110, v111\n\t"
      "s_waitcnt vmcnt(9)\n\t"
      "s_barrier\n\t"
      "v_exp_f32 v80, v80\n\t"
      "v_exp_f32 v81, v81\n\t"
      "v_exp_f32 v82, v82\n\t"
      "v_exp_f32 v83, v83\n\t"
      "v_cvt_pk_bf16_f32 v216, v80, v81\n\t"
      "s_waitcnt lgkmcnt(4)\n\t"
      "v_mfma_f32_32x32x16_bf16 v[160:175], v[208:211], v[224:227], v[160:175]\n\t"
      "v_exp_f32 v84, v84\n\t"
      "v_exp_f32 v85, v85\n\t"
      "v_cvt_pk_bf16_f32 v217, v82, v83\n\t"
      "v_exp_f32 v86, v86\n\t"
      "v_exp_f32 v87, v87\n\t"
      "v_cvt_pk_bf16_f32 v218, v84, v85\n\t"
      "v_mfma_f32_32x32x16_bf16 v[160:175], v[212:215], v[228:231], v[160:175]\n\t"
      "v_exp_f32 v88, v88\n\t"
      "v_exp_f32 v89, v89\n\t"
      "v_cvt_pk_bf16_f32 v219, v86, v87\n\t"
      "ds_read_b64_tr_b16 v[208:209], v8 offset:0\n\t"
      "ds_read_b64_tr_b16 v[210:211], v9 offset:0\n\t"
      "ds_read_b64_tr_b16 v[212:213], v8 offset:256\n\t"
      "ds_read_b64_tr_b16 v[214:215], v9 offset:256\n\t"
      "v_sub_u32 v8, v8, v10\n\t"
      "v_sub_u32 v9, v9, v10\n\t"
      "v_exp_f32 v90, v90\n\t"
      "v_exp_f32 v91, v91\n\t"
      "v_cvt_pk_bf16_f32 v220, v88, v89\n\t"
      "v_mfma_f32_32x32x16_bf16 v[48:63], v[176:179], v[180:183], v[48:63]\n\t"
      "v_exp_f32 v92, v92\n\t"
      "v_exp_f32 v93, v93\n\t"
      "v_cvt_pk_bf16_f32 v221, v90, v91\n\t"
      "v_exp_f32 v94, v94\n\t"
      "v_exp_f32 v95, v95\n\t"
      "v_cvt_pk_bf16_f32 v222, v92, v93\n\t"
      "v_cvt_pk_bf16_f32 v223, v94, v95\n\t"
      "v_exp_f32 v64, v64\n\t"
      "v_exp_f32 v65, v65\n\t"
      "v_exp_f32 v66, v66\n\t"
      "v_exp_f32 v67, v67\n\t"
      "v_cvt_pk_bf16_f32 v224, v64, v65\n\t"
      "s_waitcnt lgkmcnt(4)\n\t"
      "v_mfma_f32_32x32x16_bf16 v[160:175], v[200:203], v[216:219], v[160:175]\n\t"
      "v_exp_f32 v68, v68\n\t"
      "v_exp_f32 v69, v69\n\t"
      "v_cvt_pk_bf16_f32 v225, v66, v67\n\t"
      "v_exp_f32 v70, v70\n\t"
      "v_exp_f32 v71, v71\n\t"
      "v_cvt_pk_bf16_f32 v226, v68, v69\n\t"
      "v_mfma_f32_32x32x16_bf16 v[160:175], v[204:207], v[220:223], v[160:175]\n\t"
      "v_exp_f32 v72, v72\n\t"
      "v_exp_f32 v73, v73\n\t"
      "v_cvt_pk_bf16_f32 v227, v70, v71\n\t"
      "ds_read_b64_tr_b16 v[200:201], v8 offset:0\n\t"
      "ds_read_b64_tr_b16 v[202:203], v9 offset:0\n\t"
      "ds_read_b64_tr_b16 v[204:205], v8 offset:256\n\t"
      "ds_read_b64_tr_b16 v[206:207], v9 offset:256\n\t"
      "v_sub_u32 v8, v8, v10\n\t"
      "v_sub_u32 v9, v9, v10\n\t"
      "v_exp_f32 v74, v74\n\t"
      "v_exp_f32 v75, v75\n\t"
      "v_cvt_pk_bf16_f32 v228, v72, v73\n\t"
      "v_mfma_f32_32x32x16_bf16 v[32:47], v[176:179], v[180:183], v[32:47]\n\t"
      "v_exp_f32 v76, v76\n\t"
      "v_exp_f32 v77, v77\n\t"
      "v_cvt_pk_bf16_f32 v229, v74, v75\n\t"
      "v_exp_f32 v78, v78\n\t"
      "v_exp_f32 v79, v79\n\t"
      "v_cvt_pk_bf16_f32 v230, v76, v77\n\t"
      "v_cvt_pk_bf16_f32 v231, v78, v79\n\t"
      "s_and_b32 s49, s30, 0xffffffc0\n\t"
      "s_mov_b32 s60, s49\n\t"
      "v_mov_b32 v239, v238\n\t"
      "s_mov_b32 exec_lo, 0xf\n\t"
      "s_mov_b32 exec_hi, 0\n\t"
      "buffer_load_dword v239, v246, s[44:47], s60 offen sc1\n\t"
      "s_mov_b64 exec, -1\n\t"
      "v_exp_f32 v48, v48\n\t"
      "v_exp_f32 v49, v49\n\t"
      "v_exp_f32 v50, v50\n\t"
      "v_exp_f32 v51, v51\n\t"
      "v_cvt_pk_bf16_f32 v216, v48, v49\n\t"
      "s_waitcnt lgkmcnt(4)\n\t"
      "v_mfma_f32_32x32x16_bf16 v[160:175], v[208:211], v[224:227], v[160:175]\n\t"
      "v_exp_f32 v52, v52\n\t"
      "v_exp_f32 v53, v53\n\t"
      "v_cvt_pk_bf16_f32 v217, v50, v51\n\t"
      "v_exp_f32 v54, v54\n\t"
      "v_exp_f32 v55, v55\n\t"
      "v_cvt_pk_bf16_f32 v218, v52, v53\n\t"
      "v_mfma_f32_32x32x16_bf16 v[160:175], v[212:215], v[228:231], v[160:175]\n\t"
      "v_exp_f32 v56, v56\n\t"
      "v_exp_f32 v57, v57\n\t"
      "v_cvt_pk_bf16_f32 v219, v54, v55\n\t"
      "ds_read_b64_tr_b16 v[208:209], v8 offset:0\n\t"
      "ds_read_b64_tr_b16 v[210:211], v9 offset:0\n\t"
      "ds_read_b64_tr_b16 v[212:213], v8 offset:256\n\t"
      "ds_read_b64_tr_b16 v[214:215], v9 offset:256\n\t"
      "v_sub_u32 v8, v8, v10\n\t"
      "v_sub_u32 v9, v9, v10\n\t"
      "v_exp_f32 v58, v58\n\t"
      "v_exp_f32 v59, v59\n\t"
      "v_cvt_pk_bf16_f32 v220, v56, v57\n\t"
      "v_exp_f32 v60, v60\n\t"
      "v_exp_f32 v61, v61\n\t"
      "v_cvt_pk_bf16_f32 v221, v58, v59\n\t"
      "v_exp_f32 v62, v62\n\t"
      "v_exp_f32 v63, v63\n\t"
      "v_cvt_pk_bf16_f32 v222, v60, v61\n\t"
      "v_cvt_pk_bf16_f32 v223, v62, v63\n\t"
      "s_waitcnt vmcnt(0)\n\t"
      "v_cmp_ne_u32 vcc, v239, v238\n\t"
      "s_nop 1\n\t"
      "s_cmp_lg_u64 vcc, 0\n\t"
      "s_cbranch_scc1 Lsp2_%=\n\t"
      "Lsg2_%=:\n\t"
      "s_add_u32 s48, s28, 0\n\t"
      "buffer_load_dwordx4 v[96:99], v235, s[44:47], s48 offen offset:0 sc1\n\t"
      "s_add_u32 s48, s28, 0\n\t"
      "buffer_load_dwordx4 v[100:103], v235, s[44:47], s48 offen offset:256 sc1\n\t"
      "s_add_u32 s48, s28, 4096\n\t"
      "buffer_load_dwordx4 v[104:107], v235, s[44:47], s48 offen offset:0 sc1\n\t"
      "s_add_u32 s48, s28, 4096\n\t"
      "buffer_load_dwordx4 v[108:111], v235, s[44:47], s48 offen offset:256 sc1\n\t"
      "v_exp_f32 v32, v32\n\t"
      "v_exp_f32 v33, v33\n\t"
      "s_add_u32 s48, s28, 8192\n\t"
      "buffer_load_dwordx4 v[112:115], v235, s[44:47], s48 offen offset:0 sc1\n\t"
      "v_exp_f32 v34, v34\n\t"
      "v_exp_f32 v35, v35\n\t"
      "v_cvt_pk_bf16_f32 v224, v32, v33\n\t"
      "s_waitcnt lgkmcnt(4)\n\t"
      "v_mfma_f32_32x32x16_bf16 v[160:175], v[200:203], v[216:219], v[160:175]\n\t"
      "v_exp_f32 v36, v36\n\t"
      "v_exp_f32 v37, v37\n\t"
      "v_cvt_pk_bf16_f32 v225, v34, v35\n\t"
      "s_add_u32 s48, s28, 8192\n\t"
      "buffer_load_dwordx4 v[116:119], v235, s[44:47], s48 offen offset:256 sc1\n\t"
      "v_exp_f32 v38, v38\n\t"
      "v_exp_f32 v39, v39\n\t"
      "v_cvt_pk_bf16_f32 v226, v36, v37\n\t"
      "v_mfma_f32_32x32x16_bf16 v[160:175], v[204:207], v[220:223], v[160:175]\n\t"
      "v_exp_f32 v40, v40\n\t"
      "v_exp_f32 v41, v41\n\t"
      "v_cvt_pk_bf16_f32 v227, v38, v39\n\t"
      "ds_read_b64_tr_b16 v[200:201], v8 offset:0\n\t"
      "ds_read_b64_tr_b16 v[202:203], v9 offset:0\n\t"
      "ds_read_b64_tr_b16 v[204:205], v8 offset:256\n\t"
      "ds_read_b64_tr_b16 v[206:207], v9 offset:256\n\t"
      "v_sub_u32 v8, v8, v10\n\t"
      "v_sub_u32 v9, v9, v10\n\t"
      "v_exp_f32 v42, v42\n\t"
      "v_exp_f32 v43, v43\n\t"
      "v_cvt_pk_bf16_f32 v228, v40, v41\n\t"
      "v_exp_f32 v44, v44\n\t"
      "v_exp_f32 v45, v45\n\t"
      "v_cvt_pk_bf16_f32 v229, v42, v43\n\t"
      "s_add_u32 s48, s28, 12288\n\t"
      "buffer_load_dwordx4 v[120:123], v235, s[44:47], s48 offen offset:0 sc1\n\t"
      "v_exp_f32 v46, v46\n\t"
      "v_exp_f32 v47, v47\n\t"
      "v_cvt_pk_bf16_f32 v230, v44, v45\n\t"
      "v_cvt_pk_bf16_f32 v231, v46, v47\n\t"
      "s_waitcnt lgkmcnt(4)\n\t"
      "v_mfma_f32_32x32x16_bf16 v[160:175], v[208:211], v[224:227], v[160:175]\n\t"
      "v_mfma_f32_32x32x16_bf16 v[160:175], v[212:215], v[228:231], v[160:175]\n\t"
      "s_waitcnt lgkmcnt(0)\n\t"
      "v_mfma_f32_32x32x16_bf16 v[160:175], v[200:203], v[184:187], v[160:175]\n\t"
      "v_mfma_f32_32x32x16_bf16 v[160:175], v[204:207], v[188:191], v[160:175]\n\t"
      "s_add_u32 s48, s28, 12288\n\t"
      "buffer_load_dwordx4 v[124:127], v235, s[44:47], s48 offen offset:256 sc1\n\t"
      "s_add_u32 s48, s28, 16384\n\t"
      "buffer_load_dwordx4 v[128:131], v235, s[44:47], s48 offen offset:0 sc1\n\t"
      "s_add_u32 s48, s28, 16384\n\t"
      "buffer_load_dwordx4 v[132:135], v235, s[44:47], s48 offen offset:256 sc1\n\t"
      "s_add_u32 s48, s28, 20480\n\t"
      "buffer_load_dwordx4 v[136:139], v235, s[44:47], s48 offen offset:0 sc1\n\t"
      "s_add_u32 s48, s28, 20480\n\t"
      "buffer_load_dwordx4 v[140:143], v235, s[44:47], s48 offen offset:256 sc1\n\t"
      "s_lshr_b32 s48, s39, 2\n\t"
      "s_lshl_b32 s48, s48, 11\n\t"
      "s_add_u32 s24, s29, s48\n\t"
      "v_rcp_f32 v239, v172\n\t"
      "v_cndmask_b32 v168, v168, v170, s[40:41]\n\t"
      "v_cndmask_b32 v169, v169, v171, s[40:41]\n\t"
      "v_mul_f32 v160, v160, v239\n\t"
      "v_mul_f32 v161, v161, v239\n\t"
      "v_mul_f32 v162, v162, v239\n\t"
      "v_mul_f32 v163, v163, v239\n\t"
      "v_mul_f32 v164, v164, v239\n\t"
      "v_mul_f32 v165, v165, v239\n\t"
      "v_mul_f32 v166, v166, v239\n\t"
      "v_mul_f32 v167, v167, v239\n\t"
      "v_mul_f32 v168, v168, v239\n\t"
      "v_mul_f32 v169, v169, v239\n\t"
      "v_cvt_pk_bf16_f32 v216, v160, v161\n\t"
      "v_cvt_pk_bf16_f32 v217, v162, v163\n\t"
      "v_cvt_pk_bf16_f32 v218, v164, v165\n\t"
      "v_cvt_pk_bf16_f32 v219, v166, v167\n\t"
      "v_cvt_pk_bf16_f32 v220, v168, v169\n\t"
      "s_bfe_u32 s48, s31, 0x30002\n\t"
      "s_lshl_b32 s48, s48, 10\n\t"
      "s_add_u32 s48, s48, s29\n\t"
      "buffer_store_dwordx4 v[216:219], v240, s[44:47], s48 offen sc1\n\t"
      "buffer_store_dword v220, v11, s[44:47], s29 offen sc1\n\t"
      "s_waitcnt vmcnt(12)\n\t"
      "ds_read_b64_tr_b16 v[176:177], v232 offset:0\n\t"
      "ds_read_b64_tr_b16 v[178:179], v232 offset:2304\n\t"
      "ds_read_b64_tr_b16 v[180:181], v232 offset:32\n\t"
      "ds_read_b64_tr_b16 v[182:183], v232 offset:2336\n\t"
      "ds_read_b64_tr_b16 v[184:185], v232 offset:64\n\t"
      "ds_read_b64_tr_b16 v[186:187], v232 offset:2368\n\t"
      "ds_read_b64_tr_b16 v[188:189], v232 offset:96\n\t"
      "ds_read_b64_tr_b16 v[190:191], v232 offset:2400\n\t"
      "ds_read_b64_tr_b16 v[192:193], v232 offset:128\n\t"
      "ds_read_b64_tr_b16 v[194:195], v232 offset:2432\n\t"
      "s_waitcnt lgkmcnt(8)\n\t"
      "v_mfma_f32_16x16x32_bf16 v[0:3], v[176:179], v[96:99], 0\n\t"
      "ds_read_b64_tr_b16 v[196:197], v232 offset:160\n\t"
      "ds_read_b64_tr_b16 v[198:199], v232 offset:2464\n\t"
      "v_mfma_f32_16x16x32_bf16 v[36:39], v[176:179], v[100:103], 0\n\t"
      "s_waitcnt lgkmcnt(8)\n\t"
      "v_mfma_f32_16x16x32_bf16 v[4:7], v[180:183], v[96:99], 0\n\t"
      "ds_read_b64_tr_b16 v[176:177], v232 offset:192\n\t"
      "ds_read_b64_tr_b16 v[178:179], v232 offset:2496\n\t"
      "v_mfma_f32_16x16x32_bf16 v[40:43], v[180:183], v[100:103], 0\n\t"
      "s_waitcnt lgkmcnt(8)\n\t"
      "v_mfma_f32_16x16x32_bf16 v[8:11], v[184:187], v[96:99], 0\n\t"
      "ds_read_b64_tr_b16 v[180:181], v232 offset:224\n\t"
      "ds_read_b64_tr_b16 v[182:183], v232 offset:2528\n\t"
      "v_mfma_f32_16x16x32_bf16 v[44:47], v[184:187], v[100:103], 0\n\t"
      "s_waitcnt lgkmcnt(8)\n\t"
      "v_mfma_f32_16x16x32_bf16 v[12:15], v[188:191], v[96:99], 0\n\t"
      "ds_read_b64_tr_b16 v[184:185], v232 offset:256\n\t"
      "ds_read_b64_tr_b16 v[186:187], v232 offset:2560\n\t"
      "v_mfma_f32_16x16x32_bf16 v[48:51], v[188:191], v[100:103], 0\n\t"
      "s_waitcnt lgkmcnt(8)\n\t"
      "v_mfma_f32_16x16x32_bf16 v[16:19], v[192:195], v[96:99], 0\n\t"
      "ds_read_b64_tr_b16 v[188:189], v232 offset:18432\n\t"
      "ds_read_b64_tr_b16 v[190:191], v232 offset:20736\n\t"
      "v_mfma_f32_16x16x32_bf16 v[52:55], v[192:195], v[100:103], 0\n\t"
      "s_waitcnt lgkmcnt(8)\n\t"
      "v_mfma_f32_16x16x32_bf16 v[20:23], v[196:199], v[96:99], 0\n\t"
      "ds_read_b64_tr_b16 v[192:193], v232 offset:18464\n\t"
      "ds_read_b64_tr_b16 v[194:195], v232 offset:20768\n\t"
      "v_mfma_f32_16x16x32_bf16 v[56:59], v[196:199], v[100:103], 0\n\t"
      "s_waitcnt lgkmcnt(8)\n\t"
      "v_mfma_f32_16x16x32_bf16 v[24:27], v[176:179], v[96:99], 0\n\t"
      "ds_read_b64_tr_b16 v[196:197], v232 offset:18496\n\t"
      "ds_read_b64_tr_b16 v[198:199], v232 offset:20800\n\t"
      "v_mfma_f32_16x16x32_bf16 v[60:63], v[176:179], v[100:103], 0\n\t"
      "s_waitcnt lgkmcnt(8)\n\t"
      "v_mfma_f32_16x16x32_bf16 v[28:31], v[180:183], v[96:99], 0\n\t"
      "ds_read_b64_tr_b16 v[176:177], v232 offset:18528\n\t"
      "ds_read_b64_tr_b16 v[178:179], v232 offset:20832\n\t"
      "v_mfma_f32_16x16x32_bf16 v[64:67], v[180:183], v[100:103], 0\n\t"
      "s_waitcnt lgkmcnt(8)\n\t"
      "v_mfma_f32_16x16x32_bf16 v[32:35], v[184:187], v[96:99], 0\n\t"
      "ds_read_b64_tr_b16 v[180:181], v232 offset:18560\n\t"
      "ds_read_b64_tr_b16 v[182:183], v232 offset:20864\n\t"
      "v_mfma_f32_16x16x32_bf16 v[68:71], v[184:187], v[100:103], 0\n\t"
      "s_add_u32 s48, s28, 24576\n\t"
      "buffer_load_dwordx4 v[96:99], v235, s[44:47], s48 offen offset:0 sc1\n\t"
      "s_add_u32 s48, s28, 24576\n\t"
      "buffer_load_dwordx4 v[100:103], v235, s[44:47], s48 offen offset:256 sc1\n\t"
      "s_waitcnt vmcnt(12)\n\t"
      "s_waitcnt lgkmcnt(8)\n\t"
      "v_mfma_f32_16x16x32_bf16 v[0:3], v[188:191], v[104:107], v[0:3]\n\t"
      "ds_read_b64_tr_b16 v[184:185], v232 offset:18592\n\t"
      "ds_read_b64_tr_b16 v[186:187], v232 offset:20896\n\t"
      "v_mfma_f32_16x16x32_bf16 v[36:39], v[188:191], v[108:111], v[36:39]\n\t"
      "s_waitcnt lgkmcnt(8)\n\t"
      "v_mfma_f32_16x16x32_bf16 v[4:7], v[192:195], v[104:107], v[4:7]\n\t"
      "ds_read_b64_tr_b16 v[188:189], v232 offset:18624\n\t"
      "ds_read_b64_tr_b16 v[190:191], v232 offset:20928\n\t"
      "v_mfma_f32_16x16x32_bf16 v[40:43], v[192:195], v[108:111], v[40:43]\n\t"
      "s_waitcnt lgkmcnt(8)\n\t"
      "v_mfma_f32_16x16x32_bf16 v[8:11], v[196:199], v[104:107], v[8:11]\n\t"
      "ds_read_b64_tr_b16 v[192:193], v232 offset:18656\n\t"
      "ds_read_b64_tr_b16 v[194:195], v232 offset:20960\n\t"
      "v_mfma_f32_16x16x32_bf16 v[44:47], v[196:199], v[108:111], v[44:47]\n\t"
      "s_waitcnt lgkmcnt(8)\n\t"
      "v_mfma_f32_16x16x32_bf16 v[12:15], v[176:179], v[104:107], v[12:15]\n\t"
      "ds_read_b64_tr_b16 v[196:197], v232 offset:18688\n\t"
      "ds_read_b64_tr_b16 v[198:199], v232 offset:20992\n\t"
      "v_mfma_f32_16x16x32_bf16 v[48:51], v[176:179], v[108:111], v[48:51]\n\t"
      "s_waitcnt lgkmcnt(8)\n\t"
      "v_mfma_f32_16x16x32_bf16 v[16:19], v[180:183], v[104:107], v[16:19]\n\t"
      "ds_read_b64_tr_b16 v[176:177], v233 offset:0\n\t"
      "ds_read_b64_tr_b16 v[178:179], v233 offset:2304\n\t"
      "v_mfma_f32_16x16x32_bf16 v[52:55], v[180:183], v[108:111], v[52:55]\n\t"
      "s_waitcnt lgkmcnt(8)\n\t"
      "v_mfma_f32_16x16x32_bf16 v[20:23], v[184:187], v[104:107], v[20:23]\n\t"
      "ds_read_b64_tr_b16 v[180:181], v233 offset:32\n\t"
      "ds_read_b64_tr_b16 v[182:183], v233 offset:2336\n\t"
      "v_mfma_f32_16x16x32_bf16 v[56:59], v[184:187], v[108:111], v[56:59]\n\t"
      "s_waitcnt lgkmcnt(8)\n\t"
      "v_mfma_f32_16x16x32_bf16 v[24:27], v[188:191], v[104:107], v[24:27]\n\t"
      "ds_read_b64_tr_b16 v[184:185], v233 offset:64\n\t"
      "ds_read_b64_tr_b16 v[186:187], v233 offset:2368\n\t"
      "v_mfma_f32_16x16x32_bf16 v[60:63], v[188:191], v[108:111], v[60:63]\n\t"
      "s_waitcnt lgkmcnt(8)\n\t"
      "v_mfma_f32_16x16x32_bf16 v[28:31], v[192:195], v[104:107], v[28:31]\n\t"
      "ds_read_b64_tr_b16 v[188:189], v233 offset:96\n\t"
      "ds_read_b64_tr_b16 v[190:191], v233 offset:2400\n\t"
      "v_mfma_f32_16x16x32_bf16 v[64:67], v[192:195], v[108:111], v[64:67]\n\t"
      "s_waitcnt lgkmcnt(8)\n\t"
      "v_mfma_f32_16x16x32_bf16 v[32:35], v[196:199], v[104:107], v[32:35]\n\t"
      "ds_read_b64_tr_b16 v[192:193], v233 offset:128\n\t"
      "ds_read_b64_tr_b16 v[194:195], v233 offset:2432\n\t"
      "v_mfma_f32_16x16x32_bf16 v[68:71], v[196:199], v[108:111], v[68:71]\n\t"
      "s_add_u32 s48, s28, 28672\n\t"
      "buffer_load_dwordx4 v[104:107], v235, s[44:47], s48 offen offset:0 sc1\n\t"
      "s_add_u32 s48, s28, 28672\n\t"
      "buffer_load_dwordx4 v[108:111], v235, s[44:47], s48 offen offset:256 sc1\n\t"
      "s_waitcnt vmcnt(12)\n\t"
      "s_barrier\n\t"
      "s_waitcnt lgkmcnt(8)\n\t"
      "v_mfma_f32_16x16x32_bf16 v[0:3], v[176:179], v[112:115], v[0:3]\n\t"
      "ds_read_b64_tr_b16 v[196:197], v233 offset:160\n\t"
      "ds_read_b64_tr_b16 v[198:199], v233 offset:2464\n\t"
      "v_mfma_f32_16x16x32_bf16 v[36:39], v[176:179], v[116:119], v[36:39]\n\t"
      "s_add_u32 m0, s51, 36864\n\t"
      "s_add_u32 s49, s27, 442368\n\t"
      "buffer_load_dwordx4 v252, s[88:91], s49 offen lds\n\t"
      "s_waitcnt lgkmcnt(8)\n\t"
      "v_mfma_f32_16x16x32_bf16 v[4:7], v[180:183], v[112:115], v[4:7]\n\t"
      "ds_read_b64_tr_b16 v[176:177], v233 offset:192\n\t"
      "ds_read_b64_tr_b16 v[178:179], v233 offset:2496\n\t"
      "v_mfma_f32_16x16x32_bf16 v[40:43], v[180:183], v[116:119], v[40:43]\n\t"
      "s_waitcnt lgkmcnt(8)\n\t"
      "v_mfma_f32_16x16x32_bf16 v[8:11], v[184:187], v[112:115], v[8:11]\n\t"
      "ds_read_b64_tr_b16 v[180:181], v233 offset:224\n\t"
      "ds_read_b64_tr_b16 v[182:183], v233 offset:2528\n\t"
      "v_mfma_f32_16x16x32_bf16 v[44:47], v[184:187], v[116:119], v[44:47]\n\t"
      "s_add_u32 m0, s51, 37888\n\t"
      "s_add_u32 s49, s27, 442368\n\t"
      "buffer_load_dwordx4 v253, s[88:91], s49 offen lds\n\t"
      "s_waitcnt lgkmcnt(8)\n\t"
      "v_mfma_f32_16x16x32_bf16 v[12:15], v[188:191], v[112:115], v[12:15]\n\t"
      "ds_read_b64_tr_b16 v[184:185], v233 offset:256\n\t"
      "ds_read_b64_tr_b16 v[186:187], v233 offset:2560\n\t"
      "v_mfma_f32_16x16x32_bf16 v[48:51], v[188:191], v[116:119], v[48:51]\n\t"
      "s_add_u32 m0, s51, 38912\n\t"
      "s_add_u32 s49, s27, 442368\n\t"
      "s_mov_b64 exec, 0xffff\n\t"
      "buffer_load_dwordx4 v254, s[88:91], s49 offen lds\n\t"
      "s_mov_b64 exec, -1\n\t"
      "s_waitcnt lgkmcnt(8)\n\t"
      "v_mfma_f32_16x16x32_bf16 v[16:19], v[192:195], v[112:115], v[16:19]\n\t"
      "ds_read_b64_tr_b16 v[188:189], v233 offset:18432\n\t"
      "ds_read_b64_tr_b16 v[190:191], v233 offset:20736\n\t"
      "v_mfma_f32_16x16x32_bf16 v[52:55], v[192:195], v[116:119], v[52:55]\n\t"
      "s_waitcnt lgkmcnt(8)\n\t"
      "v_mfma_f32_16x16x32_bf16 v[20:23], v[196:199], v[112:115], v[20:23]\n\t"
      "ds_read_b64_tr_b16 v[192:193], v233 offset:18464\n\t"
      "ds_read_b64_tr_b16 v[194:195], v233 offset:20768\n\t"
      "v_mfma_f32_16x16x32_bf16 v[56:59], v[196:199], v[116:119], v[56:59]\n\t"
      "s_add_u32 m0, s51, 55296\n\t"
      "s_add_u32 s49, s27, 516096\n\t"
      "buffer_load_dwordx4 v252, s[88:91], s49 offen lds\n\t"
      "s_waitcnt lgkmcnt(8)\n\t"
      "v_mfma_f32_16x16x32_bf16 v[24:27], v[176:179], v[112:115], v[24:27]\n\t"
      "ds_read_b64_tr_b16 v[196:197], v233 offset:18496\n\t"
      "ds_read_b64_tr_b16 v[198:199], v233 offset:20800\n\t"
      "v_mfma_f32_16x16x32_bf16 v[60:63], v[176:179], v[116:119], v[60:63]\n\t"
      "s_add_u32 m0, s51, 56320\n\t"
      "s_add_u32 s49, s27, 516096\n\t"
      "buffer_load_dwordx4 v253, s[88:91], s49 offen lds\n\t"
      "s_waitcnt lgkmcnt(8)\n\t"
      "v_mfma_f32_16x16x32_bf16 v[28:31], v[180:183], v[112:115], v[28:31]\n\t"
      "ds_read_b64_tr_b16 v[176:177], v233 offset:18528\n\t"
      "ds_read_b64_tr_b16 v[178:179], v233 offset:20832\n\t"
      "v_mfma_f32_16x16x32_bf16 v[64:67], v[180:183], v[116:119], v[64:67]\n\t"
      "s_waitcnt lgkmcnt(8)\n\t"
      "v_mfma_f32_16x16x32_bf16 v[32:35], v[184:187], v[112:115], v[32:35]\n\t"
      "ds_read_b64_tr_b16 v[180:181], v233 offset:18560\n\t"
      "ds_read_b64_tr_b16 v[182:183], v233 offset:20864\n\t"
      "v_mfma_f32_16x16x32_bf16 v[68:71], v[184:187], v[116:119], v[68:71]\n\t"
      "s_add_u32 m0, s51, 57344\n\t"
      "s_add_u32 s49, s27, 516096\n\t"
      "s_mov_b64 exec, 0xffff\n\t"
      "buffer_load_dwordx4 v254, s[88:91], s49 offen lds\n\t"
      "s_mov_b64 exec, -1\n\t"
      "s_waitcnt vmcnt(10)\n\t"
      "s_mov_b64 exec, 1\n\t"
      "buffer_store_dword v238, off, s[44:47], s31 sc1\n\t"
      "s_mov_b64 exec, -1\n\t"
      "s_add_u32 s48, s28, 32768\n\t"
      "buffer_load_dwordx4 v[112:115], v235, s[44:47], s48 offen offset:0 sc1\n\t"
      "s_add_u32 s48, s28, 32768\n\t"
      "buffer_load_dwordx4 v[116:119], v235, s[44:47], s48 offen offset:256 sc1\n\t"
      "s_waitcnt lgkmcnt(8)\n\t"
      "v_mfma_f32_16x16x32_bf16 v[0:3], v[188:191], v[120:123], v[0:3]\n\t"
      "ds_read_b64_tr_b16 v[184:185], v233 offset:18592\n\t"
      "ds_read_b64_tr_b16 v[186:187], v233 offset:20896\n\t"
      "v_mfma_f32_16x16x32_bf16 v[36:39], v[188:191], v[124:127], v[36:39]\n\t"
      "s_add_u32 m0, s50, 0\n\t"
      "s_add_u32 s49, s27, 589824\n\t"
      "buffer_load_dwordx4 v252, s[88:91], s49 offen lds\n\t"
      "s_waitcnt lgkmcnt(8)\n\t"
      "v_mfma_f32_16x16x32_bf16 v[4:7], v[192:195], v[120:123], v[4:7]\n\t"
      "ds_read_b64_tr_b16 v[188:189], v233 offset:18624\n\t"
      "ds_read_b64_tr_b16 v[190:191], v233 offset:20928\n\t"
      "v_mfma_f32_16x16x32_bf16 v[40:43], v[192:195], v[124:127], v[40:43]\n\t"
      "s_waitcnt lgkmcnt(8)\n\t"
      "v_mfma_f32_16x16x32_bf16 v[8:11], v[196:199], v[120:123], v[8:11]\n\t"
      "ds_read_b64_tr_b16 v[192:193], v233 offset:18656\n\t"
      "ds_read_b64_tr_b16 v[194:195], v233 offset:20960\n\t"
      "v_mfma_f32_16x16x32_bf16 v[44:47], v[196:199], v[124:127], v[44:47]\n\t"
      "s_add_u32 m0, s50, 1024\n\t"
      "s_add_u32 s49, s27, 589824\n\t"
      "buffer_load_dwordx4 v253, s[88:91], s49 offen lds\n\t"
      "s_waitcnt lgkmcnt(8)\n\t"
      "v_mfma_f32_16x16x32_bf16 v[12:15], v[176:179], v[120:123], v[12:15]\n\t"
      "ds_read_b64_tr_b16 v[196:197], v233 offset:18688\n\t"
      "ds_read_b64_tr_b16 v[198:199], v233 offset:20992\n\t"
      "v_mfma_f32_16x16x32_bf16 v[48:51], v[176:179], v[124:127], v[48:51]\n\t"
      "s_add_u32 m0, s50, 2048\n\t"
      "s_add_u32 s49, s27, 589824\n\t"
      "s_mov_b64 exec, 0xffff\n\t"
      "buffer_load_dwordx4 v254, s[88:91], s49 offen lds\n\t"
      "s_mov_b64 exec, -1\n\t"
      "s_waitcnt lgkmcnt(8)\n\t"
      "v_mfma_f32_16x16x32_bf16 v[16:19], v[180:183], v[120:123], v[16:19]\n\t"
      "ds_read_b64_tr_b16 v[176:177], v236 offset:0\n\t"
      "ds_read_b64_tr_b16 v[178:179], v236 offset:2304\n\t"
      "v_mfma_f32_16x16x32_bf16 v[52:55], v[180:183], v[124:127], v[52:55]\n\t"
      "s_waitcnt lgkmcnt(8)\n\t"
      "v_mfma_f32_16x16x32_bf16 v[20:23], v[184:187], v[120:123], v[20:23]\n\t"
      "ds_read_b64_tr_b16 v[180:181], v236 offset:32\n\t"
      "ds_read_b64_tr_b16 v[182:183], v236 offset:2336\n\t"
      "v_mfma_f32_16x16x32_bf16 v[56:59], v[184:187], v[124:127], v[56:59]\n\t"
      "s_add_u32 m0, s50, 18432\n\t"
      "s_add_u32 s49, s27, 663552\n\t"
      "buffer_load_dwordx4 v252, s[88:91], s49 offen lds\n\t"
      "s_waitcnt lgkmcnt(8)\n\t"
      "v_mfma_f32_16x16x32_bf16 v[24:27], v[188:191], v[120:123], v[24:27]\n\t"
      "ds_read_b64_tr_b16 v[184:185], v236 offset:64\n\t"
      "ds_read_b64_tr_b16 v[186:187], v236 offset:2368\n\t"
      "v_mfma_f32_16x16x32_bf16 v[60:63], v[188:191], v[124:127], v[60:63]\n\t"
      "s_add_u32 m0, s50, 19456\n\t"
      "s_add_u32 s49, s27, 663552\n\t"
      "buffer_load_dwordx4 v253, s[88:91], s49 offen lds\n\t"
      "s_waitcnt lgkmcnt(8)\n\t"
      "v_mfma_f32_16x16x32_bf16 v[28:31], v[192:195], v[120:123], v[28:31]\n\t"
      "ds_read_b64_tr_b16 v[188:189], v236 offset:96\n\t"
      "ds_read_b64_tr_b16 v[190:191], v236 offset:2400\n\t"
      "v_mfma_f32_16x16x32_bf16 v[64:67], v[192:195], v[124:127], v[64:67]\n\t"
      "s_waitcnt lgkmcnt(8)\n\t"
      "v_mfma_f32_16x16x32_bf16 v[32:35], v[196:199], v[120:123], v[32:35]\n\t"
      "ds_read_b64_tr_b16 v[192:193], v236 offset:128\n\t"
      "ds_read_b64_tr_b16 v[194:195], v236 offset:2432\n\t"
      "v_mfma_f32_16x16x32_bf16 v[68:71], v[196:199], v[124:127], v[68:71]\n\t"
      "s_add_u32 m0, s50, 20480\n\t"
      "s_add_u32 s49, s27, 663552\n\t"
      "s_mov_b64 exec, 0xffff\n\t"
      "buffer_load_dwordx4 v254, s[88:91], s49 offen lds\n\t"
      "s_mov_b64 exec, -1\n\t"
      "s_add_u32 s48, s28, 36864\n\t"
      "buffer_load_dwordx4 v[120:123], v235, s[44:47], s48 offen offset:0 sc1\n\t"
      "s_add_u32 s48, s28, 36864\n\t"
      "buffer_load_dwordx4 v[124:127], v235, s[44:47], s48 offen offset:256 sc1\n\t"
      "s_waitcnt vmcnt(14)\n\t"
      "s_barrier\n\t"
      "s_waitcnt lgkmcnt(8)\n\t"
      "v_mfma_f32_16x16x32_bf16 v[0:3], v[176:179], v[128:131], v[0:3]\n\t"
      "ds_read_b64_tr_b16 v[196:197], v236 offset:160\n\t"
      "ds_read_b64_tr_b16 v[198:199], v236 offset:2464\n\t"
      "v_mfma_f32_16x16x32_bf16 v[36:39], v[176:179], v[132:135], v[36:39]\n\t"
      "s_waitcnt lgkmcnt(8)\n\t"
      "v_mfma_f32_16x16x32_bf16 v[4:7], v[180:183], v[128:131], v[4:7]\n\t"
      "ds_read_b64_tr_b16 v[176:177], v236 offset:192\n\t"
      "ds_read_b64_tr_b16 v[178:179], v236 offset:2496\n\t"
      "v_mfma_f32_16x16x32_bf16 v[40:43], v[180:183], v[132:135], v[40:43]\n\t"
      "s_add_u32 m0, s50, 36864\n\t"
      "s_add_u32 s49, s27, 737280\n\t"
      "buffer_load_dwordx4 v252, s[88:91], s49 offen lds\n\t"
      "s_waitcnt lgkmcnt(8)\n\t"
      "v_mfma_f32_16x16x32_bf16 v[8:11], v[184:187], v[128:131], v[8:11]\n\t"
      "ds_read_b64_tr_b16 v[180:181], v236 offset:224\n\t"
      "ds_read_b64_tr_b16 v[182:183], v236 offset:2528\n\t"
      "v_mfma_f32_16x16x32_bf16 v[44:47], v[184:187], v[132:135], v[44:47]\n\t"
      "s_waitcnt lgkmcnt(8)\n\t"
      "v_mfma_f32_16x16x32_bf16 v[12:15], v[188:191], v[128:131], v[12:15]\n\t"
      "ds_read_b64_tr_b16 v[184:185], v236 offset:256\n\t"
      "ds_read_b64_tr_b16 v[186:187], v236 offset:2560\n\t"
      "v_mfma_f32_16x16x32_bf16 v[48:51], v[188:191], v[132:135], v[48:51]\n\t"
      "s_waitcnt lgkmcnt(8)\n\t"
      "v_mfma_f32_16x16x32_bf16 v[16:19], v[192:195], v[128:131], v[16:19]\n\t"
      "ds_read_b64_tr_b16 v[188:189], v236 offset:18432\n\t"
      "ds_read_b64_tr_b16 v[190:191], v236 offset:20736\n\t"
      "v_mfma_f32_16x16x32_bf16 v[52:55], v[192:195], v[132:135], v[52:55]\n\t"
      "s_add_u32 m0, s50, 37888\n\t"
      "s_add_u32 s49, s27, 737280\n\t"
      "buffer_load_dwordx4 v253, s[88:91], s49 offen lds\n\t"
      "s_waitcnt lgkmcnt(8)\n\t"
      "v_mfma_f32_16x16x32_bf16 v[20:23], v[196:199], v[128:131], v[20:23]\n\t"
      "ds_read_b64_tr_b16 v[192:193], v236 offset:18464\n\t"
      "ds_read_b64_tr_b16 v[194:195], v236 offset:20768\n\t"
      "v_mfma_f32_16x16x32_bf16 v[56:59], v[196:199], v[132:135], v[56:59]\n\t"
      "s_waitcnt lgkmcnt(8)\n\t"
      "v_mfma_f32_16x16x32_bf16 v[24:27], v[176:179], v[128:131], v[24:27]\n\t"
      "ds_read_b64_tr_b16 v[196:197], v236 offset:18496\n\t"
      "ds_read_b64_tr_b16 v[198:199], v236 offset:20800\n\t"
      "v_mfma_f32_16x16x32_bf16 v[60:63], v[176:179], v[132:135], v[60:63]\n\t"
      "s_waitcnt lgkmcnt(8)\n\t"
      "v_mfma_f32_16x16x32_bf16 v[28:31], v[180:183], v[128:131], v[28:31]\n\t"
      "ds_read_b64_tr_b16 v[176:177], v236 offset:18528\n\t"
      "ds_read_b64_tr_b16 v[178:179], v236 offset:20832\n\t"
      "v_mfma_f32_16x16x32_bf16 v[64:67], v[180:183], v[132:135], v[64:67]\n\t"
      "s_add_u32 m0, s50, 38912\n\t"
      "s_add_u32 s49, s27, 737280\n\t"
      "s_mov_b64 exec, 0xffff\n\t"
      "buffer_load_dwordx4 v254, s[88:91], s49 offen lds\n\t"
      "s_mov_b64 exec, -1\n\t"
      "s_waitcnt lgkmcnt(8)\n\t"
      "v_mfma_f32_16x16x32_bf16 v[32:35], v[184:187], v[128:131], v[32:35]\n\t"
      "ds_read_b64_tr_b16 v[180:181], v236 offset:18560\n\t"
      "ds_read_b64_tr_b16 v[182:183], v236 offset:20864\n\t"
      "v_mfma_f32_16x16x32_bf16 v[68:71], v[184:187], v[132:135], v[68:71]\n\t"
      "s_add_u32 s48, s28, 40960\n\t"
      "buffer_load_dwordx4 v[128:131], v235, s[44:47], s48 offen offset:0 sc1\n\t"
      "s_add_u32 s48, s28, 40960\n\t"
      "buffer_load_dwordx4 v[132:135], v235, s[44:47], s48 offen offset:256 sc1\n\t"
      "s_waitcnt lgkmcnt(8)\n\t"
      "v_mfma_f32_16x16x32_bf16 v[0:3], v[188:191], v[136:139], v[0:3]\n\t"
      "ds_read_b64_tr_b16 v[184:185], v236 offset:18592\n\t"
      "ds_read_b64_tr_b16 v[186:187], v236 offset:20896\n\t"
      "v_mfma_f32_16x16x32_bf16 v[36:39], v[188:191], v[140:143], v[36:39]\n\t"
      "s_waitcnt lgkmcnt(8)\n\t"
      "v_mfma_f32_16x16x32_bf16 v[4:7], v[192:195], v[136:139], v[4:7]\n\t"
      "ds_read_b64_tr_b16 v[188:189], v236 offset:18624\n\t"
      "ds_read_b64_tr_b16 v[190:191], v236 offset:20928\n\t"
      "v_mfma_f32_16x16x32_bf16 v[40:43], v[192:195], v[140:143], v[40:43]\n\t"
      "s_add_u32 m0, s50, 55296\n\t"
      "s_add_u32 s49, s27, 811008\n\t"
      "buffer_load_dwordx4 v252, s[88:91], s49 offen lds\n\t"
      "s_waitcnt lgkmcnt(8)\n\t"
      "v_mfma_f32_16x16x32_bf16 v[8:11], v[196:199], v[136:139], v[8:11]\n\t"
      "ds_read_b64_tr_b16 v[192:193], v236 offset:18656\n\t"
      "ds_read_b64_tr_b16 v[194:195], v236 offset:20960\n\t"
      "v_mfma_f32_16x16x32_bf16 v[44:47], v[196:199], v[140:143], v[44:47]\n\t"
      "s_waitcnt lgkmcnt(8)\n\t"
      "v_mfma_f32_16x16x32_bf16 v[12:15], v[176:179], v[136:139], v[12:15]\n\t"
      "ds_read_b64_tr_b16 v[196:197], v236 offset:18688\n\t"
      "ds_read_b64_tr_b16 v[198:199], v236 offset:20992\n\t"
      "v_mfma_f32_16x16x32_bf16 v[48:51], v[176:179], v[140:143], v[48:51]\n\t"
      "s_waitcnt lgkmcnt(8)\n\t"
      "v_mfma_f32_16x16x32_bf16 v[16:19], v[180:183], v[136:139], v[16:19]\n\t"
      "ds_read_b64_tr_b16 v[176:177], v234 offset:0\n\t"
      "ds_read_b64_tr_b16 v[178:179], v234 offset:2304\n\t"
      "v_mfma_f32_16x16x32_bf16 v[52:55], v[180:183], v[140:143], v[52:55]\n\t"
      "s_add_u32 m0, s50, 56320\n\t"
      "s_add_u32 s49, s27, 811008\n\t"
      "buffer_load_dwordx4 v253, s[88:91], s49 offen lds\n\t"
      "s_waitcnt lgkmcnt(8)\n\t"
      "v_mfma_f32_16x16x32_bf16 v[20:23], v[184:187], v[136:139], v[20:23]\n\t"
      "ds_read_b64_tr_b16 v[180:181], v234 offset:32\n\t"
      "ds_read_b64_tr_b16 v[182:183], v234 offset:2336\n\t"
      "v_mfma_f32_16x16x32_bf16 v[56:59], v[184:187], v[140:143], v[56:59]\n\t"
      "s_waitcnt lgkmcnt(8)\n\t"
      "v_mfma_f32_16x16x32_bf16 v[24:27], v[188:191], v[136:139], v[24:27]\n\t"
      "ds_read_b64_tr_b16 v[184:185], v234 offset:64\n\t"
      "ds_read_b64_tr_b16 v[186:187], v234 offset:2368\n\t"
      "v_mfma_f32_16x16x32_bf16 v[60:63], v[188:191], v[140:143], v[60:63]\n\t"
      "s_waitcnt lgkmcnt(8)\n\t"
      "v_mfma_f32_16x16x32_bf16 v[28:31], v[192:195], v[136:139], v[28:31]\n\t"
      "ds_read_b64_tr_b16 v[188:189], v234 offset:96\n\t"
      "ds_read_b64_tr_b16 v[190:191], v234 offset:2400\n\t"
      "v_mfma_f32_16x16x32_bf16 v[64:67], v[192:195], v[140:143], v[64:67]\n\t"
      "s_add_u32 m0, s50, 57344\n\t"
      "s_add_u32 s49, s27, 811008\n\t"
      "s_mov_b64 exec, 0xffff\n\t"
      "buffer_load_dwordx4 v254, s[88:91], s49 offen lds\n\t"
      "s_mov_b64 exec, -1\n\t"
      "s_waitcnt lgkmcnt(8)\n\t"
      "v_mfma_f32_16x16x32_bf16 v[32:35], v[196:199], v[136:139], v[32:35]\n\t"
      "ds_read_b64_tr_b16 v[192:193], v234 offset:128\n\t"
      "ds_read_b64_tr_b16 v[194:195], v234 offset:2432\n\t"
      "v_mfma_f32_16x16x32_bf16 v[68:71], v[196:199], v[140:143], v[68:71]\n\t"
      "s_add_u32 s48, s28, 45056\n\t"
      "buffer_load_dwordx4 v[136:139], v235, s[44:47], s48 offen offset:0 sc1\n\t"
      "s_add_u32 s48, s28, 45056\n\t"
      "buffer_load_dwordx4 v[140:143], v235, s[44:47], s48 offen offset:256 sc1\n\t"
      "s_waitcnt vmcnt(15)\n\t"
      "s_barrier\n\t"
      "s_waitcnt lgkmcnt(8)\n\t"
      "v_mfma_f32_16x16x32_bf16 v[0:3], v[176:179], v[96:99], v[0:3]\n\t"
      "ds_read_b64_tr_b16 v[196:197], v234 offset:160\n\t"
      "ds_read_b64_tr_b16 v[198:199], v234 offset:2464\n\t"
      "v_mfma_f32_16x16x32_bf16 v[36:39], v[176:179], v[100:103], v[36:39]\n\t"
      "s_waitcnt lgkmcnt(8)\n\t"
      "v_mfma_f32_16x16x32_bf16 v[4:7], v[180:183], v[96:99], v[4:7]\n\t"
      "ds_read_b64_tr_b16 v[176:177], v234 offset:192\n\t"
      "ds_read_b64_tr_b16 v[178:179], v234 offset:2496\n\t"
      "v_mfma_f32_16x16x32_bf16 v[40:43], v[180:183], v[100:103], v[40:43]\n\t"
      "s_add_u32 m0, s51, 0\n\t"
      "s_add_u32 s49, s27, 884736\n\t"
      "buffer_load_dwordx4 v252, s[88:91], s49 offen lds\n\t"
      "s_waitcnt lgkmcnt(8)\n\t"
      "v_mfma_f32_16x16x32_bf16 v[8:11], v[184:187], v[96:99], v[8:11]\n\t"
      "ds_read_b64_tr_b16 v[180:181], v234 offset:224\n\t"
      "ds_read_b64_tr_b16 v[182:183], v234 offset:2528\n\t"
      "v_mfma_f32_16x16x32_bf16 v[44:47], v[184:187], v[100:103], v[44:47]\n\t"
      "s_waitcnt lgkmcnt(8)\n\t"
      "v_mfma_f32_16x16x32_bf16 v[12:15], v[188:191], v[96:99], v[12:15]\n\t"
      "ds_read_b64_tr_b16 v[184:185], v234 offset:256\n\t"
      "ds_read_b64_tr_b16 v[186:187], v234 offset:2560\n\t"
      "v_mfma_f32_16x16x32_bf16 v[48:51], v[188:191], v[100:103], v[48:51]\n\t"
      "s_waitcnt lgkmcnt(8)\n\t"
      "v_mfma_f32_16x16x32_bf16 v[16:19], v[192:195], v[96:99], v[16:19]\n\t"
      "ds_read_b64_tr_b16 v[188:189], v234 offset:18432\n\t"
      "ds_read_b64_tr_b16 v[190:191], v234 offset:20736\n\t"
      "v_mfma_f32_16x16x32_bf16 v[52:55], v[192:195], v[100:103], v[52:55]\n\t"
      "s_add_u32 m0, s51, 1024\n\t"
      "s_add_u32 s49, s27, 884736\n\t"
      "buffer_load_dwordx4 v253, s[88:91], s49 offen lds\n\t"
      "s_waitcnt lgkmcnt(8)\n\t"
      "v_mfma_f32_16x16x32_bf16 v[20:23], v[196:199], v[96:99], v[20:23]\n\t"
      "ds_read_b64_tr_b16 v[192:193], v234 offset:18464\n\t"
      "ds_read_b64_tr_b16 v[194:195], v234 offset:20768\n\t"
      "v_mfma_f32_16x16x32_bf16 v[56:59], v[196:199], v[100:103], v[56:59]\n\t"
      "s_waitcnt lgkmcnt(8)\n\t"
      "v_mfma_f32_16x16x32_bf16 v[24:27], v[176:179], v[96:99], v[24:27]\n\t"
      "ds_read_b64_tr_b16 v[196:197], v234 offset:18496\n\t"
      "ds_read_b64_tr_b16 v[198:199], v234 offset:20800\n\t"
      "v_mfma_f32_16x16x32_bf16 v[60:63], v[176:179], v[100:103], v[60:63]\n\t"
      "s_waitcnt lgkmcnt(8)\n\t"
      "v_mfma_f32_16x16x32_bf16 v[28:31], v[180:183], v[96:99], v[28:31]\n\t"
      "ds_read_b64_tr_b16 v[176:177], v234 offset:18528\n\t"
      "ds_read_b64_tr_b16 v[178:179], v234 offset:20832\n\t"
      "v_mfma_f32_16x16x32_bf16 v[64:67], v[180:183], v[100:103], v[64:67]\n\t"
      "s_add_u32 m0, s51, 2048\n\t"
      "s_add_u32 s49, s27, 884736\n\t"
      "s_mov_b64 exec, 0xffff\n\t"
      "buffer_load_dwordx4 v254, s[88:91], s49 offen lds\n\t"
      "s_mov_b64 exec, -1\n\t"
      "s_waitcnt lgkmcnt(8)\n\t"
      "v_mfma_f32_16x16x32_bf16 v[32:35], v[184:187], v[96:99], v[32:35]\n\t"
      "ds_read_b64_tr_b16 v[180:181], v234 offset:18560\n\t"
      "ds_read_b64_tr_b16 v[182:183], v234 offset:20864\n\t"
      "v_mfma_f32_16x16x32_bf16 v[68:71], v[184:187], v[100:103], v[68:71]\n\t"
      "s_add_u32 s48, s28, 49152\n\t"
      "buffer_load_dwordx4 v[96:99], v235, s[44:47], s48 offen offset:0 sc1\n\t"
      "s_add_u32 s48, s28, 49152\n\t"
      "buffer_load_dwordx4 v[100:103], v235, s[44:47], s48 offen offset:256 sc1\n\t"
      "s_waitcnt lgkmcnt(8)\n\t"
      "v_mfma_f32_16x16x32_bf16 v[0:3], v[188:191], v[104:107], v[0:3]\n\t"
      "ds_read_b64_tr_b16 v[184:185], v234 offset:18592\n\t"
      "ds_read_b64_tr_b16 v[186:187], v234 offset:20896\n\t"
      "v_mfma_f32_16x16x32_bf16 v[36:39], v[188:191], v[108:111], v[36:39]\n\t"
      "s_waitcnt lgkmcnt(8)\n\t"
      "v_mfma_f32_16x16x32_bf16 v[4:7], v[192:195], v[104:107], v[4:7]\n\t"
      "ds_read_b64_tr_b16 v[188:189], v234 offset:18624\n\t"
      "ds_read_b64_tr_b16 v[190:191], v234 offset:20928\n\t"
      "v_mfma_f32_16x16x32_bf16 v[40:43], v[192:195], v[108:111], v[40:43]\n\t"
      "s_add_u32 m0, s51, 18432\n\t"
      "s_add_u32 s49, s27, 958464\n\t"
      "buffer_load_dwordx4 v252, s[88:91], s49 offen lds\n\t"
      "s_waitcnt lgkmcnt(8)\n\t"
      "v_mfma_f32_16x16x32_bf16 v[8:11], v[196:199], v[104:107], v[8:11]\n\t"
      "ds_read_b64_tr_b16 v[192:193], v234 offset:18656\n\t"
      "ds_read_b64_tr_b16 v[194:195], v234 offset:20960\n\t"
      "v_mfma_f32_16x16x32_bf16 v[44:47], v[196:199], v[108:111], v[44:47]\n\t"
      "s_waitcnt lgkmcnt(8)\n\t"
      "v_mfma_f32_16x16x32_bf16 v[12:15], v[176:179], v[104:107], v[12:15]\n\t"
      "ds_read_b64_tr_b16 v[196:197], v234 offset:18688\n\t"
      "ds_read_b64_tr_b16 v[198:199], v234 offset:20992\n\t"
      "v_mfma_f32_16x16x32_bf16 v[48:51], v[176:179], v[108:111], v[48:51]\n\t"
      "s_waitcnt lgkmcnt(8)\n\t"
      "v_mfma_f32_16x16x32_bf16 v[16:19], v[180:183], v[104:107], v[16:19]\n\t"
      "ds_read_b64_tr_b16 v[176:177], v232 offset:0\n\t"
      "ds_read_b64_tr_b16 v[178:179], v232 offset:2304\n\t"
      "v_mfma_f32_16x16x32_bf16 v[52:55], v[180:183], v[108:111], v[52:55]\n\t"
      "s_add_u32 m0, s51, 19456\n\t"
      "s_add_u32 s49, s27, 958464\n\t"
      "buffer_load_dwordx4 v253, s[88:91], s49 offen lds\n\t"
      "s_waitcnt lgkmcnt(8)\n\t"
      "v_mfma_f32_16x16x32_bf16 v[20:23], v[184:187], v[104:107], v[20:23]\n\t"
      "ds_read_b64_tr_b16 v[180:181], v232 offset:32\n\t"
      "ds_read_b64_tr_b16 v[182:183], v232 offset:2336\n\t"
      "v_mfma_f32_16x16x32_bf16 v[56:59], v[184:187], v[108:111], v[56:59]\n\t"
      "s_waitcnt lgkmcnt(8)\n\t"
      "v_mfma_f32_16x16x32_bf16 v[24:27], v[188:191], v[104:107], v[24:27]\n\t"
      "ds_read_b64_tr_b16 v[184:185], v232 offset:64\n\t"
      "ds_read_b64_tr_b16 v[186:187], v232 offset:2368\n\t"
      "v_mfma_f32_16x16x32_bf16 v[60:63], v[188:191], v[108:111], v[60:63]\n\t"
      "s_waitcnt lgkmcnt(8)\n\t"
      "v_mfma_f32_16x16x32_bf16 v[28:31], v[192:195], v[104:107], v[28:31]\n\t"
      "ds_read_b64_tr_b16 v[188:189], v232 offset:96\n\t"
      "ds_read_b64_tr_b16 v[190:191], v232 offset:2400\n\t"
      "v_mfma_f32_16x16x32_bf16 v[64:67], v[192:195], v[108:111], v[64:67]\n\t"
      "s_add_u32 m0, s51, 20480\n\t"
      "s_add_u32 s49, s27, 958464\n\t"
      "s_mov_b64 exec, 0xffff\n\t"
      "buffer_load_dwordx4 v254, s[88:91], s49 offen lds\n\t"
      "s_mov_b64 exec, -1\n\t"
      "s_waitcnt lgkmcnt(8)\n\t"
      "v_mfma_f32_16x16x32_bf16 v[32:35], v[196:199], v[104:107], v[32:35]\n\t"
      "ds_read_b64_tr_b16 v[192:193], v232 offset:128\n\t"
      "ds_read_b64_tr_b16 v[194:195], v232 offset:2432\n\t"
      "v_mfma_f32_16x16x32_bf16 v[68:71], v[196:199], v[108:111], v[68:71]\n\t"
      "s_add_u32 s48, s28, 53248\n\t"
      "buffer_load_dwordx4 v[104:107], v235, s[44:47], s48 offen offset:0 sc1\n\t"
      "s_add_u32 s48, s28, 53248\n\t"
      "buffer_load_dwordx4 v[108:111], v235, s[44:47], s48 offen offset:256 sc1\n\t"
      "s_waitcnt vmcnt(17)\n\t"
      "s_barrier\n\t"
      "s_waitcnt lgkmcnt(8)\n\t"
      "v_mfma_f32_16x16x32_bf16 v[0:3], v[176:179], v[112:115], v[0:3]\n\t"
      "ds_read_b64_tr_b16 v[196:197], v232 offset:160\n\t"
      "ds_read_b64_tr_b16 v[198:199], v232 offset:2464\n\t"
      "v_mfma_f32_16x16x32_bf16 v[36:39], v[176:179], v[116:119], v[36:39]\n\t"
      "s_waitcnt lgkmcnt(8)\n\t"
      "v_mfma_f32_16x16x32_bf16 v[4:7], v[180:183], v[112:115], v[4:7]\n\t"
      "ds_read_b64_tr_b16 v[176:177], v232 offset:192\n\t"
      "ds_read_b64_tr_b16 v[178:179], v232 offset:2496\n\t"
      "v_mfma_f32_16x16x32_bf16 v[40:43], v[180:183], v[116:119], v[40:43]\n\t"
      "s_add_u32 m0, s51, 36864\n\t"
      "s_add_u32 s49, s27, 1032192\n\t"
      "buffer_load_dwordx4 v252, s[88:91], s49 offen lds\n\t"
      "s_waitcnt lgkmcnt(8)\n\t"
      "v_mfma_f32_16x16x32_bf16 v[8:11], v[184:187], v[112:115], v[8:11]\n\t"
      "ds_read_b64_tr_b16 v[180:181], v232 offset:224\n\t"
      "ds_read_b64_tr_b16 v[182:183], v232 offset:2528\n\t"
      "v_mfma_f32_16x16x32_bf16 v[44:47], v[184:187], v[116:119], v[44:47]\n\t"
      "s_waitcnt lgkmcnt(8)\n\t"
      "v_mfma_f32_16x16x32_bf16 v[12:15], v[188:191], v[112:115], v[12:15]\n\t"
      "ds_read_b64_tr_b16 v[184:185], v232 offset:256\n\t"
      "ds_read_b64_tr_b16 v[186:187], v232 offset:2560\n\t"
      "v_mfma_f32_16x16x32_bf16 v[48:51], v[188:191], v[116:119], v[48:51]\n\t"
      "s_waitcnt lgkmcnt(8)\n\t"
      "v_mfma_f32_16x16x32_bf16 v[16:19], v[192:195], v[112:115], v[16:19]\n\t"
      "ds_read_b64_tr_b16 v[188:189], v232 offset:18432\n\t"
      "ds_read_b64_tr_b16 v[190:191], v232 offset:20736\n\t"
      "v_mfma_f32_16x16x32_bf16 v[52:55], v[192:195], v[116:119], v[52:55]\n\t"
      "s_add_u32 m0, s51, 37888\n\t"
      "s_add_u32 s49, s27, 1032192\n\t"
      "buffer_load_dwordx4 v253, s[88:91], s49 offen lds\n\t"
      "s_waitcnt lgkmcnt(8)\n\t"
      "v_mfma_f32_16x16x32_bf16 v[20:23], v[196:199], v[112:115], v[20:23]\n\t"
      "ds_read_b64_tr_b16 v[192:193], v232 offset:18464\n\t"
      "ds_read_b64_tr_b16 v[194:195], v232 offset:20768\n\t"
      "v_mfma_f32_16x16x32_bf16 v[56:59], v[196:199], v[116:119], v[56:59]\n\t"
      "s_waitcnt lgkmcnt(8)\n\t"
      "v_mfma_f32_16x16x32_bf16 v[24:27], v[176:179], v[112:115], v[24:27]\n\t"
      "ds_read_b64_tr_b16 v[196:197], v232 offset:18496\n\t"
      "ds_read_b64_tr_b16 v[198:199], v232 offset:20800\n\t"
      "v_mfma_f32_16x16x32_bf16 v[60:63], v[176:179], v[116:119], v[60:63]\n\t"
      "s_waitcnt lgkmcnt(8)\n\t"
      "v_mfma_f32_16x16x32_bf16 v[28:31], v[180:183], v[112:115], v[28:31]\n\t"
      "ds_read_b64_tr_b16 v[176:177], v232 offset:18528\n\t"
      "ds_read_b64_tr_b16 v[178:179], v232 offset:20832\n\t"
      "v_mfma_f32_16x16x32_bf16 v[64:67], v[180:183], v[116:119], v[64:67]\n\t"
      "s_add_u32 m0, s51, 38912\n\t"
      "s_add_u32 s49, s27, 1032192\n\t"
      "s_mov_b64 exec, 0xffff\n\t"
      "buffer_load_dwordx4 v254, s[88:91], s49 offen lds\n\t"
      "s_mov_b64 exec, -1\n\t"
      "s_waitcnt lgkmcnt(8)\n\t"
      "v_mfma_f32_16x16x32_bf16 v[32:35], v[184:187], v[112:115], v[32:35]\n\t"
      "ds_read_b64_tr_b16 v[180:181], v232 offset:18560\n\t"
      "ds_read_b64_tr_b16 v[182:183], v232 offset:20864\n\t"
      "v_mfma_f32_16x16x32_bf16 v[68:71], v[184:187], v[116:119], v[68:71]\n\t"
      "s_add_u32 s48, s28, 57344\n\t"
      "buffer_load_dwordx4 v[112:115], v235, s[44:47], s48 offen offset:0 sc1\n\t"
      "s_add_u32 s48, s28, 57344\n\t"
      "buffer_load_dwordx4 v[116:119], v235, s[44:47], s48 offen offset:256 sc1\n\t"
      "s_waitcnt lgkmcnt(8)\n\t"
      "v_mfma_f32_16x16x32_bf16 v[0:3], v[188:191], v[120:123], v[0:3]\n\t"
      "ds_read_b64_tr_b16 v[184:185], v232 offset:18592\n\t"
      "ds_read_b64_tr_b16 v[186:187], v232 offset:20896\n\t"
      "v_mfma_f32_16x16x32_bf16 v[36:39], v[188:191], v[124:127], v[36:39]\n\t"
      "s_waitcnt lgkmcnt(8)\n\t"
      "v_mfma_f32_16x16x32_bf16 v[4:7], v[192:195], v[120:123], v[4:7]\n\t"
      "ds_read_b64_tr_b16 v[188:189], v232 offset:18624\n\t"
      "ds_read_b64_tr_b16 v[190:191], v232 offset:20928\n\t"
      "v_mfma_f32_16x16x32_bf16 v[40:43], v[192:195], v[124:127], v[40:43]\n\t"
      "s_add_u32 m0, s51, 55296\n\t"
      "s_add_u32 s49, s27, 1105920\n\t"
      "buffer_load_dwordx4 v252, s[88:91], s49 offen lds\n\t"
      "s_waitcnt lgkmcnt(8)\n\t"
      "v_mfma_f32_16x16x32_bf16 v[8:11], v[196:199], v[120:123], v[8:11]\n\t"
      "ds_read_b64_tr_b16 v[192:193], v232 offset:18656\n\t"
      "ds_read_b64_tr_b16 v[194:195], v232 offset:20960\n\t"
      "v_mfma_f32_16x16x32_bf16 v[44:47], v[196:199], v[124:127], v[44:47]\n\t"
      "s_waitcnt lgkmcnt(8)\n\t"
      "v_mfma_f32_16x16x32_bf16 v[12:15], v[176:179], v[120:123], v[12:15]\n\t"
      "ds_read_b64_tr_b16 v[196:197], v232 offset:18688\n\t"
      "ds_read_b64_tr_b16 v[198:199], v232 offset:20992\n\t"
      "v_mfma_f32_16x16x32_bf16 v[48:51], v[176:179], v[124:127], v[48:51]\n\t"
      "s_waitcnt lgkmcnt(8)\n\t"
      "v_mfma_f32_16x16x32_bf16 v[16:19], v[180:183], v[120:123], v[16:19]\n\t"
      "ds_read_b64_tr_b16 v[176:177], v233 offset:0\n\t"
      "ds_read_b64_tr_b16 v[178:179], v233 offset:2304\n\t"
      "v_mfma_f32_16x16x32_bf16 v[52:55], v[180:183], v[124:127], v[52:55]\n\t"
      "s_add_u32 m0, s51, 56320\n\t"
      "s_add_u32 s49, s27, 1105920\n\t"
      "buffer_load_dwordx4 v253, s[88:91], s49 offen lds\n\t"
      "s_waitcnt lgkmcnt(8)\n\t"
      "v_mfma_f32_16x16x32_bf16 v[20:23], v[184:187], v[120:123], v[20:23]\n\t"
      "ds_read_b64_tr_b16 v[180:181], v233 offset:32\n\t"
      "ds_read_b64_tr_b16 v[182:183], v233 offset:2336\n\t"
      "v_mfma_f32_16x16x32_bf16 v[56:59], v[184:187], v[124:127], v[56:59]\n\t"
      "s_waitcnt lgkmcnt(8)\n\t"
      "v_mfma_f32_16x16x32_bf16 v[24:27], v[188:191], v[120:123], v[24:27]\n\t"
      "ds_read_b64_tr_b16 v[184:185], v233 offset:64\n\t"
      "ds_read_b64_tr_b16 v[186:187], v233 offset:2368\n\t"
      "v_mfma_f32_16x16x32_bf16 v[60:63], v[188:191], v[124:127], v[60:63]\n\t"
      "s_waitcnt lgkmcnt(8)\n\t"
      "v_mfma_f32_16x16x32_bf16 v[28:31], v[192:195], v[120:123], v[28:31]\n\t"
      "ds_read_b64_tr_b16 v[188:189], v233 offset:96\n\t"
      "ds_read_b64_tr_b16 v[190:191], v233 offset:2400\n\t"
      "v_mfma_f32_16x16x32_bf16 v[64:67], v[192:195], v[124:127], v[64:67]\n\t"
      "s_add_u32 m0, s51, 57344\n\t"
      "s_add_u32 s49, s27, 1105920\n\t"
      "s_mov_b64 exec, 0xffff\n\t"
      "buffer_load_dwordx4 v254, s[88:91], s49 offen lds\n\t"
      "s_mov_b64 exec, -1\n\t"
      "s_waitcnt lgkmcnt(8)\n\t"
      "v_mfma_f32_16x16x32_bf16 v[32:35], v[196:199], v[120:123], v[32:35]\n\t"
      "ds_read_b64_tr_b16 v[192:193], v233 offset:128\n\t"
      "ds_read_b64_tr_b16 v[194:195], v233 offset:2432\n\t"
      "v_mfma_f32_16x16x32_bf16 v[68:71], v[196:199], v[124:127], v[68:71]\n\t"
      "s_and_b32 s60, s31, 0xffffffc0\n\t"
      "v_mov_b32 v239, v238\n\t"
      "s_mov_b32 exec_lo, 0xff\n\t"
      "s_mov_b32 exec_hi, 0\n\t"
      "buffer_load_dword v239, v246, s[44:47], s60 offen sc1\n\t"
      "s_mov_b64 exec, -1\n\t"
      "s_add_u32 s48, s28, 61440\n\t"
      "buffer_load_dwordx4 v[120:123], v235, s[44:47], s48 offen offset:0 sc1\n\t"
      "s_add_u32 s48, s28, 61440\n\t"
      "buffer_load_dwordx4 v[124:127], v235, s[44:47], s48 offen offset:256 sc1\n\t"
      "s_waitcnt vmcnt(18)\n\t"
      "s_barrier\n\t"
      "s_waitcnt lgkmcnt(8)\n\t"
      "v_mfma_f32_16x16x32_bf16 v[0:3], v[176:179], v[128:131], v[0:3]\n\t"
      "ds_read_b64_tr_b16 v[196:197], v233 offset:160\n\t"
      "ds_read_b64_tr_b16 v[198:199], v233 offset:2464\n\t"
      "v_mfma_f32_16x16x32_bf16 v[36:39], v[176:179], v[132:135], v[36:39]\n\t"
      "s_waitcnt lgkmcnt(8)\n\t"
      "v_mfma_f32_16x16x32_bf16 v[4:7], v[180:183], v[128:131], v[4:7]\n\t"
      "ds_read_b64_tr_b16 v[176:177], v233 offset:192\n\t"
      "ds_read_b64_tr_b16 v[178:179], v233 offset:2496\n\t"
      "v_mfma_f32_16x16x32_bf16 v[40:43], v[180:183], v[132:135], v[40:43]\n\t"
      "s_add_u32 m0, s50, 0\n\t"
      "s_add_u32 s49, s27, 1179648\n\t"
      "buffer_load_dwordx4 v252, s[88:91], s49 offen lds\n\t"
      "s_waitcnt lgkmcnt(8)\n\t"
      "v_mfma_f32_16x16x32_bf16 v[8:11], v[184:187], v[128:131], v[8:11]\n\t"
      "ds_read_b64_tr_b16 v[180:181], v233 offset:224\n\t"
      "ds_read_b64_tr_b16 v[182:183], v233 offset:2528\n\t"
      "v_mfma_f32_16x16x32_bf16 v[44:47], v[184:187], v[132:135], v[44:47]\n\t"
      "s_waitcnt lgkmcnt(8)\n\t"
      "v_mfma_f32_16x16x32_bf16 v[12:15], v[188:191], v[128:131], v[12:15]\n\t"
      "ds_read_b64_tr_b16 v[184:185], v233 offset:256\n\t"
      "ds_read_b64_tr_b16 v[186:187], v233 offset:2560\n\t"
      "v_mfma_f32_16x16x32_bf16 v[48:51], v[188:191], v[132:135], v[48:51]\n\t"
      "s_waitcnt lgkmcnt(8)\n\t"
      "v_mfma_f32_16x16x32_bf16 v[16:19], v[192:195], v[128:131], v[16:19]\n\t"
      "ds_read_b64_tr_b16 v[188:189], v233 offset:18432\n\t"
      "ds_read_b64_tr_b16 v[190:191], v233 offset:20736\n\t"
      "v_mfma_f32_16x16x32_bf16 v[52:55], v[192:195], v[132:135], v[52:55]\n\t"
      "s_add_u32 m0, s50, 1024\n\t"
      "s_add_u32 s49, s27, 1179648\n\t"
      "buffer_load_dwordx4 v253, s[88:91], s49 offen lds\n\t"
      "s_waitcnt lgkmcnt(8)\n\t"
      "v_mfma_f32_16x16x32_bf16 v[20:23], v[196:199], v[128:131], v[20:23]\n\t"
      "ds_read_b64_tr_b16 v[192:193], v233 offset:18464\n\t"
      "ds_read_b64_tr_b16 v[194:195], v233 offset:20768\n\t"
      "v_mfma_f32_16x16x32_bf16 v[56:59], v[196:199], v[132:135], v[56:59]\n\t"
      "s_waitcnt lgkmcnt(8)\n\t"
      "v_mfma_f32_16x16x32_bf16 v[24:27], v[176:179], v[128:131], v[24:27]\n\t"
      "ds_read_b64_tr_b16 v[196:197], v233 offset:18496\n\t"
      "ds_read_b64_tr_b16 v[198:199], v233 offset:20800\n\t"
      "v_mfma_f32_16x16x32_bf16 v[60:63], v[176:179], v[132:135], v[60:63]\n\t"
      "s_waitcnt lgkmcnt(8)\n\t"
      "v_mfma_f32_16x16x32_bf16 v[28:31], v[180:183], v[128:131], v[28:31]\n\t"
      "ds_read_b64_tr_b16 v[176:177], v233 offset:18528\n\t"
      "ds_read_b64_tr_b16 v[178:179], v233 offset:20832\n\t"
      "v_mfma_f32_16x16x32_bf16 v[64:67], v[180:183], v[132:135], v[64:67]\n\t"
      "s_add_u32 m0, s50, 2048\n\t"
      "s_add_u32 s49, s27, 1179648\n\t"
      "s_mov_b64 exec, 0xffff\n\t"
      "buffer_load_dwordx4 v254, s[88:91], s49 offen lds\n\t"
      "s_mov_b64 exec, -1\n\t"
      "s_waitcnt lgkmcnt(8)\n\t"
      "v_mfma_f32_16x16x32_bf16 v[32:35], v[184:187], v[128:131], v[32:35]\n\t"
      "ds_read_b64_tr_b16 v[180:181], v233 offset:18560\n\t"
      "ds_read_b64_tr_b16 v[182:183], v233 offset:20864\n\t"
      "v_mfma_f32_16x16x32_bf16 v[68:71], v[184:187], v[132:135], v[68:71]\n\t"
      "s_add_u32 s48, s28, 65536\n\t"
      "buffer_load_dwordx4 v[128:131], v235, s[44:47], s48 offen offset:0 sc1\n\t"
      "s_add_u32 s48, s28, 65536\n\t"
      "buffer_load_dwordx4 v[132:135], v235, s[44:47], s48 offen offset:256 sc1\n\t"
      "s_waitcnt lgkmcnt(8)\n\t"
      "v_mfma_f32_16x16x32_bf16 v[0:3], v[188:191], v[136:139], v[0:3]\n\t"
      "ds_read_b64_tr_b16 v[184:185], v233 offset:18592\n\t"
      "ds_read_b64_tr_b16 v[186:187], v233 offset:20896\n\t"
      "v_mfma_f32_16x16x32_bf16 v[36:39], v[188:191], v[140:143], v[36:39]\n\t"
      "s_waitcnt lgkmcnt(8)\n\t"
      "v_mfma_f32_16x16x32_bf16 v[4:7], v[192:195], v[136:139], v[4:7]\n\t"
      "ds_read_b64_tr_b16 v[188:189], v233 offset:18624\n\t"
      "ds_read_b64_tr_b16 v[190:191], v233 offset:20928\n\t"
      "v_mfma_f32_16x16x32_bf16 v[40:43], v[192:195], v[140:143], v[40:43]\n\t"
      "s_add_u32 m0, s50, 18432\n\t"
      "s_add_u32 s49, s27, 1253376\n\t"
      "buffer_load_dwordx4 v252, s[88:91], s49 offen lds\n\t"
      "s_waitcnt lgkmcnt(8)\n\t"
      "v_mfma_f32_16x16x32_bf16 v[8:11], v[196:199], v[136:139], v[8:11]\n\t"
      "ds_read_b64_tr_b16 v[192:193], v233 offset:18656\n\t"
      "ds_read_b64_tr_b16 v[194:195], v233 offset:20960\n\t"
      "v_mfma_f32_16x16x32_bf16 v[44:47], v[196:199], v[140:143], v[44:47]\n\t"
      "s_waitcnt lgkmcnt(8)\n\t"
      "v_mfma_f32_16x16x32_bf16 v[12:15], v[176:179], v[136:139], v[12:15]\n\t"
      "ds_read_b64_tr_b16 v[196:197], v233 offset:18688\n\t"
      "ds_read_b64_tr_b16 v[198:199], v233 offset:20992\n\t"
      "v_mfma_f32_16x16x32_bf16 v[48:51], v[176:179], v[140:143], v[48:51]\n\t"
      "s_waitcnt lgkmcnt(8)\n\t"
      "v_mfma_f32_16x16x32_bf16 v[16:19], v[180:183], v[136:139], v[16:19]\n\t"
      "ds_read_b64_tr_b16 v[176:177], v236 offset:0\n\t"
      "ds_read_b64_tr_b16 v[178:179], v236 offset:2304\n\t"
      "v_mfma_f32_16x16x32_bf16 v[52:55], v[180:183], v[140:143], v[52:55]\n\t"
      "s_add_u32 m0, s50, 19456\n\t"
      "s_add_u32 s49, s27, 1253376\n\t"
      "buffer_load_dwordx4 v253, s[88:91], s49 offen lds\n\t"
      "s_waitcnt lgkmcnt(8)\n\t"
      "v_mfma_f32_16x16x32_bf16 v[20:23], v[184:187], v[136:139], v[20:23]\n\t"
      "ds_read_b64_tr_b16 v[180:181], v236 offset:32\n\t"
      "ds_read_b64_tr_b16 v[182:183], v236 offset:2336\n\t"
      "v_mfma_f32_16x16x32_bf16 v[56:59], v[184:187], v[140:143], v[56:59]\n\t"
      "s_waitcnt lgkmcnt(8)\n\t"
      "v_mfma_f32_16x16x32_bf16 v[24:27], v[188:191], v[136:139], v[24:27]\n\t"
      "ds_read_b64_tr_b16 v[184:185], v236 offset:64\n\t"
      "ds_read_b64_tr_b16 v[186:187], v236 offset:2368\n\t"
      "v_mfma_f32_16x16x32_bf16 v[60:63], v[188:191], v[140:143], v[60:63]\n\t"
      "s_waitcnt lgkmcnt(8)\n\t"
      "v_mfma_f32_16x16x32_bf16 v[28:31], v[192:195], v[136:139], v[28:31]\n\t"
      "ds_read_b64_tr_b16 v[188:189], v236 offset:96\n\t"
      "ds_read_b64_tr_b16 v[190:191], v236 offset:2400\n\t"
      "v_mfma_f32_16x16x32_bf16 v[64:67], v[192:195], v[140:143], v[64:67]\n\t"
      "s_add_u32 m0, s50, 20480\n\t"
      "s_add_u32 s49, s27, 1253376\n\t"
      "s_mov_b64 exec, 0xffff\n\t"
      "buffer_load_dwordx4 v254, s[88:91], s49 offen lds\n\t"
      "s_mov_b64 exec, -1\n\t"
      "s_waitcnt lgkmcnt(8)\n\t"
      "v_mfma_f32_16x16x32_bf16 v[32:35], v[196:199], v[136:139], v[32:35]\n\t"
      "ds_read_b64_tr_b16 v[192:193], v236 offset:128\n\t"
      "ds_read_b64_tr_b16 v[194:195], v236 offset:2432\n\t"
      "v_mfma_f32_16x16x32_bf16 v[68:71], v[196:199], v[140:143], v[68:71]\n\t"
      "s_add_u32 s48, s28, 69632\n\t"
      "buffer_load_dwordx4 v[136:139], v235, s[44:47], s48 offen offset:0 sc1\n\t"
      "s_add_u32 s48, s28, 69632\n\t"
      "buffer_load_dwordx4 v[140:143], v235, s[44:47], s48 offen offset:256 sc1\n\t"
      "s_waitcnt vmcnt(18)\n\t"
      "s_barrier\n\t"
      "s_waitcnt lgkmcnt(8)\n\t"
      "v_mfma_f32_16x16x32_bf16 v[0:3], v[176:179], v[96:99], v[0:3]\n\t"
      "ds_read_b64_tr_b16 v[196:197], v236 offset:160\n\t"
      "ds_read_b64_tr_b16 v[198:199], v236 offset:2464\n\t"
      "v_mfma_f32_16x16x32_bf16 v[36:39], v[176:179], v[100:103], v[36:39]\n\t"
      "s_waitcnt lgkmcnt(8)\n\t"
      "v_mfma_f32_16x16x32_bf16 v[4:7], v[180:183], v[96:99], v[4:7]\n\t"
      "ds_read_b64_tr_b16 v[176:177], v236 offset:192\n\t"
      "ds_read_b64_tr_b16 v[178:179], v236 offset:2496\n\t"
      "v_mfma_f32_16x16x32_bf16 v[40:43], v[180:183], v[100:103], v[40:43]\n\t"
      "s_add_u32 m0, s50, 36864\n\t"
      "s_add_u32 s49, s21, 0\n\t"
      "buffer_load_dwordx4 v252, s[92:95], s49 offen lds\n\t"
      "s_waitcnt lgkmcnt(8)\n\t"
      "v_mfma_f32_16x16x32_bf16 v[8:11], v[184:187], v[96:99], v[8:11]\n\t"
      "ds_read_b64_tr_b16 v[180:181], v236 offset:224\n\t"
      "ds_read_b64_tr_b16 v[182:183], v236 offset:2528\n\t"
      "v_mfma_f32_16x16x32_bf16 v[44:47], v[184:187], v[100:103], v[44:47]\n\t"
      "s_waitcnt lgkmcnt(8)\n\t"
      "v_mfma_f32_16x16x32_bf16 v[12:15], v[188:191], v[96:99], v[12:15]\n\t"
      "ds_read_b64_tr_b16 v[184:185], v236 offset:256\n\t"
      "ds_read_b64_tr_b16 v[186:187], v236 offset:2560\n\t"
      "v_mfma_f32_16x16x32_bf16 v[48:51], v[188:191], v[100:103], v[48:51]\n\t"
      "s_waitcnt lgkmcnt(8)\n\t"
      "v_mfma_f32_16x16x32_bf16 v[16:19], v[192:195], v[96:99], v[16:19]\n\t"
      "ds_read_b64_tr_b16 v[188:189], v236 offset:18432\n\t"
      "ds_read_b64_tr_b16 v[190:191], v236 offset:20736\n\t"
      "v_mfma_f32_16x16x32_bf16 v[52:55], v[192:195], v[100:103], v[52:55]\n\t"
      "s_add_u32 m0, s50, 37888\n\t"
      "s_add_u32 s49, s21, 0\n\t"
      "buffer_load_dwordx4 v253, s[92:95], s49 offen lds\n\t"
      "s_waitcnt lgkmcnt(8)\n\t"
      "v_mfma_f32_16x16x32_bf16 v[20:23], v[196:199], v[96:99], v[20:23]\n\t"
      "ds_read_b64_tr_b16 v[192:193], v236 offset:18464\n\t"
      "ds_read_b64_tr_b16 v[194:195], v236 offset:20768\n\t"
      "v_mfma_f32_16x16x32_bf16 v[56:59], v[196:199], v[100:103], v[56:59]\n\t"
      "s_waitcnt lgkmcnt(8)\n\t"
      "v_mfma_f32_16x16x32_bf16 v[24:27], v[176:179], v[96:99], v[24:27]\n\t"
      "ds_read_b64_tr_b16 v[196:197], v236 offset:18496\n\t"
      "ds_read_b64_tr_b16 v[198:199], v236 offset:20800\n\t"
      "v_mfma_f32_16x16x32_bf16 v[60:63], v[176:179], v[100:103], v[60:63]\n\t"
      "s_waitcnt lgkmcnt(8)\n\t"
      "v_mfma_f32_16x16x32_bf16 v[28:31], v[180:183], v[96:99], v[28:31]\n\t"
      "ds_read_b64_tr_b16 v[176:177], v236 offset:18528\n\t"
      "ds_read_b64_tr_b16 v[178:179], v236 offset:20832\n\t"
      "v_mfma_f32_16x16x32_bf16 v[64:67], v[180:183], v[100:103], v[64:67]\n\t"
      "s_add_u32 m0, s50, 38912\n\t"
      "s_add_u32 s49, s21, 0\n\t"
      "s_mov_b64 exec, 0xffff\n\t"
      "buffer_load_dwordx4 v254, s[92:95], s49 offen lds\n\t"
      "s_mov_b64 exec, -1\n\t"
      "s_waitcnt lgkmcnt(8)\n\t"
      "v_mfma_f32_16x16x32_bf16 v[32:35], v[184:187], v[96:99], v[32:35]\n\t"
      "ds_read_b64_tr_b16 v[180:181], v236 offset:18560\n\t"
      "ds_read_b64_tr_b16 v[182:183], v236 offset:20864\n\t"
      "v_mfma_f32_16x16x32_bf16 v[68:71], v[184:187], v[100:103], v[68:71]\n\t"
      "s_waitcnt vmcnt(15)\n\t"
      "v_cmp_ne_u32 vcc, v239, v238\n\t"
      "s_nop 1\n\t"
      "s_cmp_lg_u64 vcc, 0\n\t"
      "s_cbranch_scc1 Lsp3_%=\n\t"
      "Lsg3_%=:\n\t"
      "s_add_u32 s48, s24, 0\n\t"
      "buffer_load_dwordx4 v[96:99], v235, s[44:47], s48 offen offset:0 sc1\n\t"
      "s_add_u32 s48, s24, 0\n\t"
      "buffer_load_dwordx4 v[100:103], v235, s[44:47], s48 offen offset:256 sc1\n\t"
      "s_waitcnt lgkmcnt(8)\n\t"
      "v_mfma_f32_16x16x32_bf16 v[0:3], v[188:191], v[104:107], v[0:3]\n\t"
      "ds_read_b64_tr_b16 v[184:185], v236 offset:18592\n\t"
      "ds_read_b64_tr_b16 v[186:187], v236 offset:20896\n\t"
      "v_mfma_f32_16x16x32_bf16 v[36:39], v[188:191], v[108:111], v[36:39]\n\t"
      "s_waitcnt lgkmcnt(8)\n\t"
      "v_mfma_f32_16x16x32_bf16 v[4:7], v[192:195], v[104:107], v[4:7]\n\t"
      "ds_read_b64_tr_b16 v[188:189], v236 offset:18624\n\t"
      "ds_read_b64_tr_b16 v[190:191], v236 offset:20928\n\t"
      "v_mfma_f32_16x16x32_bf16 v[40:43], v[192:195], v[108:111], v[40:43]\n\t"
      "s_add_u32 m0, s50, 55296\n\t"
      "s_add_u32 s49, s21, 82944\n\t"
      "buffer_load_dwordx4 v252, s[92:95], s49 offen lds\n\t"
      "s_waitcnt lgkmcnt(8)\n\t"
      "v_mfma_f32_16x16x32_bf16 v[8:11], v[196:199], v[104:107], v[8:11]\n\t"
      "ds_read_b64_tr_b16 v[192:193], v236 offset:18656\n\t"
      "ds_read_b64_tr_b16 v[194:195], v236 offset:20960\n\t"
      "v_mfma_f32_16x16x32_bf16 v[44:47], v[196:199], v[108:111], v[44:47]\n\t"
      "s_waitcnt lgkmcnt(8)\n\t"
      "v_mfma_f32_16x16x32_bf16 v[12:15], v[176:179], v[104:107], v[12:15]\n\t"
      "ds_read_b64_tr_b16 v[196:197], v236 offset:18688\n\t"
      "ds_read_b64_tr_b16 v[198:199], v236 offset:20992\n\t"
      "v_mfma_f32_16x16x32_bf16 v[48:51], v[176:179], v[108:111], v[48:51]\n\t"
      "s_waitcnt lgkmcnt(8)\n\t"
      "v_mfma_f32_16x16x32_bf16 v[16:19], v[180:183], v[104:107], v[16:19]\n\t"
      "ds_read_b64_tr_b16 v[176:177], v234 offset:0\n\t"
      "ds_read_b64_tr_b16 v[178:179], v234 offset:2304\n\t"
      "v_mfma_f32_16x16x32_bf16 v[52:55], v[180:183], v[108:111], v[52:55]\n\t"
      "s_add_u32 m0, s50, 56320\n\t"
      "s_add_u32 s49, s21, 82944\n\t"
      "buffer_load_dwordx4 v253, s[92:95], s49 offen lds\n\t"
      "s_waitcnt lgkmcnt(8)\n\t"
      "v_mfma_f32_16x16x32_bf16 v[20:23], v[184:187], v[104:107], v[20:23]\n\t"
      "ds_read_b64_tr_b16 v[180:181], v234 offset:32\n\t"
      "ds_read_b64_tr_b16 v[182:183], v234 offset:2336\n\t"
      "v_mfma_f32_16x16x32_bf16 v[56:59], v[184:187], v[108:111], v[56:59]\n\t"
      "s_waitcnt lgkmcnt(8)\n\t"
      "v_mfma_f32_16x16x32_bf16 v[24:27], v[188:191], v[104:107], v[24:27]\n\t"
      "ds_read_b64_tr_b16 v[184:185], v234 offset:64\n\t"
      "ds_read_b64_tr_b16 v[186:187], v234 offset:2368\n\t"
      "v_mfma_f32_16x16x32_bf16 v[60:63], v[188:191], v[108:111], v[60:63]\n\t"
      "s_waitcnt lgkmcnt(8)\n\t"
      "v_mfma_f32_16x16x32_bf16 v[28:31], v[192:195], v[104:107], v[28:31]\n\t"
      "ds_read_b64_tr_b16 v[188:189], v234 offset:96\n\t"
      "ds_read_b64_tr_b16 v[190:191], v234 offset:2400\n\t"
      "v_mfma_f32_16x16x32_bf16 v[64:67], v[192:195], v[108:111], v[64:67]\n\t"
      "s_add_u32 m0, s50, 57344\n\t"
      "s_add_u32 s49, s21, 82944\n\t"
      "s_mov_b64 exec, 0xffff\n\t"
      "buffer_load_dwordx4 v254, s[92:95], s49 offen lds\n\t"
      "s_mov_b64 exec, -1\n\t"
      "s_waitcnt lgkmcnt(8)\n\t"
      "v_mfma_f32_16x16x32_bf16 v[32:35], v[196:199], v[104:107], v[32:35]\n\t"
      "ds_read_b64_tr_b16 v[192:193], v234 offset:128\n\t"
      "ds_read_b64_tr_b16 v[194:195], v234 offset:2432\n\t"
      "v_mfma_f32_16x16x32_bf16 v[68:71], v[196:199], v[108:111], v[68:71]\n\t"
      "s_add_u32 s48, s24, 4096\n\t"
      "buffer_load_dwordx4 v[104:107], v235, s[44:47], s48 offen offset:0 sc1\n\t"
      "s_add_u32 s48, s24, 4096\n\t"
      "buffer_load_dwordx4 v[108:111], v235, s[44:47], s48 offen offset:256 sc1\n\t"
      "s_waitcnt vmcnt(17)\n\t"
      "s_barrier\n\t"
      "s_waitcnt lgkmcnt(8)\n\t"
      "v_mfma_f32_16x16x32_bf16 v[0:3], v[176:179], v[112:115], v[0:3]\n\t"
      "ds_read_b64_tr_b16 v[196:197], v234 offset:160\n\t"
      "ds_read_b64_tr_b16 v[198:199], v234 offset:2464\n\t"
      "v_mfma_f32_16x16x32_bf16 v[36:39], v[176:179], v[116:119], v[36:39]\n\t"
      "s_waitcnt lgkmcnt(8)\n\t"
      "v_mfma_f32_16x16x32_bf16 v[4:7], v[180:183], v[112:115], v[4:7]\n\t"
      "ds_read_b64_tr_b16 v[176:177], v234 offset:192\n\t"
      "ds_read_b64_tr_b16 v[178:179], v234 offset:2496\n\t"
      "v_mfma_f32_16x16x32_bf16 v[40:43], v[180:183], v[116:119], v[40:43]\n\t"
      "s_add_u32 m0, s51, 0\n\t"
      "s_mov_b32 s49, s22\n\t"
      "buffer_load_dwordx4 v252, s[92:95], s49 offen lds\n\t"
      "s_waitcnt lgkmcnt(8)\n\t"
      "v_mfma_f32_16x16x32_bf16 v[8:11], v[184:187], v[112:115], v[8:11]\n\t"
      "ds_read_b64_tr_b16 v[180:181], v234 offset:224\n\t"
      "ds_read_b64_tr_b16 v[182:183], v234 offset:2528\n\t"
      "v_mfma_f32_16x16x32_bf16 v[44:47], v[184:187], v[116:119], v[44:47]\n\t"
      "s_waitcnt lgkmcnt(8)\n\t"
      "v_mfma_f32_16x16x32_bf16 v[12:15], v[188:191], v[112:115], v[12:15]\n\t"
      "ds_read_b64_tr_b16 v[184:185], v234 offset:256\n\t"
      "ds_read_b64_tr_b16 v[186:187], v234 offset:2560\n\t"
      "v_mfma_f32_16x16x32_bf16 v[48:51], v[188:191], v[116:119], v[48:51]\n\t"
      "s_waitcnt lgkmcnt(8)\n\t"
      "v_mfma_f32_16x16x32_bf16 v[16:19], v[192:195], v[112:115], v[16:19]\n\t"
      "ds_read_b64_tr_b16 v[188:189], v234 offset:18432\n\t"
      "ds_read_b64_tr_b16 v[190:191], v234 offset:20736\n\t"
      "v_mfma_f32_16x16x32_bf16 v[52:55], v[192:195], v[116:119], v[52:55]\n\t"
      "s_add_u32 m0, s51, 1024\n\t"
      "s_mov_b32 s49, s22\n\t"
      "buffer_load_dwordx4 v253, s[92:95], s49 offen lds\n\t"
      "s_waitcnt lgkmcnt(8)\n\t"
      "v_mfma_f32_16x16x32_bf16 v[20:23], v[196:199], v[112:115], v[20:23]\n\t"
      "ds_read_b64_tr_b16 v[192:193], v234 offset:18464\n\t"
      "ds_read_b64_tr_b16 v[194:195], v234 offset:20768\n\t"
      "v_mfma_f32_16x16x32_bf16 v[56:59], v[196:199], v[116:119], v[56:59]\n\t"
      "s_waitcnt lgkmcnt(8)\n\t"
      "v_mfma_f32_16x16x32_bf16 v[24:27], v[176:179], v[112:115], v[24:27]\n\t"
      "ds_read_b64_tr_b16 v[196:197], v234 offset:18496\n\t"
      "ds_read_b64_tr_b16 v[198:199], v234 offset:20800\n\t"
      "v_mfma_f32_16x16x32_bf16 v[60:63], v[176:179], v[116:119], v[60:63]\n\t"
      "s_waitcnt lgkmcnt(8)\n\t"
      "v_mfma_f32_16x16x32_bf16 v[28:31], v[180:183], v[112:115], v[28:31]\n\t"
      "ds_read_b64_tr_b16 v[176:177], v234 offset:18528\n\t"
      "ds_read_b64_tr_b16 v[178:179], v234 offset:20832\n\t"
      "v_mfma_f32_16x16x32_bf16 v[64:67], v[180:183], v[116:119], v[64:67]\n\t"
      "s_add_u32 m0, s51, 2048\n\t"
      "s_mov_b32 s49, s22\n\t"
      "s_mov_b64 exec, 0xffff\n\t"
      "buffer_load_dwordx4 v254, s[92:95], s49 offen lds\n\t"
      "s_mov_b64 exec, -1\n\t"
      "s_waitcnt lgkmcnt(8)\n\t"
      "v_mfma_f32_16x16x32_bf16 v[32:35], v[184:187], v[112:115], v[32:35]\n\t"
      "ds_read_b64_tr_b16 v[180:181], v234 offset:18560\n\t"
      "ds_read_b64_tr_b16 v[182:183], v234 offset:20864\n\t"
      "v_mfma_f32_16x16x32_bf16 v[68:71], v[184:187], v[116:119], v[68:71]\n\t"
      "s_add_u32 s48, s29, 8192\n\t"
      "buffer_load_dwordx4 v[112:115], v237, s[44:47], s48 offen offset:0 sc1\n\t"
      "s_add_u32 s48, s29, 8192\n\t"
      "buffer_load_dwordx4 v[116:119], v237, s[44:47], s48 offen offset:256 sc1\n\t"
      "s_waitcnt lgkmcnt(8)\n\t"
      "v_mfma_f32_16x16x32_bf16 v[0:3], v[188:191], v[120:123], v[0:3]\n\t"
      "ds_read_b64_tr_b16 v[184:185], v234 offset:18592\n\t"
      "ds_read_b64_tr_b16 v[186:187], v234 offset:20896\n\t"
      "v_mfma_f32_16x16x32_bf16 v[36:39], v[188:191], v[124:127], v[36:39]\n\t"
      "s_waitcnt lgkmcnt(8)\n\t"
      "v_mfma_f32_16x16x32_bf16 v[4:7], v[192:195], v[120:123], v[4:7]\n\t"
      "ds_read_b64_tr_b16 v[188:189], v234 offset:18624\n\t"
      "ds_read_b64_tr_b16 v[190:191], v234 offset:20928\n\t"
      "v_mfma_f32_16x16x32_bf16 v[40:43], v[192:195], v[124:127], v[40:43]\n\t"
      "s_waitcnt lgkmcnt(8)\n\t"
      "v_mfma_f32_16x16x32_bf16 v[8:11], v[196:199], v[120:123], v[8:11]\n\t"
      "ds_read_b64_tr_b16 v[192:193], v234 offset:18656\n\t"
      "ds_read_b64_tr_b16 v[194:195], v234 offset:20960\n\t"
      "v_mfma_f32_16x16x32_bf16 v[44:47], v[196:199], v[124:127], v[44:47]\n\t"
      "s_waitcnt lgkmcnt(8)\n\t"
      "v_mfma_f32_16x16x32_bf16 v[12:15], v[176:179], v[120:123], v[12:15]\n\t"
      "ds_read_b64_tr_b16 v[196:197], v234 offset:18688\n\t"
      "ds_read_b64_tr_b16 v[198:199], v234 offset:20992\n\t"
      "v_mfma_f32_16x16x32_bf16 v[48:51], v[176:179], v[124:127], v[48:51]\n\t"
      "s_waitcnt lgkmcnt(8)\n\t"
      "v_mfma_f32_16x16x32_bf16 v[16:19], v[180:183], v[120:123], v[16:19]\n\t"
      "ds_read_b64_tr_b16 v[176:177], v232 offset:0\n\t"
      "ds_read_b64_tr_b16 v[178:179], v232 offset:2304\n\t"
      "v_mfma_f32_16x16x32_bf16 v[52:55], v[180:183], v[124:127], v[52:55]\n\t"
      "s_waitcnt lgkmcnt(8)\n\t"
      "v_mfma_f32_16x16x32_bf16 v[20:23], v[184:187], v[120:123], v[20:23]\n\t"
      "ds_read_b64_tr_b16 v[180:181], v232 offset:32\n\t"
      "ds_read_b64_tr_b16 v[182:183], v232 offset:2336\n\t"
      "v_mfma_f32_16x16x32_bf16 v[56:59], v[184:187], v[124:127], v[56:59]\n\t"
      "s_waitcnt lgkmcnt(8)\n\t"
      "v_mfma_f32_16x16x32_bf16 v[24:27], v[188:191], v[120:123], v[24:27]\n\t"
      "ds_read_b64_tr_b16 v[184:185], v232 offset:64\n\t"
      "ds_read_b64_tr_b16 v[186:187], v232 offset:2368\n\t"
      "v_mfma_f32_16x16x32_bf16 v[60:63], v[188:191], v[124:127], v[60:63]\n\t"
      "s_waitcnt lgkmcnt(8)\n\t"
      "v_mfma_f32_16x16x32_bf16 v[28:31], v[192:195], v[120:123], v[28:31]\n\t"
      "ds_read_b64_tr_b16 v[188:189], v232 offset:96\n\t"
      "ds_read_b64_tr_b16 v[190:191], v232 offset:2400\n\t"
      "v_mfma_f32_16x16x32_bf16 v[64:67], v[192:195], v[124:127], v[64:67]\n\t"
      "s_waitcnt lgkmcnt(8)\n\t"
      "v_mfma_f32_16x16x32_bf16 v[32:35], v[196:199], v[120:123], v[32:35]\n\t"
      "ds_read_b64_tr_b16 v[192:193], v232 offset:128\n\t"
      "ds_read_b64_tr_b16 v[194:195], v232 offset:2432\n\t"
      "v_mfma_f32_16x16x32_bf16 v[68:71], v[196:199], v[124:127], v[68:71]\n\t"
      "s_waitcnt vmcnt(12)\n\t"
      "s_barrier\n\t"
      "s_waitcnt lgkmcnt(8)\n\t"
      "v_mfma_f32_16x16x32_bf16 v[0:3], v[176:179], v[128:131], v[0:3]\n\t"
      "ds_read_b64_tr_b16 v[196:197], v232 offset:160\n\t"
      "ds_read_b64_tr_b16 v[198:199], v232 offset:2464\n\t"
      "v_mfma_f32_16x16x32_bf16 v[36:39], v[176:179], v[132:135], v[36:39]\n\t"
      "s_waitcnt lgkmcnt(8)\n\t"
      "v_mfma_f32_16x16x32_bf16 v[4:7], v[180:183], v[128:131], v[4:7]\n\t"
      "ds_read_b64_tr_b16 v[176:177], v232 offset:192\n\t"
      "ds_read_b64_tr_b16 v[178:179], v232 offset:2496\n\t"
      "v_mfma_f32_16x16x32_bf16 v[40:43], v[180:183], v[132:135], v[40:43]\n\t"
      "s_waitcnt lgkmcnt(8)\n\t"
      "v_mfma_f32_16x16x32_bf16 v[8:11], v[184:187], v[128:131], v[8:11]\n\t"
      "ds_read_b64_tr_b16 v[180:181], v232 offset:224\n\t"
      "ds_read_b64_tr_b16 v[182:183], v232 offset:2528\n\t"
      "v_mfma_f32_16x16x32_bf16 v[44:47], v[184:187], v[132:135], v[44:47]\n\t"
      "s_waitcnt lgkmcnt(8)\n\t"
      "v_mfma_f32_16x16x32_bf16 v[12:15], v[188:191], v[128:131], v[12:15]\n\t"
      "ds_read_b64_tr_b16 v[184:185], v232 offset:256\n\t"
      "ds_read_b64_tr_b16 v[186:187], v232 offset:2560\n\t"
      "v_mfma_f32_16x16x32_bf16 v[48:51], v[188:191], v[132:135], v[48:51]\n\t"
      "s_waitcnt lgkmcnt(8)\n\t"
      "v_mfma_f32_16x16x32_bf16 v[16:19], v[192:195], v[128:131], v[16:19]\n\t"
      "ds_read_b64_tr_b16 v[188:189], v232 offset:18432\n\t"
      "ds_read_b64_tr_b16 v[190:191], v232 offset:20736\n\t"
      "v_mfma_f32_16x16x32_bf16 v[52:55], v[192:195], v[132:135], v[52:55]\n\t"
      "s_waitcnt lgkmcnt(8)\n\t"
      "v_mfma_f32_16x16x32_bf16 v[20:23], v[196:199], v[128:131], v[20:23]\n\t"
      "ds_read_b64_tr_b16 v[192:193], v232 offset:18464\n\t"
      "ds_read_b64_tr_b16 v[194:195], v232 offset:20768\n\t"
      "v_mfma_f32_16x16x32_bf16 v[56:59], v[196:199], v[132:135], v[56:59]\n\t"
      "s_waitcnt lgkmcnt(8)\n\t"
      "v_mfma_f32_16x16x32_bf16 v[24:27], v[176:179], v[128:131], v[24:27]\n\t"
      "ds_read_b64_tr_b16 v[196:197], v232 offset:18496\n\t"
      "ds_read_b64_tr_b16 v[198:199], v232 offset:20800\n\t"
      "v_mfma_f32_16x16x32_bf16 v[60:63], v[176:179], v[132:135], v[60:63]\n\t"
      "s_waitcnt lgkmcnt(8)\n\t"
      "v_mfma_f32_16x16x32_bf16 v[28:31], v[180:183], v[128:131], v[28:31]\n\t"
      "ds_read_b64_tr_b16 v[176:177], v232 offset:18528\n\t"
      "ds_read_b64_tr_b16 v[178:179], v232 offset:20832\n\t"
      "v_mfma_f32_16x16x32_bf16 v[64:67], v[180:183], v[132:135], v[64:67]\n\t"
      "s_waitcnt lgkmcnt(8)\n\t"
      "v_mfma_f32_16x16x32_bf16 v[32:35], v[184:187], v[128:131], v[32:35]\n\t"
      "ds_read_b64_tr_b16 v[180:181], v232 offset:18560\n\t"
      "ds_read_b64_tr_b16 v[182:183], v232 offset:20864\n\t"
      "v_mfma_f32_16x16x32_bf16 v[68:71], v[184:187], v[132:135], v[68:71]\n\t"
      "s_waitcnt lgkmcnt(8)\n\t"
      "v_mfma_f32_16x16x32_bf16 v[0:3], v[188:191], v[136:139], v[0:3]\n\t"
      "ds_read_b64_tr_b16 v[184:185], v232 offset:18592\n\t"
      "ds_read_b64_tr_b16 v[186:187], v232 offset:20896\n\t"
      "v_mfma_f32_16x16x32_bf16 v[36:39], v[188:191], v[140:143], v[36:39]\n\t"
      "s_waitcnt lgkmcnt(8)\n\t"
      "v_mfma_f32_16x16x32_bf16 v[4:7], v[192:195], v[136:139], v[4:7]\n\t"
      "ds_read_b64_tr_b16 v[188:189], v232 offset:18624\n\t"
      "ds_read_b64_tr_b16 v[190:191], v232 offset:20928\n\t"
      "v_mfma_f32_16x16x32_bf16 v[40:43], v[192:195], v[140:143], v[40:43]\n\t"
      "s_waitcnt lgkmcnt(8)\n\t"
      "v_mfma_f32_16x16x32_bf16 v[8:11], v[196:199], v[136:139], v[8:11]\n\t"
      "ds_read_b64_tr_b16 v[192:193], v232 offset:18656\n\t"
      "ds_read_b64_tr_b16 v[194:195], v232 offset:20960\n\t"
      "v_mfma_f32_16x16x32_bf16 v[44:47], v[196:199], v[140:143], v[44:47]\n\t"
      "s_waitcnt lgkmcnt(8)\n\t"
      "v_mfma_f32_16x16x32_bf16 v[12:15], v[176:179], v[136:139], v[12:15]\n\t"
      "ds_read_b64_tr_b16 v[196:197], v232 offset:18688\n\t"
      "ds_read_b64_tr_b16 v[198:199], v232 offset:20992\n\t"
      "v_mfma_f32_16x16x32_bf16 v[48:51], v[176:179], v[140:143], v[48:51]\n\t"
      "s_waitcnt lgkmcnt(8)\n\t"
      "v_mfma_f32_16x16x32_bf16 v[16:19], v[180:183], v[136:139], v[16:19]\n\t"
      "ds_read_b64_tr_b16 v[176:177], v233 offset:0\n\t"
      "ds_read_b64_tr_b16 v[178:179], v233 offset:2304\n\t"
      "v_mfma_f32_16x16x32_bf16 v[52:55], v[180:183], v[140:143], v[52:55]\n\t"
      "s_waitcnt lgkmcnt(8)\n\t"
      "v_mfma_f32_16x16x32_bf16 v[20:23], v[184:187], v[136:139], v[20:23]\n\t"
      "ds_read_b64_tr_b16 v[180:181], v233 offset:32\n\t"
      "ds_read_b64_tr_b16 v[182:183], v233 offset:2336\n\t"
      "v_mfma_f32_16x16x32_bf16 v[56:59], v[184:187], v[140:143], v[56:59]\n\t"
      "s_waitcnt lgkmcnt(8)\n\t"
      "v_mfma_f32_16x16x32_bf16 v[24:27], v[188:191], v[136:139], v[24:27]\n\t"
      "ds_read_b64_tr_b16 v[184:185], v233 offset:64\n\t"
      "ds_read_b64_tr_b16 v[186:187], v233 offset:2368\n\t"
      "v_mfma_f32_16x16x32_bf16 v[60:63], v[188:191], v[140:143], v[60:63]\n\t"
      "s_waitcnt lgkmcnt(8)\n\t"
      "v_mfma_f32_16x16x32_bf16 v[28:31], v[192:195], v[136:139], v[28:31]\n\t"
      "ds_read_b64_tr_b16 v[188:189], v233 offset:96\n\t"
      "ds_read_b64_tr_b16 v[190:191], v233 offset:2400\n\t"
      "v_mfma_f32_16x16x32_bf16 v[64:67], v[192:195], v[140:143], v[64:67]\n\t"
      "s_waitcnt lgkmcnt(8)\n\t"
      "v_mfma_f32_16x16x32_bf16 v[32:35], v[196:199], v[136:139], v[32:35]\n\t"
      "ds_read_b64_tr_b16 v[192:193], v233 offset:128\n\t"
      "ds_read_b64_tr_b16 v[194:195], v233 offset:2432\n\t"
      "v_mfma_f32_16x16x32_bf16 v[68:71], v[196:199], v[140:143], v[68:71]\n\t"
      "s_mov_b32 s48, s39\n\t"
      "s_mul_i32 s49, s48, 9216\n\t"
      "s_add_u32 s52, s20, 18432\n\t"
      "s_sub_u32 s53, 18432, s20\n\t"
      "s_cmp_lt_u32 s48, 6\n\t"
      "s_cselect_b32 s52, s52, s53\n\t"
      "s_add_u32 s49, s49, s52\n\t"
      "v_add_u32 v72, s49, v240\n\t"
      "s_xor_b32 s48, s39, 4\n\t"
      "s_mul_i32 s49, s48, 9216\n\t"
      "s_add_u32 s52, s20, 18432\n\t"
      "s_sub_u32 s53, 18432, s20\n\t"
      "s_cmp_lt_u32 s48, 6\n\t"
      "s_cselect_b32 s52, s52, s53\n\t"
      "s_add_u32 s49, s49, s52\n\t"
      "v_add_u32 v73, s49, v240\n\t"
      "s_bitcmp1_b32 s33, 27\n\t"
      "s_cselect_b32 s54, 0x40000000, s34\n\t"
      "s_add_u32 s55, s54, 4096\n\t"
      "s_add_u32 s56, s54, 8192\n\t"
      "s_waitcnt vmcnt(2)\n\t"
      "s_barrier\n\t"
      "s_waitcnt lgkmcnt(8)\n\t"
      "v_mfma_f32_16x16x32_bf16 v[0:3], v[176:179], v[96:99], v[0:3]\n\t"
      "ds_read_b64_tr_b16 v[196:197], v233 offset:160\n\t"
      "ds_read_b64_tr_b16 v[198:199], v233 offset:2464\n\t"
      "v_mfma_f32_16x16x32_bf16 v[36:39], v[176:179], v[100:103], v[36:39]\n\t"
      "s_waitcnt lgkmcnt(8)\n\t"
      "v_mfma_f32_16x16x32_bf16 v[4:7], v[180:183], v[96:99], v[4:7]\n\t"
      "ds_read_b64_tr_b16 v[176:177], v233 offset:192\n\t"
      "ds_read_b64_tr_b16 v[178:179], v233 offset:2496\n\t"
      "v_mfma_f32_16x16x32_bf16 v[40:43], v[180:183], v[100:103], v[40:43]\n\t"
      "s_waitcnt lgkmcnt(8)\n\t"
      "v_mfma_f32_16x16x32_bf16 v[8:11], v[184:187], v[96:99], v[8:11]\n\t"
      "ds_read_b64_tr_b16 v[180:181], v233 offset:224\n\t"
      "ds_read_b64_tr_b16 v[182:183], v233 offset:2528\n\t"
      "v_mfma_f32_16x16x32_bf16 v[44:47], v[184:187], v[100:103], v[44:47]\n\t"
      "s_waitcnt lgkmcnt(8)\n\t"
      "v_mfma_f32_16x16x32_bf16 v[12:15], v[188:191], v[96:99], v[12:15]\n\t"
      "ds_read_b64_tr_b16 v[184:185], v233 offset:256\n\t"
      "ds_read_b64_tr_b16 v[186:187], v233 offset:2560\n\t"
      "v_mfma_f32_16x16x32_bf16 v[48:51], v[188:191], v[100:103], v[48:51]\n\t"
      "s_waitcnt lgkmcnt(8)\n\t"
      "v_mfma_f32_16x16x32_bf16 v[16:19], v[192:195], v[96:99], v[16:19]\n\t"
      "ds_read_b64_tr_b16 v[188:189], v233 offset:18432\n\t"
      "ds_read_b64_tr_b16 v[190:191], v233 offset:20736\n\t"
      "v_mfma_f32_16x16x32_bf16 v[52:55], v[192:195], v[100:103], v[52:55]\n\t"
      "s_waitcnt lgkmcnt(8)\n\t"
      "v_mfma_f32_16x16x32_bf16 v[20:23], v[196:199], v[96:99], v[20:23]\n\t"
      "ds_read_b64_tr_b16 v[192:193], v233 offset:18464\n\t"
      "ds_read_b64_tr_b16 v[194:195], v233 offset:20768\n\t"
      "v_mfma_f32_16x16x32_bf16 v[56:59], v[196:199], v[100:103], v[56:59]\n\t"
      "s_waitcnt lgkmcnt(8)\n\t"
      "v_mfma_f32_16x16x32_bf16 v[24:27], v[176:179], v[96:99], v[24:27]\n\t"
      "ds_read_b64_tr_b16 v[196:197], v233 offset:18496\n\t"
      "ds_read_b64_tr_b16 v[198:199], v233 offset:20800\n\t"
      "v_mfma_f32_16x16x32_bf16 v[60:63], v[176:179], v[100:103], v[60:63]\n\t"
      "s_waitcnt lgkmcnt(8)\n\t"
      "v_mfma_f32_16x16x32_bf16 v[28:31], v[180:183], v[96:99], v[28:31]\n\t"
      "ds_read_b64_tr_b16 v[176:177], v233 offset:18528\n\t"
      "ds_read_b64_tr_b16 v[178:179], v233 offset:20832\n\t"
      "v_mfma_f32_16x16x32_bf16 v[64:67], v[180:183], v[100:103], v[64:67]\n\t"
      "s_waitcnt lgkmcnt(8)\n\t"
      "v_mfma_f32_16x16x32_bf16 v[32:35], v[184:187], v[96:99], v[32:35]\n\t"
      "ds_read_b64_tr_b16 v[180:181], v233 offset:18560\n\t"
      "ds_read_b64_tr_b16 v[182:183], v233 offset:20864\n\t"
      "v_mfma_f32_16x16x32_bf16 v[68:71], v[184:187], v[100:103], v[68:71]\n\t"
      "s_bitcmp1_b32 s33, 27\n\t"
      "s_cbranch_scc1 Lxl%=\n\t"
      "Lxd%=:\n\t"
      "s_waitcnt lgkmcnt(8)\n\t"
      "v_mfma_f32_16x16x32_bf16 v[0:3], v[188:191], v[104:107], v[0:3]\n\t"
      "ds_read_b64_tr_b16 v[184:185], v233 offset:18592\n\t"
      "ds_read_b64_tr_b16 v[186:187], v233 offset:20896\n\t"
      "v_mfma_f32_16x16x32_bf16 v[36:39], v[188:191], v[108:111], v[36:39]\n\t"
      "s_waitcnt lgkmcnt(8)\n\t"
      "v_mfma_f32_16x16x32_bf16 v[4:7], v[192:195], v[104:107], v[4:7]\n\t"
      "ds_read_b64_tr_b16 v[188:189], v233 offset:18624\n\t"
      "ds_read_b64_tr_b16 v[190:191], v233 offset:20928\n\t"
      "v_mfma_f32_16x16x32_bf16 v[40:43], v[192:195], v[108:111], v[40:43]\n\t"
      "s_waitcnt lgkmcnt(8)\n\t"
      "v_mfma_f32_16x16x32_bf16 v[8:11], v[196:199], v[104:107], v[8:11]\n\t"
      "ds_read_b64_tr_b16 v[192:193], v233 offset:18656\n\t"
      "ds_read_b64_tr_b16 v[194:195], v233 offset:20960\n\t"
      "v_mfma_f32_16x16x32_bf16 v[44:47], v[196:199], v[108:111], v[44:47]\n\t"
      "s_waitcnt lgkmcnt(8)\n\t"
      "v_mfma_f32_16x16x32_bf16 v[12:15], v[176:179], v[104:107], v[12:15]\n\t"
      "ds_read_b64_tr_b16 v[196:197], v233 offset:18688\n\t"
      "ds_read_b64_tr_b16 v[198:199], v233 offset:20992\n\t"
      "v_mfma_f32_16x16x32_bf16 v[48:51], v[176:179], v[108:111], v[48:51]\n\t"
      "s_waitcnt lgkmcnt(8)\n\t"
      "v_mfma_f32_16x16x32_bf16 v[16:19], v[180:183], v[104:107], v[16:19]\n\t"
      "ds_read_b64_tr_b16 v[176:177], v236 offset:0\n\t"
      "ds_read_b64_tr_b16 v[178:179], v236 offset:2304\n\t"
      "v_mfma_f32_16x16x32_bf16 v[52:55], v[180:183], v[108:111], v[52:55]\n\t"
      "s_waitcnt lgkmcnt(8)\n\t"
      "v_mfma_f32_16x16x32_bf16 v[20:23], v[184:187], v[104:107], v[20:23]\n\t"
      "ds_read_b64_tr_b16 v[180:181], v236 offset:32\n\t"
      "ds_read_b64_tr_b16 v[182:183], v236 offset:2336\n\t"
      "v_mfma_f32_16x16x32_bf16 v[56:59], v[184:187], v[108:111], v[56:59]\n\t"
      "s_waitcnt lgkmcnt(8)\n\t"
      "v_mfma_f32_16x16x32_bf16 v[24:27], v[188:191], v[104:107], v[24:27]\n\t"
      "ds_read_b64_tr_b16 v[184:185], v236 offset:64\n\t"
      "ds_read_b64_tr_b16 v[186:187], v236 offset:2368\n\t"
      "v_mfma_f32_16x16x32_bf16 v[60:63], v[188:191], v[108:111], v[60:63]\n\t"
      "s_waitcnt lgkmcnt(8)\n\t"
      "v_mfma_f32_16x16x32_bf16 v[28:31], v[192:195], v[104:107], v[28:31]\n\t"
      "ds_read_b64_tr_b16 v[188:189], v236 offset:96\n\t"
      "ds_read_b64_tr_b16 v[190:191], v236 offset:2400\n\t"
      "v_mfma_f32_16x16x32_bf16 v[64:67], v[192:195], v[108:111], v[64:67]\n\t"
      "s_waitcnt lgkmcnt(8)\n\t"
      "v_mfma_f32_16x16x32_bf16 v[32:35], v[196:199], v[104:107], v[32:35]\n\t"
      "ds_read_b64_tr_b16 v[192:193], v236 offset:128\n\t"
      "ds_read_b64_tr_b16 v[194:195], v236 offset:2432\n\t"
      "v_mfma_f32_16x16x32_bf16 v[68:71], v[196:199], v[108:111], v[68:71]\n\t"
      "s_waitcnt vmcnt(0)\n\t"
      "s_cmp_ge_u32 s39, 4\n\t"
      "s_cbranch_scc1 Lskip%=\n\t"
      "s_waitcnt lgkmcnt(8)\n\t"
      "v_mfma_f32_16x16x32_bf16 v[0:3], v[176:179], v[112:115], v[0:3]\n\t"
      "ds_read_b64_tr_b16 v[196:197], v236 offset:160\n\t"
      "ds_read_b64_tr_b16 v[198:199], v236 offset:2464\n\t"
      "v_mfma_f32_16x16x32_bf16 v[36:39], v[176:179], v[116:119], v[36:39]\n\t"
      "s_waitcnt lgkmcnt(8)\n\t"
      "v_mfma_f32_16x16x32_bf16 v[4:7], v[180:183], v[112:115], v[4:7]\n\t"
      "ds_read_b64_tr_b16 v[176:177], v236 offset:192\n\t"
      "ds_read_b64_tr_b16 v[178:179], v236 offset:2496\n\t"
      "v_mfma_f32_16x16x32_bf16 v[40:43], v[180:183], v[116:119], v[40:43]\n\t"
      "s_waitcnt lgkmcnt(8)\n\t"
      "v_mfma_f32_16x16x32_bf16 v[8:11], v[184:187], v[112:115], v[8:11]\n\t"
      "ds_read_b64_tr_b16 v[180:181], v236 offset:224\n\t"
      "ds_read_b64_tr_b16 v[182:183], v236 offset:2528\n\t"
      "v_mfma_f32_16x16x32_bf16 v[44:47], v[184:187], v[116:119], v[44:47]\n\t"
      "s_waitcnt lgkmcnt(8)\n\t"
      "v_mfma_f32_16x16x32_bf16 v[12:15], v[188:191], v[112:115], v[12:15]\n\t"
      "ds_read_b64_tr_b16 v[184:185], v236 offset:256\n\t"
      "ds_read_b64_tr_b16 v[186:187], v236 offset:2560\n\t"
      "v_mfma_f32_16x16x32_bf16 v[48:51], v[188:191], v[116:119], v[48:51]\n\t"
      "s_waitcnt lgkmcnt(8)\n\t"
      "v_mfma_f32_16x16x32_bf16 v[16:19], v[192:195], v[112:115], v[16:19]\n\t"
      "v_mfma_f32_16x16x32_bf16 v[52:55], v[192:195], v[116:119], v[52:55]\n\t"
      "s_waitcnt lgkmcnt(6)\n\t"
      "v_mfma_f32_16x16x32_bf16 v[20:23], v[196:199], v[112:115], v[20:23]\n\t"
      "v_mfma_f32_16x16x32_bf16 v[56:59], v[196:199], v[116:119], v[56:59]\n\t"
      "s_waitcnt lgkmcnt(4)\n\t"
      "v_mfma_f32_16x16x32_bf16 v[24:27], v[176:179], v[112:115], v[24:27]\n\t"
      "v_mfma_f32_16x16x32_bf16 v[60:63], v[176:179], v[116:119], v[60:63]\n\t"
      "s_waitcnt lgkmcnt(2)\n\t"
      "v_mfma_f32_16x16x32_bf16 v[28:31], v[180:183], v[112:115], v[28:31]\n\t"
      "v_mfma_f32_16x16x32_bf16 v[64:67], v[180:183], v[116:119], v[64:67]\n\t"
      "s_waitcnt lgkmcnt(0)\n\t"
      "v_mfma_f32_16x16x32_bf16 v[32:35], v[184:187], v[112:115], v[32:35]\n\t"
      "v_mfma_f32_16x16x32_bf16 v[68:71], v[184:187], v[116:119], v[68:71]\n\t"
      "s_waitcnt lgkmcnt(0)\n\t"
      "s_nop 3\n\t"
      "ds_write_b128 v72, v[36:39] offset:0\n\t"
      "ds_write_b128 v72, v[40:43] offset:1024\n\t"
      "ds_write_b128 v72, v[44:47] offset:2048\n\t"
      "ds_write_b128 v72, v[48:51] offset:3072\n\t"
      "ds_write_b128 v72, v[52:55] offset:4096\n\t"
      "ds_write_b128 v72, v[56:59] offset:5120\n\t"
      "ds_write_b128 v72, v[60:63] offset:6144\n\t"
      "ds_write_b128 v72, v[64:67] offset:7168\n\t"
      "ds_write_b128 v72, v[68:71] offset:8192\n\t"
      "s_waitcnt lgkmcnt(0)\n\t"
      "s_barrier\n\t"
      "ds_read_b128 v[76:79], v73 offset:0\n\t"
      "ds_read_b128 v[80:83], v73 offset:1024\n\t"
      "ds_read_b128 v[84:87], v73 offset:2048\n\t"
      "ds_read_b128 v[88:91], v73 offset:3072\n\t"
      "ds_read_b128 v[92:95], v73 offset:4096\n\t"
      "ds_read_b128 v[96:99], v73 offset:5120\n\t"
      "ds_read_b128 v[100:103], v73 offset:6144\n\t"
      "ds_read_b128 v[104:107], v73 offset:7168\n\t"
      "ds_read_b128 v[108:111], v73 offset:8192\n\t"
      "s_waitcnt lgkmcnt(8)\n\t"
      "v_pk_add_f32 v[76:77], v[76:77], v[0:1]\n\t"
      "v_pk_add_f32 v[78:79], v[78:79], v[2:3]\n\t"
      "v_cvt_pk_bf16_f32 v112, v76, v77\n\t"
      "v_cvt_pk_bf16_f32 v113, v78, v79\n\t"
      "s_waitcnt lgkmcnt(7)\n\t"
      "v_pk_add_f32 v[80:81], v[80:81], v[4:5]\n\t"
      "v_pk_add_f32 v[82:83], v[82:83], v[6:7]\n\t"
      "v_cvt_pk_bf16_f32 v114, v80, v81\n\t"
      "v_cvt_pk_bf16_f32 v115, v82, v83\n\t"
      "buffer_store_dwordx4 v[112:115], v240, s[44:47], s54 offen offset:0 sc1\n\t"
      "s_waitcnt lgkmcnt(6)\n\t"
      "v_pk_add_f32 v[84:85], v[84:85], v[8:9]\n\t"
      "v_pk_add_f32 v[86:87], v[86:87], v[10:11]\n\t"
      "v_cvt_pk_bf16_f32 v116, v84, v85\n\t"
      "v_cvt_pk_bf16_f32 v117, v86, v87\n\t"
      "s_waitcnt lgkmcnt(5)\n\t"
      "v_pk_add_f32 v[88:89], v[88:89], v[12:13]\n\t"
      "v_pk_add_f32 v[90:91], v[90:91], v[14:15]\n\t"
      "v_cvt_pk_bf16_f32 v118, v88, v89\n\t"
      "v_cvt_pk_bf16_f32 v119, v90, v91\n\t"
      "buffer_store_dwordx4 v[116:119], v240, s[44:47], s54 offen offset:1024 sc1\n\t"
      "s_waitcnt lgkmcnt(4)\n\t"
      "v_pk_add_f32 v[92:93], v[92:93], v[16:17]\n\t"
      "v_pk_add_f32 v[94:95], v[94:95], v[18:19]\n\t"
      "v_cvt_pk_bf16_f32 v120, v92, v93\n\t"
      "v_cvt_pk_bf16_f32 v121, v94, v95\n\t"
      "s_waitcnt lgkmcnt(3)\n\t"
      "v_pk_add_f32 v[96:97], v[96:97], v[20:21]\n\t"
      "v_pk_add_f32 v[98:99], v[98:99], v[22:23]\n\t"
      "v_cvt_pk_bf16_f32 v122, v96, v97\n\t"
      "v_cvt_pk_bf16_f32 v123, v98, v99\n\t"
      "buffer_store_dwordx4 v[120:123], v240, s[44:47], s54 offen offset:2048 sc1\n\t"
      "s_waitcnt lgkmcnt(2)\n\t"
      "v_pk_add_f32 v[100:101], v[100:101], v[24:25]\n\t"
      "v_pk_add_f32 v[102:103], v[102:103], v[26:27]\n\t"
      "v_cvt_pk_bf16_f32 v124, v100, v101\n\t"
      "v_cvt_pk_bf16_f32 v125, v102, v103\n\t"
      "s_waitcnt lgkmcnt(1)\n\t"
      "v_pk_add_f32 v[104:105], v[104:105], v[28:29]\n\t"
      "v_pk_add_f32 v[106:107], v[106:107], v[30:31]\n\t"
      "v_cvt_pk_bf16_f32 v126, v104, v105\n\t"
      "v_cvt_pk_bf16_f32 v127, v106, v107\n\t"
      "buffer_store_dwordx4 v[124:127], v240, s[44:47], s54 offen offset:3072 sc1\n\t"
      "s_waitcnt lgkmcnt(0)\n\t"
      "v_pk_add_f32 v[108:109], v[108:109], v[32:33]\n\t"
      "v_pk_add_f32 v[110:111], v[110:111], v[34:35]\n\t"
      "v_cvt_pk_bf16_f32 v128, v108, v109\n\t"
      "v_cvt_pk_bf16_f32 v129, v110, v111\n\t"
      "s_branch Lred%=\n\t"
      "Lskip%=:\n\t"
      "s_waitcnt lgkmcnt(0)\n\t"
      "s_nop 3\n\t"
      "ds_write_b128 v72, v[0:3] offset:0\n\t"
      "ds_write_b128 v72, v[4:7] offset:1024\n\t"
      "ds_write_b128 v72, v[8:11] offset:2048\n\t"
      "ds_write_b128 v72, v[12:15] offset:3072\n\t"
      "ds_write_b128 v72, v[16:19] offset:4096\n\t"
      "ds_write_b128 v72, v[20:23] offset:5120\n\t"
      "ds_write_b128 v72, v[24:27] offset:6144\n\t"
      "ds_write_b128 v72, v[28:31] offset:7168\n\t"
      "ds_write_b128 v72, v[32:35] offset:8192\n\t"
      "s_waitcnt lgkmcnt(0)\n\t"
      "s_barrier\n\t"
      "ds_read_b128 v[76:79], v73 offset:0\n\t"
      "ds_read_b128 v[80:83], v73 offset:1024\n\t"
      "ds_read_b128 v[84:87], v73 offset:2048\n\t"
      "ds_read_b128 v[88:91], v73 offset:3072\n\t"
      "ds_read_b128 v[92:95], v73 offset:4096\n\t"
      "ds_read_b128 v[96:99], v73 offset:5120\n\t"
      "ds_read_b128 v[100:103], v73 offset:6144\n\t"
      "ds_read_b128 v[104:107], v73 offset:7168\n\t"
      "ds_read_b128 v[108:111], v73 offset:8192\n\t"
      "s_waitcnt lgkmcnt(8)\n\t"
      "v_pk_add_f32 v[76:77], v[76:77], v[36:37]\n\t"
      "v_pk_add_f32 v[78:79], v[78:79], v[38:39]\n\t"
      "v_cvt_pk_bf16_f32 v112, v76, v77\n\t"
      "v_cvt_pk_bf16_f32 v113, v78, v79\n\t"
      "s_waitcnt lgkmcnt(7)\n\t"
      "v_pk_add_f32 v[80:81], v[80:81], v[40:41]\n\t"
      "v_pk_add_f32 v[82:83], v[82:83], v[42:43]\n\t"
      "v_cvt_pk_bf16_f32 v114, v80, v81\n\t"
      "v_cvt_pk_bf16_f32 v115, v82, v83\n\t"
      "buffer_store_dwordx4 v[112:115], v240, s[44:47], s54 offen offset:0 sc1\n\t"
      "s_waitcnt lgkmcnt(6)\n\t"
      "v_pk_add_f32 v[84:85], v[84:85], v[44:45]\n\t"
      "v_pk_add_f32 v[86:87], v[86:87], v[46:47]\n\t"
      "v_cvt_pk_bf16_f32 v116, v84, v85\n\t"
      "v_cvt_pk_bf16_f32 v117, v86, v87\n\t"
      "s_waitcnt lgkmcnt(5)\n\t"
      "v_pk_add_f32 v[88:89], v[88:89], v[48:49]\n\t"
      "v_pk_add_f32 v[90:91], v[90:91], v[50:51]\n\t"
      "v_cvt_pk_bf16_f32 v118, v88, v89\n\t"
      "v_cvt_pk_bf16_f32 v119, v90, v91\n\t"
      "buffer_store_dwordx4 v[116:119], v240, s[44:47], s54 offen offset:1024 sc1\n\t"
      "s_waitcnt lgkmcnt(4)\n\t"
      "v_pk_add_f32 v[92:93], v[92:93], v[52:53]\n\t"
      "v_pk_add_f32 v[94:95], v[94:95], v[54:55]\n\t"
      "v_cvt_pk_bf16_f32 v120, v92, v93\n\t"
      "v_cvt_pk_bf16_f32 v121, v94, v95\n\t"
      "s_waitcnt lgkmcnt(3)\n\t"
      "v_pk_add_f32 v[96:97], v[96:97], v[56:57]\n\t"
      "v_pk_add_f32 v[98:99], v[98:99], v[58:59]\n\t"
      "v_cvt_pk_bf16_f32 v122, v96, v97\n\t"
      "v_cvt_pk_bf16_f32 v123, v98, v99\n\t"
      "buffer_store_dwordx4 v[120:123], v240, s[44:47], s54 offen offset:2048 sc1\n\t"
      "s_waitcnt lgkmcnt(2)\n\t"
      "v_pk_add_f32 v[100:101], v[100:101], v[60:61]\n\t"
      "v_pk_add_f32 v[102:103], v[102:103], v[62:63]\n\t"
      "v_cvt_pk_bf16_f32 v124, v100, v101\n\t"
      "v_cvt_pk_bf16_f32 v125, v102, v103\n\t"
      "s_waitcnt lgkmcnt(1)\n\t"
      "v_pk_add_f32 v[104:105], v[104:105], v[64:65]\n\t"
      "v_pk_add_f32 v[106:107], v[106:107], v[66:67]\n\t"
      "v_cvt_pk_bf16_f32 v126, v104, v105\n\t"
      "v_cvt_pk_bf16_f32 v127, v106, v107\n\t"
      "buffer_store_dwordx4 v[124:127], v240, s[44:47], s54 offen offset:3072 sc1\n\t"
      "s_waitcnt lgkmcnt(0)\n\t"
      "v_pk_add_f32 v[108:109], v[108:109], v[68:69]\n\t"
      "v_pk_add_f32 v[110:111], v[110:111], v[70:71]\n\t"
      "v_cvt_pk_bf16_f32 v128, v108, v109\n\t"
      "v_cvt_pk_bf16_f32 v129, v110, v111\n\t"
      "Lred%=:\n\t"
      "s_bitcmp1_b32 s33, 27\n\t"
      "s_cbranch_scc1 Lown%=\n\t"
      "v_lshrrev_b32 v20, 1, v240\n\t"
      "s_add_u32 s48, s34, 4096\n\t"
      "buffer_store_dwordx2 v[128:129], v20, s[44:47], s48 offen sc1\n\t"
      "s_waitcnt vmcnt(0)\n\t"
      "s_mov_b64 exec, 1\n\t"
      "buffer_store_dword v238, off, s[44:47], s3 sc1\n\t"
      "s_mov_b64 exec, -1\n\t"
      "s_cmp_lg_u32 s2, 1\n\t"
      "s_cbranch_scc1 Lfin%=\n\t"
      "s_mov_b32 s60, 131072\n\t"
      "Lep%=:\n\t"
      "buffer_load_dwordx4 v[24:27], v240, s[44:47], s60 offen sc1\n\t"
      "s_waitcnt vmcnt(0)\n\t"
      "v_xor_b32 v24, v24, v238\n\t"
      "v_xor_b32 v25, v25, v238\n\t"
      "v_xor_b32 v26, v26, v238\n\t"
      "v_xor_b32 v27, v27, v238\n\t"
      "v_or3_b32 v24, v24, v25, v26\n\t"
      "v_or_b32 v24, v24, v27\n\t"
      "v_cmp_ne_u32 vcc, 0, v24\n\t"
      "s_nop 1\n\t"
      "s_cmp_eq_u64 vcc, 0\n\t"
      "s_cbranch_scc1 Lepg%=\n\t"
      "s_cmp_eq_u32 s61, 0\n\t"
      "s_cbranch_scc1 Lepg%=\n\t"
      "s_sub_u32 s61, s61, 1\n\t"
      "s_sleep 4\n\t"
      "s_branch Lep%=\n\t"
      "Lepg%=:\n\t"
      "s_mov_b64 exec, 1\n\t"
      "buffer_store_dword v238, off, s[44:47], 0 sc1\n\t"
      "s_mov_b64 exec, -1\n\t"
      "s_branch Lfin%=\n\t"
      "Lown%=:\n\t"
      "s_mov_b32 s88, s68\n\t"
      "s_mov_b32 s89, s69\n\t"
      "s_mov_b32 s90, 2359296\n\t"
      "s_and_b32 s60, s33, 0x7ffffff\n\t"
      "s_and_b32 s52, s3, 0xffffffc0\n\t"
      "s_bfe_u32 s53, s3, 0x20002\n\t"
      "s_lshl_b32 s53, 1, s53\n\t"
      "s_xor_b32 s53, s53, 15\n\t"
      "v_mov_b32 v239, v238\n\t"
      "s_waitcnt vmcnt(0)\n\t"
      "v_lshlrev_b32 v0, 16, v144\n\t"
      "v_and_b32 v1, 0xffff0000, v144\n\t"
      "v_pk_add_f32 v[76:77], v[76:77], v[0:1]\n\t"
      "v_lshlrev_b32 v0, 16, v200\n\t"
      "v_and_b32 v1, 0xffff0000, v200\n\t"
      "v_pk_add_f32 v[76:77], v[76:77], v[0:1]\n\t"
      "v_lshlrev_b32 v0, 16, v145\n\t"
      "v_and_b32 v1, 0xffff0000, v145\n\t"
      "v_pk_add_f32 v[78:79], v[78:79], v[0:1]\n\t"
      "v_lshlrev_b32 v0, 16, v201\n\t"
      "v_and_b32 v1, 0xffff0000, v201\n\t"
      "v_pk_add_f32 v[78:79], v[78:79], v[0:1]\n\t"
      "v_lshlrev_b32 v0, 16, v146\n\t"
      "v_and_b32 v1, 0xffff0000, v146\n\t"
      "v_pk_add_f32 v[80:81], v[80:81], v[0:1]\n\t"
      "v_lshlrev_b32 v0, 16, v202\n\t"
      "v_and_b32 v1, 0xffff0000, v202\n\t"
      "v_pk_add_f32 v[80:81], v[80:81], v[0:1]\n\t"
      "v_lshlrev_b32 v0, 16, v147\n\t"
      "v_and_b32 v1, 0xffff0000, v147\n\t"
      "v_pk_add_f32 v[82:83], v[82:83], v[0:1]\n\t"
      "v_lshlrev_b32 v0, 16, v203\n\t"
      "v_and_b32 v1, 0xffff0000, v203\n\t"
      "v_pk_add_f32 v[82:83], v[82:83], v[0:1]\n\t"
      "v_lshlrev_b32 v0, 16, v148\n\t"
      "v_and_b32 v1, 0xffff0000, v148\n\t"
      "v_pk_add_f32 v[84:85], v[84:85], v[0:1]\n\t"
      "v_lshlrev_b32 v0, 16, v204\n\t"
      "v_and_b32 v1, 0xffff0000, v204\n\t"
      "v_pk_add_f32 v[84:85], v[84:85], v[0:1]\n\t"
      "v_lshlrev_b32 v0, 16, v149\n\t"
      "v_and_b32 v1, 0xffff0000, v149\n\t"
      "v_pk_add_f32 v[86:87], v[86:87], v[0:1]\n\t"
      "v_lshlrev_b32 v0, 16, v205\n\t"
      "v_and_b32 v1, 0xffff0000, v205\n\t"
      "v_pk_add_f32 v[86:87], v[86:87], v[0:1]\n\t"
      "v_lshlrev_b32 v0, 16, v150\n\t"
      "v_and_b32 v1, 0xffff0000, v150\n\t"
      "v_pk_add_f32 v[88:89], v[88:89], v[0:1]\n\t"
      "v_lshlrev_b32 v0, 16, v206\n\t"
      "v_and_b32 v1, 0xffff0000, v206\n\t"
      "v_pk_add_f32 v[88:89], v[88:89], v[0:1]\n\t"
      "v_lshlrev_b32 v0, 16, v151\n\t"
      "v_and_b32 v1, 0xffff0000, v151\n\t"
      "v_pk_add_f32 v[90:91], v[90:91], v[0:1]\n\t"
      "v_lshlrev_b32 v0, 16, v207\n\t"
      "v_and_b32 v1, 0xffff0000, v207\n\t"
      "v_pk_add_f32 v[90:91], v[90:91], v[0:1]\n\t"
      "v_lshlrev_b32 v0, 16, v152\n\t"
      "v_and_b32 v1, 0xffff0000, v152\n\t"
      "v_pk_add_f32 v[92:93], v[92:93], v[0:1]\n\t"
      "v_lshlrev_b32 v0, 16, v208\n\t"
      "v_and_b32 v1, 0xffff0000, v208\n\t"
      "v_pk_add_f32 v[92:93], v[92:93], v[0:1]\n\t"
      "v_lshlrev_b32 v0, 16, v153\n\t"
      "v_and_b32 v1, 0xffff0000, v153\n\t"
      "v_pk_add_f32 v[94:95], v[94:95], v[0:1]\n\t"
      "v_lshlrev_b32 v0, 16, v209\n\t"
      "v_and_b32 v1, 0xffff0000, v209\n\t"
      "v_pk_add_f32 v[94:95], v[94:95], v[0:1]\n\t"
      "v_lshlrev_b32 v0, 16, v154\n\t"
      "v_and_b32 v1, 0xffff0000, v154\n\t"
      "v_pk_add_f32 v[96:97], v[96:97], v[0:1]\n\t"
      "v_lshlrev_b32 v0, 16, v210\n\t"
      "v_and_b32 v1, 0xffff0000, v210\n\t"
      "v_pk_add_f32 v[96:97], v[96:97], v[0:1]\n\t"
      "v_lshlrev_b32 v0, 16, v155\n\t"
      "v_and_b32 v1, 0xffff0000, v155\n\t"
      "v_pk_add_f32 v[98:99], v[98:99], v[0:1]\n\t"
      "v_lshlrev_b32 v0, 16, v211\n\t"
      "v_and_b32 v1, 0xffff0000, v211\n\t"
      "v_pk_add_f32 v[98:99], v[98:99], v[0:1]\n\t"
      "v_lshlrev_b32 v0, 16, v156\n\t"
      "v_and_b32 v1, 0xffff0000, v156\n\t"
      "v_pk_add_f32 v[100:101], v[100:101], v[0:1]\n\t"
      "v_lshlrev_b32 v0, 16, v212\n\t"
      "v_and_b32 v1, 0xffff0000, v212\n\t"
      "v_pk_add_f32 v[100:101], v[100:101], v[0:1]\n\t"
      "v_lshlrev_b32 v0, 16, v157\n\t"
      "v_and_b32 v1, 0xffff0000, v157\n\t"
      "v_pk_add_f32 v[102:103], v[102:103], v[0:1]\n\t"
      "v_lshlrev_b32 v0, 16, v213\n\t"
      "v_and_b32 v1, 0xffff0000, v213\n\t"
      "v_pk_add_f32 v[102:103], v[102:103], v[0:1]\n\t"
      "v_lshlrev_b32 v0, 16, v158\n\t"
      "v_and_b32 v1, 0xffff0000, v158\n\t"
      "v_pk_add_f32 v[104:105], v[104:105], v[0:1]\n\t"
      "v_lshlrev_b32 v0, 16, v214\n\t"
      "v_and_b32 v1, 0xffff0000, v214\n\t"
      "v_pk_add_f32 v[104:105], v[104:105], v[0:1]\n\t"
      "v_lshlrev_b32 v0, 16, v159\n\t"
      "v_and_b32 v1, 0xffff0000, v159\n\t"
      "v_pk_add_f32 v[106:107], v[106:107], v[0:1]\n\t"
      "v_lshlrev_b32 v0, 16, v215\n\t"
      "v_and_b32 v1, 0xffff0000, v215\n\t"
      "v_pk_add_f32 v[106:107], v[106:107], v[0:1]\n\t"
      "v_lshlrev_b32 v0, 16, v160\n\t"
      "v_and_b32 v1, 0xffff0000, v160\n\t"
      "v_pk_add_f32 v[108:109], v[108:109], v[0:1]\n\t"
      "v_lshlrev_b32 v0, 16, v216\n\t"
      "v_and_b32 v1, 0xffff0000, v216\n\t"
      "v_pk_add_f32 v[108:109], v[108:109], v[0:1]\n\t"
      "v_lshlrev_b32 v0, 16, v161\n\t"
      "v_and_b32 v1, 0xffff0000, v161\n\t"
      "v_pk_add_f32 v[110:111], v[110:111], v[0:1]\n\t"
      "v_lshlrev_b32 v0, 16, v217\n\t"
      "v_and_b32 v1, 0xffff0000, v217\n\t"
      "v_pk_add_f32 v[110:111], v[110:111], v[0:1]\n\t"
      "s_mov_b32 exec_lo, s53\n\t"
      "s_mov_b32 exec_hi, 0\n\t"
      "buffer_load_dword v239, v246, s[44:47], s52 offen sc1\n\t"
      "s_mov_b64 exec, -1\n\t"
      "s_waitcnt vmcnt(0)\n\t"
      "v_cmp_ne_u32 vcc, v239, v238\n\t"
      "s_nop 1\n\t"
      "s_cmp_lg_u64 vcc, 0\n\t"
      "s_cbranch_scc1 Lsp4_%=\n\t"
      "Lsg4_%=:\n\t"
      "v_lshrrev_b32 v74, 1, v240\n\t"
      "s_bfe_u32 s53, s3, 0x20002\n\t"
      "s_add_u32 s53, s53, 1\n\t"
      "s_and_b32 s53, s53, 3\n\t"
      "s_mul_i32 s48, s53, 10240\n\t"
      "s_add_u32 s48, s48, s34\n\t"
      "s_add_u32 s49, s48, 4096\n\t"
      "buffer_load_dwordx4 v[0:3], v240, s[44:47], s48 offen offset:0 sc1\n\t"
      "buffer_load_dwordx4 v[4:7], v240, s[44:47], s48 offen offset:1024 sc1\n\t"
      "buffer_load_dwordx4 v[8:11], v240, s[44:47], s48 offen offset:2048 sc1\n\t"
      "buffer_load_dwordx4 v[12:15], v240, s[44:47], s48 offen offset:3072 sc1\n\t"
      "buffer_load_dwordx2 v[16:17], v74, s[44:47], s49 offen sc1\n\t"
      "s_add_u32 s53, s53, 1\n\t"
      "s_and_b32 s53, s53, 3\n\t"
      "s_mul_i32 s48, s53, 10240\n\t"
      "s_add_u32 s48, s48, s34\n\t"
      "s_add_u32 s49, s48, 4096\n\t"
      "buffer_load_dwordx4 v[20:23], v240, s[44:47], s48 offen offset:0 sc1\n\t"
      "buffer_load_dwordx4 v[24:27], v240, s[44:47], s48 offen offset:1024 sc1\n\t"
      "buffer_load_dwordx4 v[28:31], v240, s[44:47], s48 offen offset:2048 sc1\n\t"
      "buffer_load_dwordx4 v[32:35], v240, s[44:47], s48 offen offset:3072 sc1\n\t"
      "buffer_load_dwordx2 v[36:37], v74, s[44:47], s49 offen sc1\n\t"
      "s_add_u32 s53, s53, 1\n\t"
      "s_and_b32 s53, s53, 3\n\t"
      "s_mul_i32 s48, s53, 10240\n\t"
      "s_add_u32 s48, s48, s34\n\t"
      "s_add_u32 s49, s48, 4096\n\t"
      "buffer_load_dwordx4 v[40:43], v240, s[44:47], s48 offen offset:0 sc1\n\t"
      "buffer_load_dwordx4 v[44:47], v240, s[44:47], s48 offen offset:1024 sc1\n\t"
      "buffer_load_dwordx4 v[48:51], v240, s[44:47], s48 offen offset:2048 sc1\n\t"
      "buffer_load_dwordx4 v[52:55], v240, s[44:47], s48 offen offset:3072 sc1\n\t"
      "buffer_load_dwordx2 v[56:57], v74, s[44:47], s49 offen sc1\n\t"
      "s_mov_b32 s88, s82\n\t"
      "s_mov_b32 s89, s83\n\t"
      "s_waitcnt vmcnt(10)\n\t"
      "v_lshlrev_b32 v60, 16, v0\n\t"
      "v_and_b32 v61, 0xffff0000, v0\n\t"
      "v_pk_add_f32 v[76:77], v[76:77], v[60:61]\n\t"
      "v_lshlrev_b32 v62, 16, v1\n\t"
      "v_and_b32 v63, 0xffff0000, v1\n\t"
      "v_pk_add_f32 v[78:79], v[78:79], v[62:63]\n\t"
      "v_lshlrev_b32 v64, 16, v2\n\t"
      "v_and_b32 v65, 0xffff0000, v2\n\t"
      "v_pk_add_f32 v[80:81], v[80:81], v[64:65]\n\t"
      "v_lshlrev_b32 v66, 16, v3\n\t"
      "v_and_b32 v67, 0xffff0000, v3\n\t"
      "v_pk_add_f32 v[82:83], v[82:83], v[66:67]\n\t"
      "v_lshlrev_b32 v60, 16, v4\n\t"
      "v_and_b32 v61, 0xffff0000, v4\n\t"
      "v_pk_add_f32 v[84:85], v[84:85], v[60:61]\n\t"
      "v_lshlrev_b32 v62, 16, v5\n\t"
      "v_and_b32 v63, 0xffff0000, v5\n\t"
      "v_pk_add_f32 v[86:87], v[86:87], v[62:63]\n\t"
      "v_lshlrev_b32 v64, 16, v6\n\t"
      "v_and_b32 v65, 0xffff0000, v6\n\t"
      "v_pk_add_f32 v[88:89], v[88:89], v[64:65]\n\t"
      "v_lshlrev_b32 v66, 16, v7\n\t"
      "v_and_b32 v67, 0xffff0000, v7\n\t"
      "v_pk_add_f32 v[90:91], v[90:91], v[66:67]\n\t"
      "v_lshlrev_b32 v60, 16, v8\n\t"
      "v_and_b32 v61, 0xffff0000, v8\n\t"
      "v_pk_add_f32 v[92:93], v[92:93], v[60:61]\n\t"
      "v_lshlrev_b32 v62, 16, v9\n\t"
      "v_and_b32 v63, 0xffff0000, v9\n\t"
      "v_pk_add_f32 v[94:95], v[94:95], v[62:63]\n\t"
      "v_lshlrev_b32 v64, 16, v10\n\t"
      "v_and_b32 v65, 0xffff0000, v10\n\t"
      "v_pk_add_f32 v[96:97], v[96:97], v[64:65]\n\t"
      "v_lshlrev_b32 v66, 16, v11\n\t"
      "v_and_b32 v67, 0xffff0000, v11\n\t"
      "v_pk_add_f32 v[98:99], v[98:99], v[66:67]\n\t"
      "v_lshlrev_b32 v60, 16, v12\n\t"
      "v_and_b32 v61, 0xffff0000, v12\n\t"
      "v_pk_add_f32 v[100:101], v[100:101], v[60:61]\n\t"
      "v_lshlrev_b32 v62, 16, v13\n\t"
      "v_and_b32 v63, 0xffff0000, v13\n\t"
      "v_pk_add_f32 v[102:103], v[102:103], v[62:63]\n\t"
      "v_lshlrev_b32 v64, 16, v14\n\t"
      "v_and_b32 v65, 0xffff0000, v14\n\t"
      "v_pk_add_f32 v[104:105], v[104:105], v[64:65]\n\t"
      "v_lshlrev_b32 v66, 16, v15\n\t"
      "v_and_b32 v67, 0xffff0000, v15\n\t"
      "v_pk_add_f32 v[106:107], v[106:107], v[66:67]\n\t"
      "v_lshlrev_b32 v60, 16, v16\n\t"
      "v_and_b32 v61, 0xffff0000, v16\n\t"
      "v_pk_add_f32 v[108:109], v[108:109], v[60:61]\n\t"
      "v_lshlrev_b32 v62, 16, v17\n\t"
      "v_and_b32 v63, 0xffff0000, v17\n\t"
      "v_pk_add_f32 v[110:111], v[110:111], v[62:63]\n\t"
      "s_waitcnt vmcnt(5)\n\t"
      "v_lshlrev_b32 v60, 16, v20\n\t"
      "v_and_b32 v61, 0xffff0000, v20\n\t"
      "v_pk_add_f32 v[76:77], v[76:77], v[60:61]\n\t"
      "v_lshlrev_b32 v62, 16, v21\n\t"
      "v_and_b32 v63, 0xffff0000, v21\n\t"
      "v_pk_add_f32 v[78:79], v[78:79], v[62:63]\n\t"
      "v_lshlrev_b32 v64, 16, v22\n\t"
      "v_and_b32 v65, 0xffff0000, v22\n\t"
      "v_pk_add_f32 v[80:81], v[80:81], v[64:65]\n\t"
      "v_lshlrev_b32 v66, 16, v23\n\t"
      "v_and_b32 v67, 0xffff0000, v23\n\t"
      "v_pk_add_f32 v[82:83], v[82:83], v[66:67]\n\t"
      "v_lshlrev_b32 v60, 16, v24\n\t"
      "v_and_b32 v61, 0xffff0000, v24\n\t"
      "v_pk_add_f32 v[84:85], v[84:85], v[60:61]\n\t"
      "v_lshlrev_b32 v62, 16, v25\n\t"
      "v_and_b32 v63, 0xffff0000, v25\n\t"
      "v_pk_add_f32 v[86:87], v[86:87], v[62:63]\n\t"
      "v_lshlrev_b32 v64, 16, v26\n\t"
      "v_and_b32 v65, 0xffff0000, v26\n\t"
      "v_pk_add_f32 v[88:89], v[88:89], v[64:65]\n\t"
      "v_lshlrev_b32 v66, 16, v27\n\t"
      "v_and_b32 v67, 0xffff0000, v27\n\t"
      "v_pk_add_f32 v[90:91], v[90:91], v[66:67]\n\t"
      "v_lshlrev_b32 v60, 16, v28\n\t"
      "v_and_b32 v61, 0xffff0000, v28\n\t"
      "v_pk_add_f32 v[92:93], v[92:93], v[60:61]\n\t"
      "v_lshlrev_b32 v62, 16, v29\n\t"
      "v_and_b32 v63, 0xffff0000, v29\n\t"
      "v_pk_add_f32 v[94:95], v[94:95], v[62:63]\n\t"
      "v_lshlrev_b32 v64, 16, v30\n\t"
      "v_and_b32 v65, 0xffff0000, v30\n\t"
      "v_pk_add_f32 v[96:97], v[96:97], v[64:65]\n\t"
      "v_lshlrev_b32 v66, 16, v31\n\t"
      "v_and_b32 v67, 0xffff0000, v31\n\t"
      "v_pk_add_f32 v[98:99], v[98:99], v[66:67]\n\t"
      "v_lshlrev_b32 v60, 16, v32\n\t"
      "v_and_b32 v61, 0xffff0000, v32\n\t"
      "v_pk_add_f32 v[100:101], v[100:101], v[60:61]\n\t"
      "v_lshlrev_b32 v62, 16, v33\n\t"
      "v_and_b32 v63, 0xffff0000, v33\n\t"
      "v_pk_add_f32 v[102:103], v[102:103], v[62:63]\n\t"
      "v_lshlrev_b32 v64, 16, v34\n\t"
      "v_and_b32 v65, 0xffff0000, v34\n\t"
      "v_pk_add_f32 v[104:105], v[104:105], v[64:65]\n\t"
      "v_lshlrev_b32 v66, 16, v35\n\t"
      "v_and_b32 v67, 0xffff0000, v35\n\t"
      "v_pk_add_f32 v[106:107], v[106:107], v[66:67]\n\t"
      "v_lshlrev_b32 v60, 16, v36\n\t"
      "v_and_b32 v61, 0xffff0000, v36\n\t"
      "v_pk_add_f32 v[108:109], v[108:109], v[60:61]\n\t"
      "v_lshlrev_b32 v62, 16, v37\n\t"
      "v_and_b32 v63, 0xffff0000, v37\n\t"
      "v_pk_add_f32 v[110:111], v[110:111], v[62:63]\n\t"
      "s_waitcnt vmcnt(0)\n\t"
      "v_lshlrev_b32 v60, 16, v40\n\t"
      "v_and_b32 v61, 0xffff0000, v40\n\t"
      "v_pk_add_f32 v[76:77], v[76:77], v[60:61]\n\t"
      "v_lshlrev_b32 v62, 16, v41\n\t"
      "v_and_b32 v63, 0xffff0000, v41\n\t"
      "v_pk_add_f32 v[78:79], v[78:79], v[62:63]\n\t"
      "v_cvt_pk_bf16_f32 v0, v76, v77\n\t"
      "v_cvt_pk_bf16_f32 v1, v78, v79\n\t"
      "buffer_store_dwordx2 v[0:1], v241, s[88:91], s60 offen offset:0 \n\t"
      "v_lshlrev_b32 v64, 16, v42\n\t"
      "v_and_b32 v65, 0xffff0000, v42\n\t"
      "v_pk_add_f32 v[80:81], v[80:81], v[64:65]\n\t"
      "v_lshlrev_b32 v66, 16, v43\n\t"
      "v_and_b32 v67, 0xffff0000, v43\n\t"
      "v_pk_add_f32 v[82:83], v[82:83], v[66:67]\n\t"
      "v_cvt_pk_bf16_f32 v2, v80, v81\n\t"
      "v_cvt_pk_bf16_f32 v3, v82, v83\n\t"
      "buffer_store_dwordx2 v[2:3], v241, s[88:91], s60 offen offset:32 \n\t"
      "v_lshlrev_b32 v60, 16, v44\n\t"
      "v_and_b32 v61, 0xffff0000, v44\n\t"
      "v_pk_add_f32 v[84:85], v[84:85], v[60:61]\n\t"
      "v_lshlrev_b32 v62, 16, v45\n\t"
      "v_and_b32 v63, 0xffff0000, v45\n\t"
      "v_pk_add_f32 v[86:87], v[86:87], v[62:63]\n\t"
      "v_cvt_pk_bf16_f32 v4, v84, v85\n\t"
      "v_cvt_pk_bf16_f32 v5, v86, v87\n\t"
      "buffer_store_dwordx2 v[4:5], v241, s[88:91], s60 offen offset:64 \n\t"
      "v_lshlrev_b32 v64, 16, v46\n\t"
      "v_and_b32 v65, 0xffff0000, v46\n\t"
      "v_pk_add_f32 v[88:89], v[88:89], v[64:65]\n\t"
      "v_lshlrev_b32 v66, 16, v47\n\t"
      "v_and_b32 v67, 0xffff0000, v47\n\t"
      "v_pk_add_f32 v[90:91], v[90:91], v[66:67]\n\t"
      "v_cvt_pk_bf16_f32 v6, v88, v89\n\t"
      "v_cvt_pk_bf16_f32 v7, v90, v91\n\t"
      "buffer_store_dwordx2 v[6:7], v241, s[88:91], s60 offen offset:96 \n\t"
      "v_lshlrev_b32 v60, 16, v48\n\t"
      "v_and_b32 v61, 0xffff0000, v48\n\t"
      "v_pk_add_f32 v[92:93], v[92:93], v[60:61]\n\t"
      "v_lshlrev_b32 v62, 16, v49\n\t"
      "v_and_b32 v63, 0xffff0000, v49\n\t"
      "v_pk_add_f32 v[94:95], v[94:95], v[62:63]\n\t"
      "v_cvt_pk_bf16_f32 v8, v92, v93\n\t"
      "v_cvt_pk_bf16_f32 v9, v94, v95\n\t"
      "buffer_store_dwordx2 v[8:9], v241, s[88:91], s60 offen offset:128 \n\t"
      "v_lshlrev_b32 v64, 16, v50\n\t"
      "v_and_b32 v65, 0xffff0000, v50\n\t"
      "v_pk_add_f32 v[96:97], v[96:97], v[64:65]\n\t"
      "v_lshlrev_b32 v66, 16, v51\n\t"
      "v_and_b32 v67, 0xffff0000, v51\n\t"
      "v_pk_add_f32 v[98:99], v[98:99], v[66:67]\n\t"
      "v_cvt_pk_bf16_f32 v10, v96, v97\n\t"
      "v_cvt_pk_bf16_f32 v11, v98, v99\n\t"
      "buffer_store_dwordx2 v[10:11], v241, s[88:91], s60 offen offset:160 \n\t"
      "v_lshlrev_b32 v60, 16, v52\n\t"
      "v_and_b32 v61, 0xffff0000, v52\n\t"
      "v_pk_add_f32 v[100:101], v[100:101], v[60:61]\n\t"
      "v_lshlrev_b32 v62, 16, v53\n\t"
      "v_and_b32 v63, 0xffff0000, v53\n\t"
      "v_pk_add_f32 v[102:103], v[102:103], v[62:63]\n\t"
      "v_cvt_pk_bf16_f32 v12, v100, v101\n\t"
      "v_cvt_pk_bf16_f32 v13, v102, v103\n\t"
      "buffer_store_dwordx2 v[12:13], v241, s[88:91], s60 offen offset:192 \n\t"
      "v_lshlrev_b32 v64, 16, v54\n\t"
      "v_and_b32 v65, 0xffff0000, v54\n\t"
      "v_pk_add_f32 v[104:105], v[104:105], v[64:65]\n\t"
      "v_lshlrev_b32 v66, 16, v55\n\t"
      "v_and_b32 v67, 0xffff0000, v55\n\t"
      "v_pk_add_f32 v[106:107], v[106:107], v[66:67]\n\t"
      "v_cvt_pk_bf16_f32 v14, v104, v105\n\t"
      "v_cvt_pk_bf16_f32 v15, v106, v107\n\t"
      "buffer_store_dwordx2 v[14:15], v241, s[88:91], s60 offen offset:224 \n\t"
      "v_lshlrev_b32 v60, 16, v56\n\t"
      "v_and_b32 v61, 0xffff0000, v56\n\t"
      "v_pk_add_f32 v[108:109], v[108:109], v[60:61]\n\t"
      "v_lshlrev_b32 v62, 16, v57\n\t"
      "v_and_b32 v63, 0xffff0000, v57\n\t"
      "v_pk_add_f32 v[110:111], v[110:111], v[62:63]\n\t"
      "v_cvt_pk_bf16_f32 v16, v108, v109\n\t"
      "v_cvt_pk_bf16_f32 v17, v110, v111\n\t"
      "buffer_store_dwordx2 v[16:17], v241, s[88:91], s60 offen offset:256 \n\t"
      "s_branch Lfin%=\n\t"
      "Lsp1_%=:\n\t"
      "s_cmp_eq_u32 s61, 0\n\t"
      "s_cbranch_scc1 Lsg1_%=\n\t"
      "s_sub_u32 s61, s61, 1\n\t"
      "s_sleep 2\n\t"
      "s_mov_b32 exec_lo, s57\n\t"
      "s_mov_b32 exec_hi, s26\n\t"
      "buffer_load_dword v239, v246, s[44:47], s25 offen sc1\n\t"
      "s_mov_b64 exec, -1\n\t"
      "s_waitcnt vmcnt(0)\n\t"
      "v_cmp_ne_u32 vcc, v239, v238\n\t"
      "s_nop 1\n\t"
      "s_cmp_lg_u64 vcc, 0\n\t"
      "s_cbranch_scc1 Lsp1_%=\n\t"
      "s_branch Lsg1_%=\n\t"
      "Lfix%=:\n\t"
      "s_nop 7\n\t"
      "v_mov_b32 v16, 0xff800000\n\t"
      "v_mov_b32 v17, 0xff800000\n\t"
      "v_mov_b32 v18, 0xff800000\n\t"
      "v_mov_b32 v19, 0xff800000\n\t"
      "v_mov_b32 v20, 0xff800000\n\t"
      "v_mov_b32 v21, 0xff800000\n\t"
      "v_mov_b32 v22, 0xff800000\n\t"
      "v_mov_b32 v23, 0xff800000\n\t"
      "v_mov_b32 v24, 0xff800000\n\t"
      "v_mov_b32 v25, 0xff800000\n\t"
      "v_mov_b32 v26, 0xff800000\n\t"
      "v_mov_b32 v27, 0xff800000\n\t"
      "v_mov_b32 v28, 0xff800000\n\t"
      "v_mov_b32 v29, 0xff800000\n\t"
      "v_mov_b32 v30, 0xff800000\n\t"
      "v_mov_b32 v31, 0xff800000\n\t"
      "s_cmp_eq_u32 s48, 1\n\t"
      "s_cbranch_scc1 Lfixd%=\n\t"
      "v_mov_b32 v32, 0xff800000\n\t"
      "v_mov_b32 v33, 0xff800000\n\t"
      "v_mov_b32 v34, 0xff800000\n\t"
      "v_mov_b32 v35, 0xff800000\n\t"
      "v_mov_b32 v36, 0xff800000\n\t"
      "v_mov_b32 v37, 0xff800000\n\t"
      "v_mov_b32 v38, 0xff800000\n\t"
      "v_mov_b32 v39, 0xff800000\n\t"
      "v_mov_b32 v40, 0xff800000\n\t"
      "v_mov_b32 v41, 0xff800000\n\t"
      "v_mov_b32 v42, 0xff800000\n\t"
      "v_mov_b32 v43, 0xff800000\n\t"
      "v_mov_b32 v44, 0xff800000\n\t"
      "v_mov_b32 v45, 0xff800000\n\t"
      "v_mov_b32 v46, 0xff800000\n\t"
      "v_mov_b32 v47, 0xff800000\n\t"
      "s_cmp_eq_u32 s48, 2\n\t"
      "s_cbranch_scc1 Lfixd%=\n\t"
      "v_mov_b32 v48, 0xff800000\n\t"
      "v_mov_b32 v49, 0xff800000\n\t"
      "v_mov_b32 v50, 0xff800000\n\t"
      "v_mov_b32 v51, 0xff800000\n\t"
      "v_mov_b32 v52, 0xff800000\n\t"
      "v_mov_b32 v53, 0xff800000\n\t"
      "v_mov_b32 v54, 0xff800000\n\t"
      "v_mov_b32 v55, 0xff800000\n\t"
      "v_mov_b32 v56, 0xff800000\n\t"
      "v_mov_b32 v57, 0xff800000\n\t"
      "v_mov_b32 v58, 0xff800000\n\t"
      "v_mov_b32 v59, 0xff800000\n\t"
      "v_mov_b32 v60, 0xff800000\n\t"
      "v_mov_b32 v61, 0xff800000\n\t"
      "v_mov_b32 v62, 0xff800000\n\t"
      "v_mov_b32 v63, 0xff800000\n\t"
      "s_cmp_eq_u32 s48, 3\n\t"
      "s_cbranch_scc1 Lfixd%=\n\t"
      "v_mov_b32 v64, 0xff800000\n\t"
      "v_mov_b32 v65, 0xff800000\n\t"
      "v_mov_b32 v66, 0xff800000\n\t"
      "v_mov_b32 v67, 0xff800000\n\t"
      "v_mov_b32 v68, 0xff800000\n\t"
      "v_mov_b32 v69, 0xff800000\n\t"
      "v_mov_b32 v70, 0xff800000\n\t"
      "v_mov_b32 v71, 0xff800000\n\t"
      "v_mov_b32 v72, 0xff800000\n\t"
      "v_mov_b32 v73, 0xff800000\n\t"
      "v_mov_b32 v74, 0xff800000\n\t"
      "v_mov_b32 v75, 0xff800000\n\t"
      "v_mov_b32 v76, 0xff800000\n\t"
      "v_mov_b32 v77, 0xff800000\n\t"
      "v_mov_b32 v78, 0xff800000\n\t"
      "v_mov_b32 v79, 0xff800000\n\t"
      "s_cmp_eq_u32 s48, 4\n\t"
      "s_cbranch_scc1 Lfixd%=\n\t"
      "v_mov_b32 v80, 0xff800000\n\t"
      "v_mov_b32 v81, 0xff800000\n\t"
      "v_mov_b32 v82, 0xff800000\n\t"
      "v_mov_b32 v83, 0xff800000\n\t"
      "v_mov_b32 v84, 0xff800000\n\t"
      "v_mov_b32 v85, 0xff800000\n\t"
      "v_mov_b32 v86, 0xff800000\n\t"
      "v_mov_b32 v87, 0xff800000\n\t"
      "v_mov_b32 v88, 0xff800000\n\t"
      "v_mov_b32 v89, 0xff800000\n\t"
      "v_mov_b32 v90, 0xff800000\n\t"
      "v_mov_b32 v91, 0xff800000\n\t"
      "v_mov_b32 v92, 0xff800000\n\t"
      "v_mov_b32 v93, 0xff800000\n\t"
      "v_mov_b32 v94, 0xff800000\n\t"
      "v_mov_b32 v95, 0xff800000\n\t"
      "s_cmp_eq_u32 s48, 5\n\t"
      "s_cbranch_scc1 Lfixd%=\n\t"
      "v_mov_b32 v96, 0xff800000\n\t"
      "v_mov_b32 v97, 0xff800000\n\t"
      "v_mov_b32 v98, 0xff800000\n\t"
      "v_mov_b32 v99, 0xff800000\n\t"
      "v_mov_b32 v100, 0xff800000\n\t"
      "v_mov_b32 v101, 0xff800000\n\t"
      "v_mov_b32 v102, 0xff800000\n\t"
      "v_mov_b32 v103, 0xff800000\n\t"
      "v_mov_b32 v104, 0xff800000\n\t"
      "v_mov_b32 v105, 0xff800000\n\t"
      "v_mov_b32 v106, 0xff800000\n\t"
      "v_mov_b32 v107, 0xff800000\n\t"
      "v_mov_b32 v108, 0xff800000\n\t"
      "v_mov_b32 v109, 0xff800000\n\t"
      "v_mov_b32 v110, 0xff800000\n\t"
      "v_mov_b32 v111, 0xff800000\n\t"
      "s_cmp_eq_u32 s48, 6\n\t"
      "s_cbranch_scc1 Lfixd%=\n\t"
      "v_mov_b32 v112, 0xff800000\n\t"
      "v_mov_b32 v113, 0xff800000\n\t"
      "v_mov_b32 v114, 0xff800000\n\t"
      "v_mov_b32 v115, 0xff800000\n\t"
      "v_mov_b32 v116, 0xff800000\n\t"
      "v_mov_b32 v117, 0xff800000\n\t"
      "v_mov_b32 v118, 0xff800000\n\t"
      "v_mov_b32 v119, 0xff800000\n\t"
      "v_mov_b32 v120, 0xff800000\n\t"
      "v_mov_b32 v121, 0xff800000\n\t"
      "v_mov_b32 v122, 0xff800000\n\t"
      "v_mov_b32 v123, 0xff800000\n\t"
      "v_mov_b32 v124, 0xff800000\n\t"
      "v_mov_b32 v125, 0xff800000\n\t"
      "v_mov_b32 v126, 0xff800000\n\t"
      "v_mov_b32 v127, 0xff800000\n\t"
      "s_cmp_eq_u32 s48, 7\n\t"
      "s_cbranch_scc1 Lfixd%=\n\t"
      "v_mov_b32 v128, 0xff800000\n\t"
      "v_mov_b32 v129, 0xff800000\n\t"
      "v_mov_b32 v130, 0xff800000\n\t"
      "v_mov_b32 v131, 0xff800000\n\t"
      "v_mov_b32 v132, 0xff800000\n\t"
      "v_mov_b32 v133, 0xff800000\n\t"
      "v_mov_b32 v134, 0xff800000\n\t"
      "v_mov_b32 v135, 0xff800000\n\t"
      "v_mov_b32 v136, 0xff800000\n\t"
      "v_mov_b32 v137, 0xff800000\n\t"
      "v_mov_b32 v138, 0xff800000\n\t"
      "v_mov_b32 v139, 0xff800000\n\t"
      "v_mov_b32 v140, 0xff800000\n\t"
      "v_mov_b32 v141, 0xff800000\n\t"
      "v_mov_b32 v142, 0xff800000\n\t"
      "v_mov_b32 v143, 0xff800000\n\t"
      "s_cmp_eq_u32 s48, 8\n\t"
      "s_cbranch_scc1 Lfixd%=\n\t"
      "s_branch Lfixd%=\n\t"
      "Lsp2_%=:\n\t"
      "s_cmp_eq_u32 s61, 0\n\t"
      "s_cbranch_scc1 Lsg2_%=\n\t"
      "s_sub_u32 s61, s61, 1\n\t"
      "s_sleep 4\n\t"
      "s_mov_b32 exec_lo, 0xf\n\t"
      "s_mov_b32 exec_hi, 0\n\t"
      "buffer_load_dword v239, v246, s[44:47], s60 offen sc1\n\t"
      "s_mov_b64 exec, -1\n\t"
      "s_waitcnt vmcnt(0)\n\t"
      "v_cmp_ne_u32 vcc, v239, v238\n\t"
      "s_nop 1\n\t"
      "s_cmp_lg_u64 vcc, 0\n\t"
      "s_cbranch_scc1 Lsp2_%=\n\t"
      "s_branch Lsg2_%=\n\t"
      "Lsp3_%=:\n\t"
      "s_cmp_eq_u32 s61, 0\n\t"
      "s_cbranch_scc1 Lsg3_%=\n\t"
      "s_sub_u32 s61, s61, 1\n\t"
      "s_sleep 4\n\t"
      "s_mov_b32 exec_lo, 0xff\n\t"
      "s_mov_b32 exec_hi, 0\n\t"
      "buffer_load_dword v239, v246, s[44:47], s60 offen sc1\n\t"
      "s_mov_b64 exec, -1\n\t"
      "s_waitcnt vmcnt(0)\n\t"
      "v_cmp_ne_u32 vcc, v239, v238\n\t"
      "s_nop 1\n\t"
      "s_cmp_lg_u64 vcc, 0\n\t"
      "s_cbranch_scc1 Lsp3_%=\n\t"
      "s_branch Lsg3_%=\n\t"
      "Lxl%=:\n\t"
      "s_mov_b32 s4, s68\n\t"
      "s_mov_b32 s5, s69\n\t"
      "s_mov_b32 s6, 0x240000\n\t"
      "s_mov_b32 s7, 0x20000\n\t"
      "s_mov_b32 s8, s80\n\t"
      "s_mov_b32 s9, s81\n\t"
      "s_mov_b32 s10, 0x480\n\t"
      "s_mov_b32 s11, 0x20000\n\t"
      "s_and_b32 s12, s33, 0x7ffffff\n\t"
      "buffer_load_dwordx2 v[144:145], v241, s[4:7], s12 offen offset:0\n\t"
      "buffer_load_dwordx2 v[200:201], v242, s[8:11], 0 offen offset:0\n\t"
      "buffer_load_dwordx2 v[146:147], v241, s[4:7], s12 offen offset:32\n\t"
      "buffer_load_dwordx2 v[202:203], v242, s[8:11], 0 offen offset:32\n\t"
      "buffer_load_dwordx2 v[148:149], v241, s[4:7], s12 offen offset:64\n\t"
      "buffer_load_dwordx2 v[204:205], v242, s[8:11], 0 offen offset:64\n\t"
      "buffer_load_dwordx2 v[150:151], v241, s[4:7], s12 offen offset:96\n\t"
      "buffer_load_dwordx2 v[206:207], v242, s[8:11], 0 offen offset:96\n\t"
      "buffer_load_dwordx2 v[152:153], v241, s[4:7], s12 offen offset:128\n\t"
      "buffer_load_dwordx2 v[208:209], v242, s[8:11], 0 offen offset:128\n\t"
      "buffer_load_dwordx2 v[154:155], v241, s[4:7], s12 offen offset:160\n\t"
      "buffer_load_dwordx2 v[210:211], v242, s[8:11], 0 offen offset:160\n\t"
      "buffer_load_dwordx2 v[156:157], v241, s[4:7], s12 offen offset:192\n\t"
      "buffer_load_dwordx2 v[212:213], v242, s[8:11], 0 offen offset:192\n\t"
      "buffer_load_dwordx2 v[158:159], v241, s[4:7], s12 offen offset:224\n\t"
      "buffer_load_dwordx2 v[214:215], v242, s[8:11], 0 offen offset:224\n\t"
      "buffer_load_dwordx2 v[160:161], v241, s[4:7], s12 offen offset:256\n\t"
      "buffer_load_dwordx2 v[216:217], v242, s[8:11], 0 offen offset:256\n\t"
      "s_branch Lxd%=\n\t"
      "Lsp4_%=:\n\t"
      "s_cmp_eq_u32 s61, 0\n\t"
      "s_cbranch_scc1 Lsg4_%=\n\t"
      "s_sub_u32 s61, s61, 1\n\t"
      "s_sleep 1\n\t"
      "s_mov_b32 exec_lo, s53\n\t"
      "s_mov_b32 exec_hi, 0\n\t"
      "buffer_load_dword v239, v246, s[44:47], s52 offen sc1\n\t"
      "s_mov_b64 exec, -1\n\t"
      "s_waitcnt vmcnt(0)\n\t"
      "v_cmp_ne_u32 vcc, v239, v238\n\t"
      "s_nop 1\n\t"
      "s_cmp_lg_u64 vcc, 0\n\t"
      "s_cbranch_scc1 Lsp4_%=\n\t"
      "s_branch Lsg4_%=\n\t"
      "Lfin%=:\n\t"
      "Lend%=:\n\t"
      : "+{v0}"(tid), "+{s2}"(bid), "+{s[0:1]}"(ka)
      :
      : "v1", "v2", "v3", "v4", "v5", "v6", "v7", "v8", "v9", "v10", "v11", "v12", "v13", "v14", "v15", "v16", "v17", "v18", "v19", "v20", "v21", "v22", "v23", "v24", "v25", "v26", "v27", "v28", "v29", "v30", "v31", "v32", "v33", "v34", "v35", "v36", "v37", "v38", "v39", "v40", "v41", "v42", "v43", "v44", "v45", "v46", "v47", "v48", "v49", "v50", "v51", "v52", "v53", "v54", "v55", "v56", "v57", "v58", "v59", "v60", "v61", "v62", "v63", "v64", "v65", "v66", "v67", "v68", "v69", "v70", "v71", "v72", "v73", "v74", "v75", "v76", "v77", "v78", "v79", "v80", "v81", "v82", "v83", "v84", "v85", "v86", "v87", "v88", "v89", "v90", "v91", "v92", "v93", "v94", "v95", "v96", "v97", "v98", "v99", "v100", "v101", "v102", "v103", "v104", "v105", "v106", "v107", "v108", "v109", "v110", "v111", "v112", "v113", "v114", "v115", "v116", "v117", "v118", "v119", "v120", "v121", "v122", "v123", "v124", "v125", "v126", "v127", "v128", "v129", "v130", "v131", "v132", "v133", "v134", "v135", "v136", "v137", "v138", "v139", "v140", "v141", "v142", "v143", "v144", "v145", "v146", "v147", "v148", "v149", "v150", "v151", "v152", "v153", "v154", "v155", "v156", "v157", "v158", "v159", "v160", "v161", "v162", "v163", "v164", "v165", "v166", "v167", "v168", "v169", "v170", "v171", "v172", "v173", "v174", "v175", "v176", "v177", "v178", "v179", "v180", "v181", "v182", "v183", "v184", "v185", "v186", "v187", "v188", "v189", "v190", "v191", "v192", "v193", "v194", "v195", "v196", "v197", "v198", "v199", "v200", "v201", "v202", "v203", "v204", "v205", "v206", "v207", "v208", "v209", "v210", "v211", "v212", "v213", "v214", "v215", "v216", "v217", "v218", "v219", "v220", "v221", "v222", "v223", "v224", "v225", "v226", "v227", "v228", "v229", "v230", "v231", "v232", "v233", "v234", "v235", "v236", "v237", "v238", "v239", "v240", "v241", "v242", "v243", "v244", "v245", "v246", "v247", "v248", "v249", "v250", "v251", "v252", "v253", "v254", "v255", "s3", "s4", "s5", "s6", "s7", "s8", "s9", "s10", "s11", "s12", "s13", "s14", "s15", "s16", "s17", "s18", "s19", "s20", "s21", "s22", "s23", "s24", "s25", "s26", "s27", "s28", "s29", "s30", "s31", "s33", "s34", "s35", "s36", "s37", "s38", "s39", "s40", "s41", "s42", "s43", "s44", "s45", "s46", "s47", "s48", "s49", "s50", "s51", "s52", "s53", "s54", "s55", "s56", "s57", "s58", "s59", "s60", "s61", "s62", "s63", "s64", "s65", "s66", "s67", "s68", "s69", "s70", "s71", "s72", "s73", "s74", "s75", "s76", "s77", "s78", "s79", "s80", "s81", "s82", "s83", "s84", "s85", "s86", "s87", "s88", "s89", "s90", "s91", "s92", "s93", "s94", "s95", "vcc", "scc", "memory");
}

extern "C" void kernel_launch(void* const* d_in, const int* in_sizes, int n_in, void* d_out, int out_size, void* d_ws, size_t ws_size,
                              hipStream_t stream) {
  const u16* x = (const u16*)d_in[0]; const u16* wqkv = (const u16*)d_in[1]; const u16* wo = (const u16*)d_in[2];
  const u16* w1 = (const u16*)d_in[4]; const u16* b1 = (const u16*)d_in[5]; const u16* w2 = (const u16*)d_in[6]; const u16* b2 = (const u16*)d_in[7];
  (void)hipFuncSetAttribute((const void*)fused_kernel, hipFuncAttributeMaxDynamicSharedMemorySize, K1_LDS);
  fused_kernel<<<256, 512, K1_LDS, stream>>>(x, wqkv, w1, b1, wo, w2, b2, (u16*)d_out, (char*)d_ws);
}
```
